```python
import torch

def _softmax(x, axis=-1):
    return torch.softmax(x, dim=axis)
def _gelu(x):
    return torch.nn.functional.gelu(x, approximate="tanh")
def _silu(x):
    return torch.nn.functional.silu(x)
def _sigmoid(x):
    return torch.sigmoid(x)
def _layernorm(x, axis=-1, eps=1e-5):
    m = x.mean(dim=axis, keepdim=True)
    v = x.var(dim=axis, keepdim=True, unbiased=False)
    return (x - m) / torch.sqrt(v + eps)
def _scatter_add(tgt, idx, src, axis=0):
    return tgt.clone().index_add_(axis, idx, src)
def _segment_sum(src, seg, n):
    out = torch.zeros((n,) + src.shape[1:], dtype=src.dtype)
    return out.index_add_(0, seg, src)
def _pad(x, pw):
    flat = tuple(v for pair in reversed(pw) for v in pair)
    return torch.nn.functional.pad(x, flat)

kernel_name = 'interleave_layers__linear_attn_block__glu_gated_swiglu__prompt'

def setup_inputs():

    _w = (lambda a: (lambda t: t.to(torch.bfloat16) if t.is_floating_point() else t)(torch.as_tensor(a)))
    def _A_setup():
        B, S, D = 1, 2048, 448
        return {
            "x": _w((lambda *s: torch.randn(*s, dtype=torch.bfloat16) * 0.05)(B, S, D)),
            "wqkv": _w((lambda *s: torch.randn(*s, dtype=torch.bfloat16) * 0.05)(D, 3 * D)),
            "wo": _w((lambda *s: torch.randn(*s, dtype=torch.bfloat16) * 0.05)(D, D)),
        }
    _A_in = _A_setup()

    def _B_setup():
        B, S, D, F = 1, 2048, 448, 1792
        return {
            "x": _w((lambda *s: torch.randn(*s, dtype=torch.bfloat16) * 0.05)(B, S, D)),
            "w1": _w((lambda *s: torch.randn(*s, dtype=torch.bfloat16) * 0.4)(D, F) * (2.0 / D) ** 0.5),
            "w3": _w((lambda *s: torch.randn(*s, dtype=torch.bfloat16) * 0.4)(D, F) * (2.0 / D) ** 0.5),
            "w2": _w((lambda *s: torch.randn(*s, dtype=torch.bfloat16) * 0.4)(F, D) * (2.0 / F) ** 0.5),
        }
    _B_in = _B_setup()

    return {
        "A_x": _A_in["x"],
        "A_wqkv": _A_in["wqkv"],
        "A_wo": _A_in["wo"],
        "B_x": _B_in["x"],
        "B_w1": _B_in["w1"],
        "B_w3": _B_in["w3"],
        "B_w2": _B_in["w2"],
    }

def reference(A_x, A_wqkv, A_wo, B_x, B_w1, B_w3, B_w2):
    def _A_ref(x, wqkv, wo):
        B, S, D = x.shape
        H = 16; Dh = D // H
        qkv = (lambda x, *d: x.permute(*d))((x @ wqkv).reshape(B, S, 3, H, Dh), 2, 0, 3, 1, 4)
        q, k, v = qkv[0], qkv[1], qkv[2]
        phi = lambda t: torch.where(t > 0, t + 1, torch.exp(t)) + 1e-6
        pq, pk = phi(q), phi(k)
        kv = torch.einsum("bhsd,bhse->bhde", pk, v)
        z = torch.einsum("bhsd->bhd", pk)
        num = torch.einsum("bhqd,bhde->bhqe", pq, kv)
        den = torch.einsum("bhqd,bhd->bhq", pq, z)[..., None] + 1e-6
        o = num / den
        return (lambda x, *d: x.permute(*d))(o, 0, 2, 1, 3).reshape(B, S, D) @ wo

    def _B_ref(x, w1, w3, w2):
        gate = torch.einsum("bsd,df->bsf", x, w1)
        up = torch.einsum("bsd,df->bsf", x, w3)
        h = _silu(gate) * up
        out = torch.einsum("bsf,fd->bsd", h, w2)
        return out

    x = A_x
    x = x + _A_ref(_layernorm(x, axis=-1), A_wqkv, A_wo)
    x = x + _B_ref(_layernorm(x, axis=-1), B_w1, B_w3, B_w2)
    x = x + _A_ref(_layernorm(x, axis=-1), A_wqkv, A_wo)
    x = x + _B_ref(_layernorm(x, axis=-1), B_w1, B_w3, B_w2)
    return x
```

```cpp
#include <hip/hip_runtime.h>
#include <hip/hip_bf16.h>
#include <stdint.h>

typedef unsigned short u16;
__device__ __forceinline__ float bf2f(u16 b) { return __uint_as_float(((unsigned)b) << 16); }
__device__ __forceinline__ u16 f2bf(float f) { __hip_bfloat16 h = __float2bfloat16(f); return *(u16*)&h; }
__device__ __forceinline__ float rbf(float f) { return bf2f(f2bf(f)); }

constexpr int S = 2048, D = 448, H = 16, DH = 28, F = 1792;

__global__ void k_ln(const u16* x, u16* y) {
  int r = blockIdx.x; int t = threadIdx.x;
  float s = 0.f;
  for (int i = t; i < D; i += 64) s += bf2f(x[r * D + i]);
  for (int m = 32; m >= 1; m >>= 1) s += __shfl_xor(s, m);
  float mean = s / D;
  float v = 0.f;
  for (int i = t; i < D; i += 64) { float d = bf2f(x[r * D + i]) - mean; v += d * d; }
  for (int m = 32; m >= 1; m >>= 1) v += __shfl_xor(v, m);
  float var = rbf(v / D);
  float mb = rbf(mean);
  float den = rbf(sqrtf(rbf(var + 1e-5f)));
  for (int i = t; i < D; i += 64) y[r * D + i] = f2bf(rbf(bf2f(x[r * D + i]) - mb) / den);
}
__global__ void k_gemm(const u16* A, const u16* B, u16* C, const u16* res, int M, int N, int K, int mode) {
  int n = blockIdx.x * 64 + threadIdx.x; int m = blockIdx.y * 4 + threadIdx.y;
  if (n >= N || m >= M) return;
  float acc = 0.f;
  for (int k = 0; k < K; ++k) acc += bf2f(A[m * K + k]) * bf2f(B[k * N + n]);
  float o = rbf(acc);
  if (mode == 1) o = bf2f(res[m * N + n]) + o;
  C[m * N + n] = f2bf(o);
}
__device__ __forceinline__ float phi(float t) {
  float r = t > 0.f ? rbf(t + 1.f) : rbf(expf(t));
  return rbf(r + 1e-6f);
}
__global__ void k_kv(const u16* qkv, float* kv, float* z) {
  int h = blockIdx.x; int d = blockIdx.y; int e = threadIdx.x;
  if (e > 28) return;
  float acc = 0.f;
  for (int s = 0; s < S; ++s) {
    float pk = phi(bf2f(qkv[s * 3 * D + D + h * DH + d]));
    float v = e < 28 ? bf2f(qkv[s * 3 * D + 2 * D + h * DH + e]) : 1.f;
    acc += pk * v;
  }
  if (e < 28) kv[(h * DH + d) * DH + e] = rbf(acc); else z[h * DH + d] = rbf(acc);
}
__global__ void k_attn(const u16* qkv, const float* kv, const float* z, u16* o) {
  int s = blockIdx.x; int c = threadIdx.x;
  int h = c / DH, e = c % DH;
  float num = 0.f, den = 0.f;
  for (int d = 0; d < DH; ++d) {
    float pq = phi(bf2f(qkv[s * 3 * D + h * DH + d]));
    num += pq * kv[(h * DH + d) * DH + e];
    den += pq * z[h * DH + d];
  }
  num = rbf(num); den = rbf(rbf(den) + 1e-6f);
  o[s * D + c] = f2bf(num / den);
}
__global__ void k_glu(const u16* g, const u16* u, u16* h, int n) {
  int i = blockIdx.x * 256 + threadIdx.x; if (i >= n) return;
  float gv = bf2f(g[i]);
  float sg = rbf(gv / (1.f + expf(-gv)));
  h[i] = f2bf(sg * bf2f(u[i]));
}

extern "C" void kernel_launch(void* const* d_in, const int* in_sizes, int n_in,
                              void* d_out, int out_size, void* d_ws, size_t ws_size,
                              hipStream_t stream) {
  const u16* x = (const u16*)d_in[0];
  const u16* wqkv = (const u16*)d_in[1];
  const u16* wo = (const u16*)d_in[2];
  const u16* w1 = (const u16*)d_in[4];
  const u16* w3 = (const u16*)d_in[5];
  const u16* w2 = (const u16*)d_in[6];
  char* ws = (char*)d_ws;
  u16* xn = (u16*)(ws);
  u16* qkv = (u16*)(ws + (4 << 20));
  u16* o = (u16*)(ws + (12 << 20));
  u16* x1 = (u16*)(ws + (16 << 20));
  u16* x2 = (u16*)(ws + (20 << 20));
  u16* g = (u16*)(ws + (24 << 20));
  u16* u = (u16*)(ws + (32 << 20));
  u16* hh = (u16*)(ws + (40 << 20));
  float* kv = (float*)(ws + (48 << 20));
  float* z = (float*)(ws + (49 << 20));
  const u16* cur = x;
  for (int l = 0; l < 2; ++l) {
    k_ln<<<S, 64, 0, stream>>>(cur, xn);
    k_gemm<<<dim3(3 * D / 64, S / 4), dim3(64, 4), 0, stream>>>(xn, wqkv, qkv, nullptr, S, 3 * D, D, 0);
    k_kv<<<dim3(H, DH), 64, 0, stream>>>(qkv, kv, z);
    k_attn<<<S, D, 0, stream>>>(qkv, kv, z, o);
    k_gemm<<<dim3(D / 64, S / 4), dim3(64, 4), 0, stream>>>(o, wo, x1, cur, S, D, D, 1);
    k_ln<<<S, 64, 0, stream>>>(x1, xn);
    k_gemm<<<dim3(F / 64, S / 4), dim3(64, 4), 0, stream>>>(xn, w1, g, nullptr, S, F, D, 0);
    k_gemm<<<dim3(F / 64, S / 4), dim3(64, 4), 0, stream>>>(xn, w3, u, nullptr, S, F, D, 0);
    k_glu<<<S * F / 256, 256, 0, stream>>>(g, u, hh, S * F);
    u16* dst = l == 0 ? x2 : (u16*)d_out;
    k_gemm<<<dim3(D / 64, S / 4), dim3(64, 4), 0, stream>>>(hh, w2, dst, x1, S, D, F, 1);
    cur = x2;
  }
}
```

```cpp
#include <hip/hip_runtime.h>
#include <hip/hip_bf16.h>
#include <stdint.h>

#define DI __device__ __forceinline__
typedef unsigned v4u __attribute__((ext_vector_type(4)));
typedef unsigned v2u __attribute__((ext_vector_type(2)));
typedef float f32x2 __attribute__((ext_vector_type(2)));
typedef __bf16 bf16x2 __attribute__((ext_vector_type(2)));
typedef unsigned short u16;
constexpr int D_ = 448, DH_ = 28, F_ = 1792;
constexpr int WQ_OFF = 0;
constexpr int WO_OFF = 1425408;
constexpr int WG_OFF = 1884160;
constexpr int W2_OFF = 5210112;
constexpr int ACT_OFF = 6815744;
constexpr int XSZ = 1835008;
constexpr int SSZ = 229376;
constexpr int A_XIN = 0;
constexpr int A_SIN = 1835008;
constexpr int A_X1 = 2064384;
constexpr int A_S1 = 3899392;
constexpr int A_PQ = 4128768;
constexpr int A_KV1 = 6225920;
constexpr int A_HH = 7274496;
constexpr int LSZ = 14614528;
constexpr int WS_END = 36044800;
constexpr int LDS_BYTES = 151040;
constexpr unsigned SENT = 0xFFFFFFFFu;
constexpr int SC1 = 16;

DI unsigned pack2(float lo, float hi) { f32x2 v = {lo, hi}; return __builtin_bit_cast(unsigned, __builtin_convertvector(v, bf16x2)); }
DI float rbf(float f) { return __uint_as_float(pack2(f, f) & 0xFFFF0000u); }
DI float dot2(unsigned a, unsigned b, float c) { return __builtin_amdgcn_fdot2_f32_bf16(__builtin_bit_cast(bf16x2, a), __builtin_bit_cast(bf16x2, b), c, false); }
DI float swap32_add(float s) { auto r = __builtin_amdgcn_permlane32_swap(__float_as_uint(s), __float_as_uint(s), false, false); return __uint_as_float(r[0]) + __uint_as_float(r[1]); }
DI unsigned san(unsigned x) { return x < 0xFFFFFFFEu ? x : 0xFFFFFFFEu; }

struct P0 { const u16* x; const u16* wqkv; const u16* wo; const u16* w1; const u16* w3; const u16* w2; unsigned char* ws; };
constexpr int J_X = 168, J_G = J_X + 448, J_2 = J_G + 392, J_O = J_2 + 196, J_P = J_O + 56, NWAVES = 1536;
__global__ void __launch_bounds__(256) k_prep(P0 p) {
  __shared__ __attribute__((aligned(16))) unsigned char smem[4 * 9216];
  const int lane = threadIdx.x & 63, w = __builtin_amdgcn_readfirstlane(threadIdx.x >> 6), hh = lane >> 5, r = lane & 31;
  const int job = blockIdx.x * 4 + w;
  asm volatile("" :: "s"(p.x), "s"(p.wqkv), "s"(p.wo), "s"(p.w1), "s"(p.w3), "s"(p.w2), "s"(p.ws));
  const __amdgpu_buffer_rsrc_t rs = __builtin_amdgcn_make_buffer_rsrc((void*)p.ws, 0, WS_END, 0x00020000);
  if (job >= J_P) {
    if (job >= NWAVES - 64) {
      const unsigned* code; int len;
      asm volatile("s_getpc_b64 s[40:41]\n\ts_add_u32 s40, s40, K2_begin@rel32@lo+4\n\ts_addc_u32 s41, s41, K2_begin@rel32@hi+12\n\ts_mov_b64 %0, s[40:41]\n\ts_mov_b32 %1, K2_end-K2_begin" : "=s"(code), "=s"(len) :: "scc", "s40", "s41");
      const int o = ((((job - (NWAVES - 64)) >> 5) * 4 + w) * 64 + lane) * 128;
      if (o < len) { unsigned t = code[o >> 2]; asm volatile("" :: "v"(t)); }
    }
    const v4u s = {SENT, SENT, SENT, SENT};
    for (int i = job - J_P; i < (A_HH - A_X1) / 1024; i += NWAVES - J_P) __builtin_amdgcn_raw_buffer_store_b128(s, rs, lane * 16, ACT_OFF + A_X1 + i * 1024, SC1);
    return;
  }
  unsigned char* T = smem + w * 9216;
  const unsigned tl = (unsigned)(size_t)T;
  if (job >= J_X && job < J_G) {
    const int tt = ((job - J_X) * 9363) >> 16, cb = job - J_X - 7 * tt;
    v4u v[4];
#pragma unroll
    for (int n = 0; n < 4; ++n) v[n] = *((const v4u*)(p.x + (32 * tt + 8 * n + (lane >> 3)) * D_ + 64 * cb + 8 * (lane & 7)));
#pragma unroll
    for (int n = 0; n < 4; ++n) *(v4u*)(T + (8 * n + (lane >> 3)) * 144 + 16 * (lane & 7)) = v[n];
#pragma unroll
    for (int f = 0; f < 2; ++f) {
      float s = 0.f, q = 0.f;
#pragma unroll
      for (int i = 0; i < 2; ++i) {
        v4u u = *(const v4u*)(T + r * 144 + 64 * f + 32 * i + 16 * hh);
        u.x = san(u.x); u.y = san(u.y); u.z = san(u.z); u.w = san(u.w);
        __builtin_amdgcn_raw_buffer_store_b128(u, rs, lane * 16, ACT_OFF + A_XIN + (tt * 28 + 4 * cb + 2 * f + i) * 1024, SC1);
#pragma unroll
        for (int j = 0; j < 4; ++j) { s = dot2(u[j], 0x3f803f80u, s); q = dot2(u[j], u[j], q); }
      }
      s = swap32_add(s); q = swap32_add(q);
      const v2u o = {san(__float_as_uint(s)), san(__float_as_uint(q))};
      __builtin_amdgcn_raw_buffer_store_b64(o, rs, hh ? 0x40000000 : lane * 8, ACT_OFF + A_SIN + tt * 3584 + (2 * cb + f) * 256, SC1);
    }
    return;
  }
  const bool k0 = job < J_X, k01 = job < J_2, k3 = job >= J_O;
  const int kind = k0 ? 0 : (k01 ? 1 : (k3 ? 3 : 2));
  const int jj = job - (k0 ? 0 : (k01 ? J_G : (k3 ? J_O : J_2)));
  const int kb = (jj * (k0 ? 2731 : (k01 ? 1171 : 9363))) >> 16, cb = jj - kb * (k0 ? 24 : (k01 ? 56 : 7));
  const int ld = k0 ? 3 * D_ : (k01 ? F_ : D_);
  const unsigned long long pa = k0 ? (unsigned long long)p.wqkv : (k01 ? (unsigned long long)p.w1 : (k3 ? (unsigned long long)p.wo : (unsigned long long)p.w2));
  const unsigned long long pb = kind == 1 ? (unsigned long long)p.w3 - 64 : pa;
  const int ch = min(lane & 7, k0 ? 6 : 7);
  typedef const __attribute__((address_space(1))) u16* gptr;
  gptr src = (gptr)(ch >= 4 ? pb : pa) + ((k0 ? 56 : (k01 ? 32 : 64)) * cb + ((k3 ? 56 : 64) * kb + (lane >> 3)) * ld + 8 * ch);
  v4u v[8];
#pragma unroll
  for (int n = 0; n < 8; ++n) v[n] = *((const __attribute__((address_space(1))) v4u*)(src + (k3 && n == 7 ? 6 : n) * 8 * ld));
  __builtin_amdgcn_sched_barrier(0);
  const int q = (lane >> 2) & 3, pc = lane & 3, up = (lane >> 4) & 1, pp = ((pc & 1) << 1) | (pc >> 1);
  const int colb = kind == 0 ? 32 * up + 8 * pc : (kind == 1 ? 64 * up + 8 * pp : 32 * up + 8 * pp);
  const int gstep = kind == 0 ? 56 : (kind == 1 ? 32 : 64);
  const unsigned a0 = tl + (q + (kind == 3 ? 4 : 8) * hh) * 144 + colb, tstep = (kind == 3 ? 8 : 4) * 144;
  const int sel = cb >> 3, wh = sel == 0 ? 2 : sel - 1;
  const int dst0 = kind == 0 ? WQ_OFF + ((2 * (cb & 7) * 3 + wh) * 29 + 4 * kb) * 1024 : (kind == 1 ? WG_OFF + (2 * cb * 29 + 4 * kb) * 1024 : (kind == 2 ? W2_OFF + (2 * cb * 112 + 4 * kb) * 1024 : WO_OFF + (2 * cb * 32 + 4 * kb) * 1024));
  const int dstep = (kind == 0 ? 87 : (kind == 1 ? 29 : (kind == 2 ? 112 : 32))) * 1024;
#pragma unroll
  for (int n = 0; n < 8; ++n) *(v4u*)(T + (8 * n + (lane >> 3)) * 144 + 16 * (lane & 7)) = v[n];
  for (int g = 0; g < 2; ++g) {
    v2u t[8];
    const unsigned a = a0 + g * gstep, a2 = a + (kind == 3 ? 28 - 32 : 0) * 144;
    asm volatile("ds_read_b64_tr_b16 %0, %8\n\tds_read_b64_tr_b16 %1, %9\n\tds_read_b64_tr_b16 %2, %8 offset:2304\n\tds_read_b64_tr_b16 %3, %9 offset:2304\n\t"
                 "ds_read_b64_tr_b16 %4, %10 offset:4608\n\tds_read_b64_tr_b16 %5, %11 offset:4608\n\tds_read_b64_tr_b16 %6, %10 offset:6912\n\tds_read_b64_tr_b16 %7, %11 offset:6912\n\ts_waitcnt lgkmcnt(0)"
                 : "=&v"(t[0]), "=&v"(t[1]), "=&v"(t[2]), "=&v"(t[3]), "=&v"(t[4]), "=&v"(t[5]), "=&v"(t[6]), "=&v"(t[7])
                 : "v"(a), "v"(a + tstep), "v"(a2), "v"(a2 + tstep) : "memory");
    float cs = 0.f;
#pragma unroll
    for (int n = 0; n < 4; ++n) {
      v4u o = {t[2 * n].x, t[2 * n].y, t[2 * n + 1].x, t[2 * n + 1].y};
      if (kind == 0 && r >= DH_) o = v4u{0u, 0u, 0u, 0u};
      if (kind == 3 && (n & 1) && hh) { o.z = 0u; o.w = 0u; }
#pragma unroll
      for (int j = 0; j < 4; ++j) cs = dot2(o[j], 0x3f803f80u, cs);
      __builtin_amdgcn_raw_buffer_store_b128(o, rs, lane * 16, dst0 + g * dstep + n * 1024, SC1);
    }
    if (kind <= 1) {
      cs = swap32_add(cs);
      const float c0 = rbf(cs);
      const unsigned val = hh == 0 ? pack2(c0, cs - c0) : ((kind == 0 && wh == 1 && r == DH_) ? 0x3f80u : 0u);
      const int vo = hh == 0 ? ((kb >> 2) * 32 + r) * 16 + (kb & 3) * 4 : (kb == 6 ? (32 + r) * 16 + 12 : 0x40000000);
      __builtin_amdgcn_raw_buffer_store_b32(val, rs, vo, dst0 + g * dstep + (28 - 4 * kb) * 1024, SC1);
    }
  }
}


__global__ void __launch_bounds__(256, 1) k_main(unsigned char* ws, u16* out) {
  const int b = blockIdx.x;
  const int w = __builtin_amdgcn_readfirstlane(threadIdx.x >> 6);
  const int xcd = b & 7, slot = b >> 3;
  const int qR = (xcd & 1) * 8 + (slot & 7), qH = (xcd >> 1) * 4 + (slot >> 3), pi = 4 * ((xcd & 1) ^ 1) + (xcd >> 1);
  const int sq = (slot * 37) >> 8;
  const int act = (slot + 4) >> 5 ^ 1, og = slot - 7 * sq, ota = 8 * pi + 2 * (sq & 3);
  const int gR = qR, gS = qH, slw = slot * 4 + w;
  asm volatile(
"K2_begin:\n"
"s_nop 4\n"
"s_mov_b64 s[40:41], %[ws]\n"
"s_and_b32 s41, s41, 0xffff\n"
"s_mov_b32 s42, 36044800\n"
"s_mov_b32 s43, 0x20000\n"
"v_mbcnt_lo_u32_b32 v0, -1, 0\n"
"v_mbcnt_hi_u32_b32 v0, -1, v0\n"
"s_mul_i32 s92, %[w], 29\n"
"s_lshr_b32 s92, s92, 1\n"
"s_lshl_b32 s92, s92, 10\n"
"s_mul_i32 s71, %[qH], 89088\n"
"s_add_u32 s71, s71, s92\n"
"v_lshlrev_b32 v4, 3, v0\n"
"v_mov_b32 v30, v0\n"
"v_lshlrev_b32 v0, 4, v0\n"
"s_getpc_b64 s[48:49]\n"
"K2_pc:\n"
"s_and_b32 s49, s49, 0xffff\n"
"s_mov_b32 s50, K2_end-K2_pc\n"
"s_mov_b32 s51, 0x20000\n"
"s_mul_i32 s60, %[slw], 384\n"
"v_lshlrev_b32 v19, 3, v0\n"
"v_cmp_gt_u32 vcc, 384, v19\n"
"v_mov_b32 v16, 0x40000000\n"
"s_nop 0\n"
"v_cndmask_b32 v19, v16, v19, vcc\n"
"buffer_load_dword v17, v19, s[48:51], s60 offen\n"
"s_add_u32 s44, s40, 6815744\n"
"s_addc_u32 s45, s41, 0\n"
"s_mov_b32 s46, 29229056\n"
"s_mov_b32 s47, 0x20000\n"
"s_mov_b64 s[52:53], %[out]\n"
"s_and_b32 s53, s53, 0xffff\n"
"s_mov_b32 s54, 1835008\n"
"s_add_u32 m0, s92, 0\n"
"s_add_u32 s95, s71, 0\n"
"buffer_load_dwordx4 v0, s[40:43], s95 offen lds\n"
"s_mov_b32 s55, 0x20000\n"
"s_mov_b32 s57, 65536\n"
"s_mov_b32 s58, 0x358637bd\n"
"s_mov_b32 s61, 0x3fb8aa3b\n"
"s_mov_b32 s62, -1\n"
"s_mov_b32 s63, 0\n"
"s_mul_i32 s64, %[qR], 114688\n"
"buffer_load_dwordx4 v0, s[40:43], s95 offen offset:1024 lds\n"
"s_mul_i32 s59, %[w], 28672\n"
"s_add_u32 s64, s64, s59\n"
"s_mul_i32 s65, %[qR], 14336\n"
"s_mul_i32 s59, %[w], 3584\n"
"s_add_u32 s65, s65, s59\n"
"s_add_u32 s65, s65, 1835008\n"
"s_lshr_b32 s85, %[w], 1\n"
"buffer_load_dwordx4 v0, s[40:43], s95 offen offset:2048 lds\n"
"s_and_b32 s86, %[w], 1\n"
"s_lshl_b32 s60, %[og], 1\n"
"s_add_u32 s85, s85, s60\n"
"s_add_u32 s86, s86, %[ota]\n"
"s_xor_b32 s90, s85, 1\n"
"s_xor_b32 s91, s86, 1\n"
"s_mul_i32 s66, s85, 32768\n"
"buffer_load_dwordx4 v0, s[40:43], s95 offen offset:3072 lds\n"
"s_mul_i32 s59, %[w], 8192\n"
"s_add_u32 s66, s66, s59\n"
"s_add_u32 s66, s66, 1425408\n"
"s_mul_i32 s67, s90, 32768\n"
"s_mul_i32 s59, %[w], 8192\n"
"s_add_u32 s67, s67, s59\n"
"s_add_u32 s67, s67, 1425408\n"
"s_add_u32 m0, s92, 4096\n"
"s_add_u32 s95, s71, 4096\n"
"buffer_load_dwordx4 v0, s[40:43], s95 offen lds\n"
"s_mul_i32 s69, s85, 114688\n"
"s_mul_i32 s59, %[w], 28672\n"
"s_add_u32 s69, s69, s59\n"
"s_add_u32 s69, s69, 5210112\n"
"s_mul_i32 s70, s90, 114688\n"
"s_mul_i32 s59, %[w], 28672\n"
"s_add_u32 s70, s70, s59\n"
"buffer_load_dwordx4 v0, s[40:43], s95 offen offset:1024 lds\n"
"s_add_u32 s70, s70, 5210112\n"
"s_mul_i32 s76, s86, 32768\n"
"s_mul_i32 s59, %[w], 8192\n"
"s_add_u32 s76, s76, s59\n"
"s_add_u32 s76, s76, 4128768\n"
"s_mul_i32 s77, s91, 32768\n"
"s_mul_i32 s59, %[w], 8192\n"
"buffer_load_dwordx4 v0, s[40:43], s95 offen offset:2048 lds\n"
"s_add_u32 s77, s77, s59\n"
"s_add_u32 s77, s77, 4128768\n"
"s_mul_i32 s83, s86, 114688\n"
"s_mul_i32 s59, %[w], 28672\n"
"s_add_u32 s83, s83, s59\n"
"s_add_u32 s83, s83, 7274496\n"
"s_mul_i32 s84, s91, 114688\n"
"buffer_load_dwordx4 v0, s[40:43], s95 offen offset:3072 lds\n"
"s_mul_i32 s59, %[w], 28672\n"
"s_add_u32 s84, s84, s59\n"
"s_add_u32 s84, s84, 7274496\n"
"s_mul_i32 s78, s86, 28672\n"
"s_mul_i32 s59, s85, 2048\n"
"s_add_u32 s78, s78, s59\n"
"s_mul_i32 s79, s86, 3584\n"
"s_add_u32 m0, s92, 8192\n"
"s_add_u32 s95, s71, 8192\n"
"buffer_load_dwordx4 v0, s[40:43], s95 offen lds\n"
"s_mul_i32 s59, s85, 256\n"
"s_add_u32 s79, s79, s59\n"
"s_mul_i32 s82, s86, 28672\n"
"s_mul_i32 s59, s85, 64\n"
"s_add_u32 s82, s82, s59\n"
"v_and_b32 v25, 31, v30\n"
"v_lshrrev_b32 v26, 5, v30\n"
"buffer_load_dwordx4 v0, s[40:43], s95 offen offset:1024 lds\n"
"v_mov_b32 v27, v30\n"
"v_mul_u32_u24 v30, 896, v25\n"
"v_lshl_add_u32 v30, v26, 4, v30\n"
"v_add_u32 v30, s82, v30\n"
"v_bfe_u32 v19, v27, 4, 1\n"
"v_mul_u32_u24 v19, 14304, v19\n"
"v_sub_u32 v30, v30, v19\n"
"buffer_load_dwordx4 v0, s[40:43], s95 offen offset:2048 lds\n"
"s_mul_i32 s87, %[w], 22\n"
"s_min_u32 s87, s87, 65\n"
"s_lshl_b32 s87, s87, 10\n"
"s_mul_i32 s89, %[w], 7168\n"
"s_sub_u32 s86, s71, s92\n"
"s_add_u32 s86, s86, s89\n"
"s_mul_i32 s93, %[ota], 1024\n"
"buffer_load_dwordx4 v0, s[40:43], s95 offen offset:3072 lds\n"
"s_add_u32 s93, s93, 6225920\n"
"s_mul_i32 s68, %[gS], 207872\n"
"s_add_u32 s68, s68, 1884160\n"
"s_add_u32 s88, s68, s89\n"
"s_add_u32 s68, s68, s87\n"
"s_mul_i32 s72, %[qH], 65536\n"
"s_mul_i32 s59, %[qR], 4096\n"
"s_add_u32 m0, s92, 12288\n"
"s_add_u32 s95, s71, 12288\n"
"buffer_load_dwordx4 v0, s[40:43], s95 offen lds\n"
"s_add_u32 s72, s72, s59\n"
"s_mul_i32 s59, %[w], 1024\n"
"s_add_u32 s72, s72, s59\n"
"s_add_u32 s72, s72, 6225920\n"
"s_mul_i32 s73, %[qH], 65536\n"
"s_mul_i32 s59, %[w], 1024\n"
"s_add_u32 s73, s73, s59\n"
"buffer_load_dwordx4 v0, s[40:43], s95 offen offset:1024 lds\n"
"s_add_u32 s73, s73, 6225920\n"
"s_lshr_b32 s60, %[w], 1\n"
"s_and_b32 s74, %[w], 1\n"
"s_mul_i32 s74, s74, 8\n"
"s_mul_i32 s59, s60, 1024\n"
"s_add_u32 s74, s74, s59\n"
"s_add_u32 s74, s74, 148992\n"
"buffer_load_dwordx4 v0, s[40:43], s95 offen offset:2048 lds\n"
"s_mul_i32 s75, %[qR], 131072\n"
"s_mul_i32 s59, %[w], 32768\n"
"s_add_u32 s75, s75, s59\n"
"s_mul_i32 s59, %[qH], 2048\n"
"s_add_u32 s75, s75, s59\n"
"s_add_u32 s75, s75, 4128768\n"
"s_mul_i32 s80, %[gR], 114688\n"
"buffer_load_dwordx4 v[32:35], v0, s[44:47], s64 offen sc1\n"
"s_mul_i32 s59, %[w], 28672\n"
"s_add_u32 s80, s80, s59\n"
"s_add_u32 s80, s80, 2064384\n"
"s_mul_i32 s81, %[gR], 14336\n"
"s_mul_i32 s59, %[w], 3584\n"
"s_add_u32 s81, s81, s59\n"
"s_add_u32 s81, s81, 3899392\n"
"buffer_load_dwordx4 v[36:39], v0, s[44:47], s64 offen offset:1024 sc1\n"
"s_mul_i32 s82, %[gR], 458752\n"
"s_mul_i32 s59, %[w], 114688\n"
"s_add_u32 s82, s82, s59\n"
"s_mul_i32 s59, %[gS], 7168\n"
"s_add_u32 s82, s82, s59\n"
"s_add_u32 s82, s82, 7274496\n"
"v_add_u32 v1, 65536, v0\n"
"buffer_load_dwordx4 v[40:43], v0, s[44:47], s64 offen offset:2048 sc1\n"
"v_add_u32 v2, 131072, v0\n"
"v_cmp_gt_u32 vcc, 7, v27\n"
"s_nop 1\n"
"v_cndmask_b32 v3, 0, v27, vcc\n"
"v_lshlrev_b32 v3, 12, v3\n"
"v_add_u32 v3, 4092, v3\n"
"s_mul_i32 s60, %[w], 12288\n"
"s_add_u32 s60, s60, 89088\n"
"buffer_load_dwordx4 v[44:47], v0, s[44:47], s64 offen offset:3072 sc1\n"
"v_add_u32 v5, s60, v0\n"
"s_xor_b32 s60, %[w], 1\n"
"s_mul_i32 s60, s60, 12288\n"
"s_add_u32 s60, s60, 89088\n"
"v_add_u32 v6, s60, v0\n"
"s_xor_b32 s60, %[w], 2\n"
"s_mul_i32 s60, s60, 12288\n"
"s_add_u32 s94, s64, 4096\n"
"buffer_load_dwordx4 v[48:51], v0, s[44:47], s94 offen sc1\n"
"s_add_u32 s60, s60, 93184\n"
"v_add_u32 v7, s60, v0\n"
"s_xor_b32 s60, %[w], 3\n"
"s_mul_i32 s60, s60, 12288\n"
"s_add_u32 s60, s60, 97280\n"
"v_add_u32 v24, s60, v0\n"
"s_mul_i32 s90, %[w], 4096\n"
"buffer_load_dwordx4 v[52:55], v0, s[44:47], s94 offen offset:1024 sc1\n"
"s_add_u32 s90, s90, 89088\n"
"s_mul_i32 s91, %[w], 1024\n"
"s_add_u32 s91, s91, 89088\n"
"s_mul_i32 s60, %[w], 128\n"
"s_add_u32 s60, s60, 148480\n"
"v_bfe_u32 v25, v0, 4, 5\n"
"v_lshrrev_b32 v26, 9, v0\n"
"buffer_load_dwordx4 v[56:59], v0, s[44:47], s94 offen offset:2048 sc1\n"
"v_lshl_add_u32 v25, v25, 2, s60\n"
"v_lshl_add_u32 v26, v26, 4, s60\n"
"v_and_b32 v28, 15, v27\n"
"v_lshlrev_b32 v28, 12, v28\n"
"v_add_u32 v28, 1020, v28\n"
"s_sub_u32 s60, s77, s76\n"
"v_bfe_u32 v29, v27, 3, 1\n"
"buffer_load_dwordx4 v[60:63], v0, s[44:47], s94 offen offset:3072 sc1\n"
"v_mul_lo_u32 v29, v29, 0\n"
"v_and_b32 v19, 3, v27\n"
"v_lshl_add_u32 v29, v19, 11, v29\n"
"v_add_u32 v29, s76, v29\n"
"v_add_u32 v29, 2044, v29\n"
"s_add_u32 s94, s64, 8192\n"
"buffer_load_dwordx4 v[64:67], v0, s[44:47], s94 offen sc1\n"
"buffer_load_dwordx4 v[68:71], v0, s[44:47], s94 offen offset:1024 sc1\n"
"buffer_load_dwordx4 v[72:75], v0, s[44:47], s94 offen offset:2048 sc1\n"
"buffer_load_dwordx4 v[76:79], v0, s[44:47], s94 offen offset:3072 sc1\n"
"buffer_load_dwordx2 v[224:225], v4, s[44:47], s65 offen sc1\n"
"buffer_load_dwordx2 v[226:227], v4, s[44:47], s65 offen offset:512 sc1\n"
"buffer_load_dwordx2 v[228:229], v4, s[44:47], s65 offen offset:1024 sc1\n"
"buffer_load_dwordx2 v[230:231], v4, s[44:47], s65 offen offset:1536 sc1\n"
"buffer_load_dwordx2 v[232:233], v4, s[44:47], s65 offen offset:2048 sc1\n"
"buffer_load_dwordx2 v[234:235], v4, s[44:47], s65 offen offset:2560 sc1\n"
"buffer_load_dwordx2 v[236:237], v4, s[44:47], s65 offen offset:3072 sc1\n"
"L_xp_0:\n"
"v_mov_b32 v19, 0\n"
"s_add_u32 s60, s78, 0\n"
"buffer_load_dwordx4 v[8:11], v0, s[44:47], s60 offen\n"
"buffer_load_dwordx4 v[12:15], v0, s[44:47], s60 offen offset:1024\n"
"s_waitcnt vmcnt(21)\n"
"s_barrier\n"
"ds_read_b128 v[192:195], v0\n"
"ds_read_b128 v[196:199], v0 offset:29696\n"
"ds_read_b128 v[200:203], v0 offset:1024\n"
"ds_read_b128 v[204:207], v0 offset:30720\n"
"ds_read_b128 v[208:211], v0 offset:2048\n"
"ds_read_b128 v[212:215], v0 offset:31744\n"
"ds_read_b128 v[216:219], v0 offset:3072\n"
"ds_read_b128 v[220:223], v0 offset:32768\n"
"v_mov_b32 v176, -1\n"
"v_mov_b32 v177, -1\n"
"v_mov_b32 v178, -1\n"
"v_mov_b32 v179, -1\n"
"s_waitcnt vmcnt(20) lgkmcnt(6)\n"
"v_mfma_f32_32x32x16_bf16 v[144:159], v[32:35], v[192:195], 0\n"
"v_max3_u32 v19, v19, v32, v33\n"
"s_add_u32 s94, s64, 12288\n"
"buffer_load_dwordx4 v[80:83], v0, s[44:47], s94 offen sc1\n"
"v_mfma_f32_32x32x16_bf16 v[160:175], v[32:35], v[196:199], 0\n"
"ds_read_b128 v[192:195], v0 offset:4096\n"
"ds_read_b128 v[196:199], v0 offset:33792\n"
"v_max3_u32 v19, v19, v34, v35\n"
"s_waitcnt vmcnt(20) lgkmcnt(6)\n"
"v_mfma_f32_32x32x16_bf16 v[144:159], v[36:39], v[200:203], v[144:159]\n"
"v_max3_u32 v19, v19, v36, v37\n"
"buffer_load_dwordx4 v[84:87], v0, s[44:47], s94 offen offset:1024 sc1\n"
"v_mfma_f32_32x32x16_bf16 v[160:175], v[36:39], v[204:207], v[160:175]\n"
"ds_read_b128 v[200:203], v0 offset:5120\n"
"ds_read_b128 v[204:207], v0 offset:34816\n"
"v_max3_u32 v19, v19, v38, v39\n"
"s_waitcnt vmcnt(20) lgkmcnt(6)\n"
"v_mfma_f32_32x32x16_bf16 v[144:159], v[40:43], v[208:211], v[144:159]\n"
"v_max3_u32 v19, v19, v40, v41\n"
"buffer_load_dwordx4 v[88:91], v0, s[44:47], s94 offen offset:2048 sc1\n"
"v_mfma_f32_32x32x16_bf16 v[160:175], v[40:43], v[212:215], v[160:175]\n"
"ds_read_b128 v[208:211], v0 offset:6144\n"
"ds_read_b128 v[212:215], v0 offset:35840\n"
"v_max3_u32 v19, v19, v42, v43\n"
"s_waitcnt vmcnt(20) lgkmcnt(6)\n"
"v_mfma_f32_32x32x16_bf16 v[144:159], v[44:47], v[216:219], v[144:159]\n"
"v_max3_u32 v19, v19, v44, v45\n"
"buffer_load_dwordx4 v[92:95], v0, s[44:47], s94 offen offset:3072 sc1\n"
"v_mfma_f32_32x32x16_bf16 v[160:175], v[44:47], v[220:223], v[160:175]\n"
"ds_read_b128 v[216:219], v0 offset:7168\n"
"ds_read_b128 v[220:223], v0 offset:36864\n"
"v_max3_u32 v19, v19, v46, v47\n"
"s_waitcnt vmcnt(20) lgkmcnt(6)\n"
"v_mfma_f32_32x32x16_bf16 v[144:159], v[48:51], v[192:195], v[144:159]\n"
"v_max3_u32 v19, v19, v48, v49\n"
"s_add_u32 s94, s64, 16384\n"
"buffer_load_dwordx4 v[96:99], v0, s[44:47], s94 offen sc1\n"
"v_mfma_f32_32x32x16_bf16 v[160:175], v[48:51], v[196:199], v[160:175]\n"
"ds_read_b128 v[192:195], v0 offset:8192\n"
"ds_read_b128 v[196:199], v0 offset:37888\n"
"v_max3_u32 v19, v19, v50, v51\n"
"buffer_load_dwordx4 v[100:103], v0, s[44:47], s94 offen offset:1024 sc1\n"
"s_waitcnt vmcnt(21) lgkmcnt(6)\n"
"v_mfma_f32_32x32x16_bf16 v[144:159], v[52:55], v[200:203], v[144:159]\n"
"v_max3_u32 v19, v19, v52, v53\n"
"v_mfma_f32_32x32x16_bf16 v[160:175], v[52:55], v[204:207], v[160:175]\n"
"ds_read_b128 v[200:203], v0 offset:9216\n"
"ds_read_b128 v[204:207], v0 offset:38912\n"
"v_max3_u32 v19, v19, v54, v55\n"
"buffer_load_dwordx4 v[104:107], v0, s[44:47], s94 offen offset:2048 sc1\n"
"s_waitcnt vmcnt(21) lgkmcnt(6)\n"
"v_mfma_f32_32x32x16_bf16 v[144:159], v[56:59], v[208:211], v[144:159]\n"
"v_max3_u32 v19, v19, v56, v57\n"
"v_mfma_f32_32x32x16_bf16 v[160:175], v[56:59], v[212:215], v[160:175]\n"
"ds_read_b128 v[208:211], v0 offset:10240\n"
"ds_read_b128 v[212:215], v0 offset:39936\n"
"v_max3_u32 v19, v19, v58, v59\n"
"buffer_load_dwordx4 v[108:111], v0, s[44:47], s94 offen offset:3072 sc1\n"
"s_waitcnt vmcnt(21) lgkmcnt(6)\n"
"v_mfma_f32_32x32x16_bf16 v[144:159], v[60:63], v[216:219], v[144:159]\n"
"v_max3_u32 v19, v19, v60, v61\n"
"v_mfma_f32_32x32x16_bf16 v[160:175], v[60:63], v[220:223], v[160:175]\n"
"ds_read_b128 v[216:219], v0 offset:11264\n"
"ds_read_b128 v[220:223], v0 offset:40960\n"
"v_max3_u32 v19, v19, v62, v63\n"
"s_add_u32 s94, s64, 20480\n"
"buffer_load_dwordx4 v[112:115], v0, s[44:47], s94 offen sc1\n"
"s_waitcnt vmcnt(21) lgkmcnt(6)\n"
"v_mfma_f32_32x32x16_bf16 v[144:159], v[64:67], v[192:195], v[144:159]\n"
"v_max3_u32 v19, v19, v64, v65\n"
"v_mfma_f32_32x32x16_bf16 v[160:175], v[64:67], v[196:199], v[160:175]\n"
"ds_read_b128 v[192:195], v0 offset:12288\n"
"ds_read_b128 v[196:199], v0 offset:41984\n"
"v_max3_u32 v19, v19, v66, v67\n"
"buffer_load_dwordx4 v[116:119], v0, s[44:47], s94 offen offset:1024 sc1\n"
"s_waitcnt vmcnt(21) lgkmcnt(6)\n"
"v_mfma_f32_32x32x16_bf16 v[144:159], v[68:71], v[200:203], v[144:159]\n"
"v_max3_u32 v19, v19, v68, v69\n"
"v_mfma_f32_32x32x16_bf16 v[160:175], v[68:71], v[204:207], v[160:175]\n"
"ds_read_b128 v[200:203], v0 offset:13312\n"
"ds_read_b128 v[204:207], v0 offset:43008\n"
"v_max3_u32 v19, v19, v70, v71\n"
"buffer_load_dwordx4 v[120:123], v0, s[44:47], s94 offen offset:2048 sc1\n"
"s_waitcnt vmcnt(21) lgkmcnt(6)\n"
"v_mfma_f32_32x32x16_bf16 v[144:159], v[72:75], v[208:211], v[144:159]\n"
"v_max3_u32 v19, v19, v72, v73\n"
"buffer_load_dwordx4 v[124:127], v0, s[44:47], s94 offen offset:3072 sc1\n"
"v_mfma_f32_32x32x16_bf16 v[160:175], v[72:75], v[212:215], v[160:175]\n"
"ds_read_b128 v[208:211], v0 offset:14336\n"
"ds_read_b128 v[212:215], v0 offset:44032\n"
"v_max3_u32 v19, v19, v74, v75\n"
"s_waitcnt vmcnt(21) lgkmcnt(6)\n"
"v_mfma_f32_32x32x16_bf16 v[144:159], v[76:79], v[216:219], v[144:159]\n"
"v_max3_u32 v19, v19, v76, v77\n"
"s_add_u32 s94, s64, 24576\n"
"buffer_load_dwordx4 v[128:131], v0, s[44:47], s94 offen sc1\n"
"v_mfma_f32_32x32x16_bf16 v[160:175], v[76:79], v[220:223], v[160:175]\n"
"ds_read_b128 v[216:219], v0 offset:15360\n"
"ds_read_b128 v[220:223], v0 offset:45056\n"
"v_max3_u32 v19, v19, v78, v79\n"
"s_waitcnt vmcnt(12) lgkmcnt(6)\n"
"v_mfma_f32_32x32x16_bf16 v[144:159], v[80:83], v[192:195], v[144:159]\n"
"v_max3_u32 v19, v19, v80, v81\n"
"buffer_load_dwordx4 v[132:135], v0, s[44:47], s94 offen offset:1024 sc1\n"
"v_mfma_f32_32x32x16_bf16 v[160:175], v[80:83], v[196:199], v[160:175]\n"
"ds_read_b128 v[192:195], v0 offset:16384\n"
"ds_read_b128 v[196:199], v0 offset:46080\n"
"v_max3_u32 v19, v19, v82, v83\n"
"s_waitcnt vmcnt(12) lgkmcnt(6)\n"
"v_mfma_f32_32x32x16_bf16 v[144:159], v[84:87], v[200:203], v[144:159]\n"
"v_max3_u32 v19, v19, v84, v85\n"
"buffer_load_dwordx4 v[136:139], v0, s[44:47], s94 offen offset:2048 sc1\n"
"v_max3_u32 v19, v19, v224, v225\n"
"v_max3_u32 v19, v19, v226, v227\n"
"v_mfma_f32_32x32x16_bf16 v[160:175], v[84:87], v[204:207], v[160:175]\n"
"ds_read_b128 v[200:203], v0 offset:17408\n"
"ds_read_b128 v[204:207], v0 offset:47104\n"
"v_max3_u32 v19, v19, v86, v87\n"
"v_max3_u32 v19, v19, v228, v229\n"
"v_max3_u32 v19, v19, v230, v231\n"
"v_max3_u32 v19, v19, v232, v233\n"
"s_waitcnt vmcnt(12) lgkmcnt(6)\n"
"v_mfma_f32_32x32x16_bf16 v[144:159], v[88:91], v[208:211], v[144:159]\n"
"v_max3_u32 v19, v19, v88, v89\n"
"buffer_load_dwordx4 v[140:143], v0, s[44:47], s94 offen offset:3072 sc1\n"
"v_max3_u32 v19, v19, v234, v235\n"
"v_mfma_f32_32x32x16_bf16 v[160:175], v[88:91], v[212:215], v[160:175]\n"
"ds_read_b128 v[208:211], v0 offset:18432\n"
"ds_read_b128 v[212:215], v0 offset:48128\n"
"v_max3_u32 v19, v19, v90, v91\n"
"v_max3_u32 v19, v19, v236, v237\n"
"v_add_f32 v224, v224, v226\n"
"v_add_f32 v225, v225, v227\n"
"v_add_f32 v228, v228, v230\n"
"s_waitcnt vmcnt(12) lgkmcnt(6)\n"
"v_mfma_f32_32x32x16_bf16 v[144:159], v[92:95], v[216:219], v[144:159]\n"
"v_max3_u32 v19, v19, v92, v93\n"
"s_add_u32 m0, s89, 59392\n"
"s_add_u32 s95, s86, 59392\n"
"buffer_load_dwordx4 v0, s[40:43], s95 offen lds\n"
"v_add_f32 v229, v229, v231\n"
"v_mfma_f32_32x32x16_bf16 v[160:175], v[92:95], v[220:223], v[160:175]\n"
"ds_read_b128 v[216:219], v0 offset:19456\n"
"ds_read_b128 v[220:223], v0 offset:49152\n"
"v_max3_u32 v19, v19, v94, v95\n"
"buffer_load_dwordx4 v0, s[40:43], s95 offen offset:1024 lds\n"
"v_add_f32 v232, v232, v234\n"
"s_waitcnt vmcnt(13) lgkmcnt(6)\n"
"v_mfma_f32_32x32x16_bf16 v[144:159], v[96:99], v[192:195], v[144:159]\n"
"v_max3_u32 v19, v19, v96, v97\n"
"v_add_f32 v233, v233, v235\n"
"v_add_f32 v224, v224, v228\n"
"v_add_f32 v225, v225, v229\n"
"v_mfma_f32_32x32x16_bf16 v[160:175], v[96:99], v[196:199], v[160:175]\n"
"ds_read_b128 v[192:195], v0 offset:20480\n"
"ds_read_b128 v[196:199], v0 offset:50176\n"
"v_max3_u32 v19, v19, v98, v99\n"
"buffer_load_dwordx4 v0, s[40:43], s95 offen offset:2048 lds\n"
"v_add_f32 v232, v232, v236\n"
"s_waitcnt vmcnt(13) lgkmcnt(6)\n"
"v_mfma_f32_32x32x16_bf16 v[144:159], v[100:103], v[200:203], v[144:159]\n"
"v_max3_u32 v19, v19, v100, v101\n"
"v_add_f32 v233, v233, v237\n"
"v_add_f32 v224, v224, v232\n"
"v_add_f32 v225, v225, v233\n"
"v_mov_b32 v240, v224\n"
"v_mfma_f32_32x32x16_bf16 v[160:175], v[100:103], v[204:207], v[160:175]\n"
"ds_read_b128 v[200:203], v0 offset:21504\n"
"ds_read_b128 v[204:207], v0 offset:51200\n"
"v_max3_u32 v19, v19, v102, v103\n"
"buffer_load_dwordx4 v0, s[40:43], s95 offen offset:3072 lds\n"
"v_mov_b32 v241, v225\n"
"s_waitcnt vmcnt(13) lgkmcnt(6)\n"
"v_mfma_f32_32x32x16_bf16 v[144:159], v[104:107], v[208:211], v[144:159]\n"
"v_max3_u32 v19, v19, v104, v105\n"
"v_permlane32_swap_b32 v224, v240\n"
"v_permlane32_swap_b32 v225, v241\n"
"v_add_f32 v224, v224, v240\n"
"v_mfma_f32_32x32x16_bf16 v[160:175], v[104:107], v[212:215], v[160:175]\n"
"ds_read_b128 v[208:211], v0 offset:22528\n"
"ds_read_b128 v[212:215], v0 offset:52224\n"
"v_max3_u32 v19, v19, v106, v107\n"
"s_add_u32 m0, s89, 63488\n"
"s_add_u32 s95, s86, 63488\n"
"buffer_load_dwordx4 v0, s[40:43], s95 offen lds\n"
"v_add_f32 v225, v225, v241\n"
"s_waitcnt vmcnt(13) lgkmcnt(6)\n"
"v_mfma_f32_32x32x16_bf16 v[144:159], v[108:111], v[216:219], v[144:159]\n"
"v_max3_u32 v19, v19, v108, v109\n"
"v_mul_f32 v224, 0x3b124925, v224\n"
"v_mul_f32 v225, 0x3b124925, v225\n"
"v_fma_f32 v225, -v224, v224, v225\n"
"v_max_f32 v225, 0, v225\n"
"v_mfma_f32_32x32x16_bf16 v[160:175], v[108:111], v[220:223], v[160:175]\n"
"ds_read_b128 v[216:219], v0 offset:23552\n"
"ds_read_b128 v[220:223], v0 offset:53248\n"
"v_max3_u32 v19, v19, v110, v111\n"
"buffer_load_dwordx4 v0, s[40:43], s95 offen offset:1024 lds\n"
"v_cvt_pk_bf16_f32 v225, v225, v225\n"
"s_waitcnt vmcnt(13) lgkmcnt(6)\n"
"v_mfma_f32_32x32x16_bf16 v[144:159], v[112:115], v[192:195], v[144:159]\n"
"v_max3_u32 v19, v19, v112, v113\n"
"buffer_load_dwordx4 v0, s[40:43], s95 offen offset:2048 lds\n"
"v_and_b32 v225, 0xffff0000, v225\n"
"v_mfma_f32_32x32x16_bf16 v[160:175], v[112:115], v[196:199], v[160:175]\n"
"ds_read_b128 v[192:195], v0 offset:24576\n"
"ds_read_b128 v[196:199], v0 offset:54272\n"
"v_max3_u32 v19, v19, v114, v115\n"
"v_add_f32 v225, 0x3727c5ac, v225\n"
"v_cvt_pk_bf16_f32 v225, v225, v225\n"
"v_and_b32 v225, 0xffff0000, v225\n"
"s_waitcnt vmcnt(13) lgkmcnt(6)\n"
"v_mfma_f32_32x32x16_bf16 v[144:159], v[116:119], v[200:203], v[144:159]\n"
"v_max3_u32 v19, v19, v116, v117\n"
"buffer_load_dwordx4 v0, s[40:43], s95 offen offset:3072 lds\n"
"v_mfma_f32_32x32x16_bf16 v[160:175], v[116:119], v[204:207], v[160:175]\n"
"ds_read_b128 v[200:203], v0 offset:25600\n"
"ds_read_b128 v[204:207], v0 offset:55296\n"
"v_max3_u32 v19, v19, v118, v119\n"
"v_sqrt_f32 v225, v225\n"
"s_nop 0\n"
"v_cvt_pk_bf16_f32 v225, v225, v225\n"
"v_and_b32 v225, 0xffff0000, v225\n"
"s_waitcnt vmcnt(13) lgkmcnt(6)\n"
"v_mfma_f32_32x32x16_bf16 v[144:159], v[120:123], v[208:211], v[144:159]\n"
"v_max3_u32 v19, v19, v120, v121\n"
"s_add_u32 s94, s72, 14614528\n"
"buffer_store_dwordx4 v[176:179], v0, s[44:47], s94 offen sc1\n"
"v_mfma_f32_32x32x16_bf16 v[160:175], v[120:123], v[212:215], v[160:175]\n"
"ds_read_b128 v[208:211], v0 offset:26624\n"
"ds_read_b128 v[212:215], v0 offset:56320\n"
"v_max3_u32 v19, v19, v122, v123\n"
"v_rcp_f32 v16, v225\n"
"v_cvt_pk_bf16_f32 v20, -v224, -v224\n"
"v_lshrrev_b32 v241, 16, v225\n"
"s_waitcnt vmcnt(13) lgkmcnt(6)\n"
"v_mfma_f32_32x32x16_bf16 v[144:159], v[124:127], v[216:219], v[144:159]\n"
"v_max3_u32 v19, v19, v124, v125\n"
"v_mul_f32 v18, v225, v225\n"
"v_mov_b32 v21, v20\n"
"v_mov_b32 v22, v20\n"
"v_cndmask_b32_e64 v23, v241, v20, s[62:63]\n"
"v_mfma_f32_32x32x16_bf16 v[160:175], v[124:127], v[220:223], v[160:175]\n"
"ds_read_b128 v[216:219], v0 offset:27648\n"
"ds_read_b128 v[220:223], v0 offset:57344\n"
"v_max3_u32 v19, v19, v126, v127\n"
"v_mul_f32 v17, 0xbfb8aa3b, v16\n"
"ds_write_b32 v25, v16\n"
"ds_read_b128 v[224:227], v26\n"
"s_waitcnt vmcnt(12) lgkmcnt(8)\n"
"v_mfma_f32_32x32x16_bf16 v[144:159], v[128:131], v[192:195], v[144:159]\n"
"v_max3_u32 v19, v19, v128, v129\n"
"ds_read_b128 v[228:231], v26 offset:32\n"
"ds_read_b128 v[232:235], v26 offset:64\n"
"ds_read_b128 v[236:239], v26 offset:96\n"
"v_mfma_f32_32x32x16_bf16 v[160:175], v[128:131], v[196:199], v[160:175]\n"
"ds_read_b128 v[192:195], v0 offset:28672\n"
"ds_read_b128 v[196:199], v0 offset:58368\n"
"v_max3_u32 v19, v19, v130, v131\n"
"s_waitcnt vmcnt(11) lgkmcnt(11)\n"
"v_mfma_f32_32x32x16_bf16 v[144:159], v[132:135], v[200:203], v[144:159]\n"
"v_max3_u32 v19, v19, v132, v133\n"
"v_mfma_f32_32x32x16_bf16 v[160:175], v[132:135], v[204:207], v[160:175]\n"
"v_max3_u32 v19, v19, v134, v135\n"
"s_waitcnt vmcnt(10) lgkmcnt(9)\n"
"v_mfma_f32_32x32x16_bf16 v[144:159], v[136:139], v[208:211], v[144:159]\n"
"v_max3_u32 v19, v19, v136, v137\n"
"v_mfma_f32_32x32x16_bf16 v[160:175], v[136:139], v[212:215], v[160:175]\n"
"v_max3_u32 v19, v19, v138, v139\n"
"s_waitcnt vmcnt(9) lgkmcnt(7)\n"
"v_mfma_f32_32x32x16_bf16 v[144:159], v[140:143], v[216:219], v[144:159]\n"
"v_max3_u32 v19, v19, v140, v141\n"
"v_mfma_f32_32x32x16_bf16 v[160:175], v[140:143], v[220:223], v[160:175]\n"
"v_max3_u32 v19, v19, v142, v143\n"
"s_waitcnt lgkmcnt(0)\n"
"v_mfma_f32_32x32x16_bf16 v[144:159], v[20:23], v[192:195], v[144:159]\n"
"v_mfma_f32_32x32x16_bf16 v[160:175], v[20:23], v[196:199], v[160:175]\n"
"v_mov_b32 v180, -1\n"
"v_mov_b32 v181, -1\n"
"v_mov_b32 v182, -1\n"
"v_mov_b32 v183, -1\n"
"v_mov_b32 v248, s58\n"
"v_mov_b32 v249, s58\n"
"v_mov_b32 v250, s61\n"
"v_mov_b32 v251, s61\n"
"v_mov_b32 v252, v16\n"
"v_mov_b32 v253, v16\n"
"v_add_u32 v244, s90, v0\n"
"v_add_u32 v245, s91, v0\n"
"v_pk_fma_f32 v[144:145], v[144:145], v[224:225], v[248:249]\n"
"v_pk_fma_f32 v[146:147], v[146:147], v[226:227], v[248:249]\n"
"v_pk_mul_f32 v[240:241], v[144:145], v[250:251]\n"
"v_pk_mul_f32 v[242:243], v[146:147], v[250:251]\n"
"v_exp_f32_e64 v240, v240 clamp\n"
"v_exp_f32_e64 v241, v241 clamp\n"
"v_exp_f32_e64 v242, v242 clamp\n"
"v_exp_f32_e64 v243, v243 clamp\n"
"s_add_u32 s94, s82, 0\n"
"buffer_store_dwordx4 v[180:183], v0, s[44:47], s94 offen sc1\n"
"v_max_f32 v144, s58, v144\n"
"v_max_f32 v145, s58, v145\n"
"v_max_f32 v146, s58, v146\n"
"v_max_f32 v147, s58, v147\n"
"v_pk_add_f32 v[144:145], v[144:145], v[240:241]\n"
"v_pk_add_f32 v[146:147], v[146:147], v[242:243]\n"
"v_pk_fma_f32 v[148:149], v[148:149], v[228:229], v[248:249]\n"
"v_pk_fma_f32 v[150:151], v[150:151], v[230:231], v[248:249]\n"
"s_add_u32 s94, s82, 0\n"
"buffer_store_dwordx4 v[180:183], v0, s[44:47], s94 offen offset:1024 sc1\n"
"v_pk_mul_f32 v[240:241], v[148:149], v[250:251]\n"
"v_pk_mul_f32 v[242:243], v[150:151], v[250:251]\n"
"v_exp_f32_e64 v240, v240 clamp\n"
"v_exp_f32_e64 v241, v241 clamp\n"
"v_exp_f32_e64 v242, v242 clamp\n"
"v_exp_f32_e64 v243, v243 clamp\n"
"v_max_f32 v148, s58, v148\n"
"v_max_f32 v149, s58, v149\n"
"s_add_u32 s94, s82, 0\n"
"buffer_store_dwordx4 v[180:183], v0, s[44:47], s94 offen offset:2048 sc1\n"
"v_max_f32 v150, s58, v150\n"
"v_max_f32 v151, s58, v151\n"
"v_pk_add_f32 v[148:149], v[148:149], v[240:241]\n"
"v_pk_add_f32 v[150:151], v[150:151], v[242:243]\n"
"v_pk_fma_f32 v[152:153], v[152:153], v[232:233], v[248:249]\n"
"v_pk_fma_f32 v[154:155], v[154:155], v[234:235], v[248:249]\n"
"v_pk_mul_f32 v[240:241], v[152:153], v[250:251]\n"
"v_pk_mul_f32 v[242:243], v[154:155], v[250:251]\n"
"s_add_u32 s94, s82, 0\n"
"buffer_store_dwordx4 v[180:183], v0, s[44:47], s94 offen offset:3072 sc1\n"
"v_exp_f32_e64 v240, v240 clamp\n"
"v_exp_f32_e64 v241, v241 clamp\n"
"v_exp_f32_e64 v242, v242 clamp\n"
"v_exp_f32_e64 v243, v243 clamp\n"
"v_max_f32 v152, s58, v152\n"
"v_max_f32 v153, s58, v153\n"
"v_max_f32 v154, s58, v154\n"
"v_max_f32 v155, s58, v155\n"
"s_add_u32 s94, s82, 4096\n"
"buffer_store_dwordx4 v[180:183], v0, s[44:47], s94 offen sc1\n"
"v_pk_add_f32 v[152:153], v[152:153], v[240:241]\n"
"v_pk_add_f32 v[154:155], v[154:155], v[242:243]\n"
"v_pk_fma_f32 v[156:157], v[156:157], v[236:237], v[248:249]\n"
"v_pk_fma_f32 v[158:159], v[158:159], v[238:239], v[248:249]\n"
"v_pk_mul_f32 v[240:241], v[156:157], v[250:251]\n"
"v_pk_mul_f32 v[242:243], v[158:159], v[250:251]\n"
"v_exp_f32_e64 v240, v240 clamp\n"
"v_exp_f32_e64 v241, v241 clamp\n"
"s_add_u32 s94, s82, 4096\n"
"buffer_store_dwordx4 v[180:183], v0, s[44:47], s94 offen offset:1024 sc1\n"
"v_exp_f32_e64 v242, v242 clamp\n"
"v_exp_f32_e64 v243, v243 clamp\n"
"v_max_f32 v156, s58, v156\n"
"v_max_f32 v157, s58, v157\n"
"v_max_f32 v158, s58, v158\n"
"v_max_f32 v159, s58, v159\n"
"v_pk_add_f32 v[156:157], v[156:157], v[240:241]\n"
"v_pk_add_f32 v[158:159], v[158:159], v[242:243]\n"
"s_add_u32 s94, s82, 4096\n"
"buffer_store_dwordx4 v[180:183], v0, s[44:47], s94 offen offset:2048 sc1\n"
"v_cvt_pk_bf16_f32 v144, v144, v145\n"
"v_cvt_pk_bf16_f32 v145, v146, v147\n"
"v_cvt_pk_bf16_f32 v146, v148, v149\n"
"v_cvt_pk_bf16_f32 v147, v150, v151\n"
"v_cvt_pk_bf16_f32 v148, v152, v153\n"
"v_cvt_pk_bf16_f32 v149, v154, v155\n"
"v_cvt_pk_bf16_f32 v150, v156, v157\n"
"v_cvt_pk_bf16_f32 v151, v158, v159\n"
"v_pk_mul_f32 v[160:161], v[160:161], v[224:225]\n"
"v_pk_mul_f32 v[162:163], v[162:163], v[226:227]\n"
"v_pk_mul_f32 v[164:165], v[164:165], v[228:229]\n"
"v_pk_mul_f32 v[166:167], v[166:167], v[230:231]\n"
"v_pk_mul_f32 v[168:169], v[168:169], v[232:233]\n"
"v_pk_mul_f32 v[170:171], v[170:171], v[234:235]\n"
"v_pk_mul_f32 v[172:173], v[172:173], v[236:237]\n"
"v_pk_mul_f32 v[174:175], v[174:175], v[238:239]\n"
"v_cvt_pk_bf16_f32 v160, v160, v161\n"
"v_cvt_pk_bf16_f32 v161, v162, v163\n"
"v_cvt_pk_bf16_f32 v162, v164, v165\n"
"v_cvt_pk_bf16_f32 v163, v166, v167\n"
"v_cvt_pk_bf16_f32 v164, v168, v169\n"
"v_cvt_pk_bf16_f32 v165, v170, v171\n"
"v_cvt_pk_bf16_f32 v166, v172, v173\n"
"v_cvt_pk_bf16_f32 v167, v174, v175\n"
"v_mfma_f32_32x32x16_bf16 v[224:239], v[144:147], v[160:163], 0\n"
"s_nop 0\n"
"v_mfma_f32_32x32x16_bf16 v[224:239], v[148:151], v[164:167], v[224:239]\n"
"s_nop 11\n"
"ds_write_b128 v244, v[224:227]\n"
"ds_write_b128 v244, v[228:231] offset:1024\n"
"ds_write_b128 v244, v[232:235] offset:2048\n"
"ds_write_b128 v244, v[236:239] offset:3072\n"
"s_waitcnt vmcnt(8)\n"
"s_waitcnt lgkmcnt(0)\n"
"s_barrier\n"
"ds_read_b128 v[160:163], v245\n"
"ds_read_b128 v[164:167], v245 offset:4096\n"
"ds_read_b128 v[168:171], v245 offset:8192\n"
"ds_read_b128 v[172:175], v245 offset:12288\n"
"ds_read_b128 v[200:203], v0 offset:59392\n"
"ds_read_b128 v[204:207], v0 offset:60416\n"
"ds_read_b128 v[208:211], v0 offset:61440\n"
"ds_read_b128 v[212:215], v0 offset:62464\n"
"ds_read_b128 v[216:219], v0 offset:63488\n"
"ds_read_b128 v[220:223], v0 offset:64512\n"
"ds_read_b128 v[192:195], v1\n"
"ds_read_b128 v[196:199], v1 offset:1024\n"
"s_waitcnt lgkmcnt(10)\n"
"v_pk_add_f32 v[160:161], v[160:161], v[164:165]\n"
"v_pk_add_f32 v[162:163], v[162:163], v[166:167]\n"
"s_waitcnt lgkmcnt(8)\n"
"v_pk_add_f32 v[168:169], v[168:169], v[172:173]\n"
"v_pk_add_f32 v[170:171], v[170:171], v[174:175]\n"
"v_pk_add_f32 v[160:161], v[160:161], v[168:169]\n"
"v_pk_add_f32 v[162:163], v[162:163], v[170:171]\n"
"v_min_u32 v160, -2, v160\n"
"v_min_u32 v161, -2, v161\n"
"v_min_u32 v162, -2, v162\n"
"v_min_u32 v163, -2, v163\n"
"s_waitcnt vmcnt(7)\n"
"buffer_store_dwordx4 v[160:163], v0, s[44:47], s72 offen sc1\n"
"s_waitcnt lgkmcnt(6)\n"
"v_mfma_f32_32x32x16_bf16 v[176:191], v[200:203], v[32:35], 0\n"
"buffer_load_dwordx4 a[224:227], v0, s[40:43], s67 offen\n"
"v_mfma_f32_32x32x16_bf16 v[176:191], v[204:207], v[36:39], v[176:191]\n"
"ds_read_b128 v[200:203], v1 offset:2048\n"
"ds_read_b128 v[204:207], v1 offset:3072\n"
"s_waitcnt lgkmcnt(6)\n"
"v_mfma_f32_32x32x16_bf16 v[176:191], v[208:211], v[40:43], v[176:191]\n"
"buffer_load_dwordx4 a[228:231], v0, s[40:43], s67 offen offset:1024\n"
"v_mfma_f32_32x32x16_bf16 v[176:191], v[212:215], v[44:47], v[176:191]\n"
"ds_read_b128 v[208:211], v1 offset:4096\n"
"ds_read_b128 v[212:215], v1 offset:5120\n"
"s_waitcnt lgkmcnt(6)\n"
"v_mfma_f32_32x32x16_bf16 v[176:191], v[216:219], v[48:51], v[176:191]\n"
"buffer_load_dwordx4 a[232:235], v0, s[40:43], s67 offen offset:2048\n"
"v_mfma_f32_32x32x16_bf16 v[176:191], v[220:223], v[52:55], v[176:191]\n"
"ds_read_b128 v[216:219], v1 offset:6144\n"
"ds_read_b128 v[220:223], v1 offset:7168\n"
"s_waitcnt lgkmcnt(6)\n"
"v_mfma_f32_32x32x16_bf16 v[176:191], v[192:195], v[56:59], v[176:191]\n"
"buffer_load_dwordx4 a[236:239], v0, s[40:43], s67 offen offset:3072\n"
"v_mfma_f32_32x32x16_bf16 v[176:191], v[196:199], v[60:63], v[176:191]\n"
"ds_read_b128 v[192:195], v1 offset:8192\n"
"ds_read_b128 v[196:199], v1 offset:9216\n"
"s_waitcnt lgkmcnt(6)\n"
"v_mfma_f32_32x32x16_bf16 v[176:191], v[200:203], v[64:67], v[176:191]\n"
"s_add_u32 s94, s67, 4096\n"
"buffer_load_dwordx4 a[240:243], v0, s[40:43], s94 offen\n"
"v_mfma_f32_32x32x16_bf16 v[176:191], v[204:207], v[68:71], v[176:191]\n"
"ds_read_b128 v[200:203], v1 offset:10240\n"
"ds_read_b128 v[204:207], v1 offset:11264\n"
"s_waitcnt lgkmcnt(6)\n"
"v_mfma_f32_32x32x16_bf16 v[176:191], v[208:211], v[72:75], v[176:191]\n"
"buffer_load_dwordx4 a[244:247], v0, s[40:43], s94 offen offset:1024\n"
"v_mfma_f32_32x32x16_bf16 v[176:191], v[212:215], v[76:79], v[176:191]\n"
"ds_read_b128 v[208:211], v1 offset:12288\n"
"ds_read_b128 v[212:215], v1 offset:13312\n"
"s_waitcnt lgkmcnt(6)\n"
"v_mfma_f32_32x32x16_bf16 v[176:191], v[216:219], v[80:83], v[176:191]\n"
"buffer_load_dwordx4 a[248:251], v0, s[40:43], s94 offen offset:2048\n"
"v_mfma_f32_32x32x16_bf16 v[176:191], v[220:223], v[84:87], v[176:191]\n"
"ds_read_b128 v[216:219], v1 offset:14336\n"
"ds_read_b128 v[220:223], v1 offset:15360\n"
"s_waitcnt lgkmcnt(6)\n"
"v_mfma_f32_32x32x16_bf16 v[176:191], v[192:195], v[88:91], v[176:191]\n"
"buffer_load_dwordx4 a[252:255], v0, s[40:43], s94 offen offset:3072\n"
"v_mfma_f32_32x32x16_bf16 v[176:191], v[196:199], v[92:95], v[176:191]\n"
"ds_read_b128 v[192:195], v1 offset:16384\n"
"ds_read_b128 v[196:199], v1 offset:17408\n"
"s_waitcnt lgkmcnt(6)\n"
"v_mfma_f32_32x32x16_bf16 v[176:191], v[200:203], v[96:99], v[176:191]\n"
"v_mfma_f32_32x32x16_bf16 v[176:191], v[204:207], v[100:103], v[176:191]\n"
"ds_read_b128 v[200:203], v1 offset:18432\n"
"ds_read_b128 v[204:207], v1 offset:19456\n"
"s_waitcnt lgkmcnt(6)\n"
"v_mfma_f32_32x32x16_bf16 v[176:191], v[208:211], v[104:107], v[176:191]\n"
"v_mfma_f32_32x32x16_bf16 v[176:191], v[212:215], v[108:111], v[176:191]\n"
"ds_read_b128 v[208:211], v1 offset:20480\n"
"ds_read_b128 v[212:215], v1 offset:21504\n"
"s_waitcnt lgkmcnt(6)\n"
"v_mfma_f32_32x32x16_bf16 v[176:191], v[216:219], v[112:115], v[176:191]\n"
"v_mfma_f32_32x32x16_bf16 v[176:191], v[220:223], v[116:119], v[176:191]\n"
"ds_read_b128 v[216:219], v1 offset:22528\n"
"s_waitcnt lgkmcnt(5)\n"
"v_mfma_f32_32x32x16_bf16 v[176:191], v[192:195], v[120:123], v[176:191]\n"
"v_mfma_f32_32x32x16_bf16 v[176:191], v[196:199], v[124:127], v[176:191]\n"
"s_waitcnt lgkmcnt(3)\n"
"v_mfma_f32_32x32x16_bf16 v[176:191], v[200:203], v[128:131], v[176:191]\n"
"v_mfma_f32_32x32x16_bf16 v[176:191], v[204:207], v[132:135], v[176:191]\n"
"s_waitcnt lgkmcnt(1)\n"
"v_mfma_f32_32x32x16_bf16 v[176:191], v[208:211], v[136:139], v[176:191]\n"
"v_mfma_f32_32x32x16_bf16 v[176:191], v[212:215], v[140:143], v[176:191]\n"
"s_waitcnt lgkmcnt(0)\n"
"v_mfma_f32_32x32x16_bf16 v[176:191], v[216:219], v[20:23], v[176:191]\n"
"s_barrier\n"
"s_nop 10\n"
"v_pk_fma_f32 v[176:177], v[176:177], v[252:253], v[248:249]\n"
"v_pk_fma_f32 v[178:179], v[178:179], v[252:253], v[248:249]\n"
"v_pk_mul_f32 v[240:241], v[176:177], v[250:251]\n"
"v_pk_mul_f32 v[242:243], v[178:179], v[250:251]\n"
"v_exp_f32_e64 v240, v240 clamp\n"
"v_exp_f32_e64 v241, v241 clamp\n"
"v_exp_f32_e64 v242, v242 clamp\n"
"v_exp_f32_e64 v243, v243 clamp\n"
"v_max_f32 v176, s58, v176\n"
"v_max_f32 v177, s58, v177\n"
"v_max_f32 v178, s58, v178\n"
"v_max_f32 v179, s58, v179\n"
"v_pk_add_f32 v[176:177], v[176:177], v[240:241]\n"
"v_pk_add_f32 v[178:179], v[178:179], v[242:243]\n"
"v_pk_fma_f32 v[180:181], v[180:181], v[252:253], v[248:249]\n"
"v_pk_fma_f32 v[182:183], v[182:183], v[252:253], v[248:249]\n"
"v_pk_mul_f32 v[240:241], v[180:181], v[250:251]\n"
"v_pk_mul_f32 v[242:243], v[182:183], v[250:251]\n"
"v_exp_f32_e64 v240, v240 clamp\n"
"v_exp_f32_e64 v241, v241 clamp\n"
"v_exp_f32_e64 v242, v242 clamp\n"
"v_exp_f32_e64 v243, v243 clamp\n"
"v_max_f32 v180, s58, v180\n"
"v_max_f32 v181, s58, v181\n"
"v_max_f32 v182, s58, v182\n"
"v_max_f32 v183, s58, v183\n"
"v_pk_add_f32 v[180:181], v[180:181], v[240:241]\n"
"v_pk_add_f32 v[182:183], v[182:183], v[242:243]\n"
"v_pk_fma_f32 v[184:185], v[184:185], v[252:253], v[248:249]\n"
"v_pk_fma_f32 v[186:187], v[186:187], v[252:253], v[248:249]\n"
"v_pk_mul_f32 v[240:241], v[184:185], v[250:251]\n"
"v_pk_mul_f32 v[242:243], v[186:187], v[250:251]\n"
"v_exp_f32_e64 v240, v240 clamp\n"
"v_exp_f32_e64 v241, v241 clamp\n"
"v_exp_f32_e64 v242, v242 clamp\n"
"v_exp_f32_e64 v243, v243 clamp\n"
"v_max_f32 v184, s58, v184\n"
"v_max_f32 v185, s58, v185\n"
"v_max_f32 v186, s58, v186\n"
"v_max_f32 v187, s58, v187\n"
"v_pk_add_f32 v[184:185], v[184:185], v[240:241]\n"
"v_pk_add_f32 v[186:187], v[186:187], v[242:243]\n"
"v_pk_fma_f32 v[188:189], v[188:189], v[252:253], v[248:249]\n"
"v_pk_fma_f32 v[190:191], v[190:191], v[252:253], v[248:249]\n"
"v_pk_mul_f32 v[240:241], v[188:189], v[250:251]\n"
"v_pk_mul_f32 v[242:243], v[190:191], v[250:251]\n"
"v_exp_f32_e64 v240, v240 clamp\n"
"v_exp_f32_e64 v241, v241 clamp\n"
"v_exp_f32_e64 v242, v242 clamp\n"
"v_exp_f32_e64 v243, v243 clamp\n"
"v_max_f32 v188, s58, v188\n"
"v_max_f32 v189, s58, v189\n"
"v_max_f32 v190, s58, v190\n"
"v_max_f32 v191, s58, v191\n"
"v_pk_add_f32 v[188:189], v[188:189], v[240:241]\n"
"v_pk_add_f32 v[190:191], v[190:191], v[242:243]\n"
"v_cvt_pk_bf16_f32 v176, v176, v177\n"
"v_cvt_pk_bf16_f32 v177, v178, v179\n"
"v_cvt_pk_bf16_f32 v178, v180, v181\n"
"v_cvt_pk_bf16_f32 v179, v182, v183\n"
"v_cvt_pk_bf16_f32 v180, v184, v185\n"
"v_cvt_pk_bf16_f32 v181, v186, v187\n"
"v_cvt_pk_bf16_f32 v182, v188, v189\n"
"buffer_load_dwordx4 v[32:35], v0, s[44:47], s73 offen sc1\n"
"v_cvt_pk_bf16_f32 v183, v190, v191\n"
"v_cndmask_b32_e64 v182, 0, v182, s[62:63]\n"
"v_cndmask_b32_e64 v183, 0, v183, s[62:63]\n"
"s_add_u32 s94, s73, 4096\n"
"buffer_load_dwordx4 v[36:39], v0, s[44:47], s94 offen sc1\n"
"s_add_u32 s94, s73, 8192\n"
"buffer_load_dwordx4 v[40:43], v0, s[44:47], s94 offen sc1\n"
"s_add_u32 s94, s73, 12288\n"
"buffer_load_dwordx4 v[44:47], v0, s[44:47], s94 offen sc1\n"
"s_add_u32 s94, s73, 16384\n"
"buffer_load_dwordx4 v[48:51], v0, s[44:47], s94 offen sc1\n"
"s_add_u32 s94, s73, 20480\n"
"buffer_load_dwordx4 v[52:55], v0, s[44:47], s94 offen sc1\n"
"s_add_u32 s94, s73, 24576\n"
"buffer_load_dwordx4 v[56:59], v0, s[44:47], s94 offen sc1\n"
"s_add_u32 s94, s73, 28672\n"
"buffer_load_dwordx4 v[60:63], v0, s[44:47], s94 offen sc1\n"
"s_add_u32 s94, s73, 32768\n"
"buffer_load_dwordx4 v[64:67], v0, s[44:47], s94 offen sc1\n"
"s_add_u32 s94, s73, 36864\n"
"buffer_load_dwordx4 v[68:71], v0, s[44:47], s94 offen sc1\n"
"s_add_u32 s94, s73, 40960\n"
"buffer_load_dwordx4 v[72:75], v0, s[44:47], s94 offen sc1\n"
"s_add_u32 s94, s73, 45056\n"
"buffer_load_dwordx4 v[76:79], v0, s[44:47], s94 offen sc1\n"
"s_add_u32 s94, s73, 49152\n"
"buffer_load_dwordx4 v[80:83], v0, s[44:47], s94 offen sc1\n"
"s_add_u32 s94, s73, 53248\n"
"buffer_load_dwordx4 v[84:87], v0, s[44:47], s94 offen sc1\n"
"s_add_u32 s94, s73, 57344\n"
"buffer_load_dwordx4 v[88:91], v0, s[44:47], s94 offen sc1\n"
"s_add_u32 s94, s73, 61440\n"
"buffer_load_dwordx4 v[92:95], v0, s[44:47], s94 offen sc1\n"
"buffer_load_dwordx4 v[96:99], v0, s[40:43], s66 offen\n"
"buffer_load_dwordx4 v[100:103], v0, s[40:43], s66 offen offset:1024\n"
"buffer_load_dwordx4 v[104:107], v0, s[40:43], s66 offen offset:2048\n"
"buffer_load_dwordx4 v[108:111], v0, s[40:43], s66 offen offset:3072\n"
"s_add_u32 s94, s66, 4096\n"
"buffer_load_dwordx4 v[112:115], v0, s[40:43], s94 offen\n"
"buffer_load_dwordx4 v[116:119], v0, s[40:43], s94 offen offset:1024\n"
"buffer_load_dwordx4 v[120:123], v0, s[40:43], s94 offen offset:2048\n"
"buffer_load_dwordx4 v[124:127], v0, s[40:43], s94 offen offset:3072\n"
"L_ks_0:\n"
"s_waitcnt vmcnt(22)\n"
"v_pk_add_f32 v[168:169], v[32:33], v[36:37]\n"
"v_pk_add_f32 v[170:171], v[34:35], v[38:39]\n"
"s_waitcnt vmcnt(21)\n"
"v_pk_add_f32 v[168:169], v[168:169], v[40:41]\n"
"v_pk_add_f32 v[170:171], v[170:171], v[42:43]\n"
"s_waitcnt vmcnt(20)\n"
"v_pk_add_f32 v[168:169], v[168:169], v[44:45]\n"
"v_pk_add_f32 v[170:171], v[170:171], v[46:47]\n"
"s_waitcnt vmcnt(19)\n"
"v_pk_add_f32 v[168:169], v[168:169], v[48:49]\n"
"v_pk_add_f32 v[170:171], v[170:171], v[50:51]\n"
"s_waitcnt vmcnt(18)\n"
"v_pk_add_f32 v[168:169], v[168:169], v[52:53]\n"
"v_pk_add_f32 v[170:171], v[170:171], v[54:55]\n"
"s_waitcnt vmcnt(17)\n"
"v_pk_add_f32 v[168:169], v[168:169], v[56:57]\n"
"v_pk_add_f32 v[170:171], v[170:171], v[58:59]\n"
"s_waitcnt vmcnt(16)\n"
"v_pk_add_f32 v[168:169], v[168:169], v[60:61]\n"
"v_pk_add_f32 v[170:171], v[170:171], v[62:63]\n"
"s_waitcnt vmcnt(15)\n"
"v_pk_add_f32 v[168:169], v[168:169], v[64:65]\n"
"v_pk_add_f32 v[170:171], v[170:171], v[66:67]\n"
"s_waitcnt vmcnt(14)\n"
"v_pk_add_f32 v[168:169], v[168:169], v[68:69]\n"
"v_pk_add_f32 v[170:171], v[170:171], v[70:71]\n"
"s_waitcnt vmcnt(13)\n"
"v_pk_add_f32 v[168:169], v[168:169], v[72:73]\n"
"v_pk_add_f32 v[170:171], v[170:171], v[74:75]\n"
"s_waitcnt vmcnt(12)\n"
"v_pk_add_f32 v[168:169], v[168:169], v[76:77]\n"
"v_pk_add_f32 v[170:171], v[170:171], v[78:79]\n"
"s_waitcnt vmcnt(11)\n"
"v_pk_add_f32 v[168:169], v[168:169], v[80:81]\n"
"v_pk_add_f32 v[170:171], v[170:171], v[82:83]\n"
"s_waitcnt vmcnt(10)\n"
"v_pk_add_f32 v[168:169], v[168:169], v[84:85]\n"
"v_pk_add_f32 v[170:171], v[170:171], v[86:87]\n"
"s_waitcnt vmcnt(9)\n"
"v_pk_add_f32 v[168:169], v[168:169], v[88:89]\n"
"v_pk_add_f32 v[170:171], v[170:171], v[90:91]\n"
"s_waitcnt vmcnt(8)\n"
"v_pk_add_f32 v[168:169], v[168:169], v[92:93]\n"
"v_pk_add_f32 v[170:171], v[170:171], v[94:95]\n"
"v_add_f32 v240, v168, v169\n"
"v_add_f32 v241, v170, v171\n"
"v_cvt_pk_bf16_f32 v242, v168, v169\n"
"v_cvt_pk_bf16_f32 v243, v170, v171\n"
"v_cmp_u_f32 vcc, v240, v241\n"
"v_add_u32 v244, s74, v0\n"
"s_cbranch_vccnz L_kn_s_0\n"
"L_kv_go_0:\n"
"ds_write_b64 v244, v[242:243]\n"
"s_waitcnt lgkmcnt(0)\n"
"s_barrier\n"
"ds_read_b128 v[160:163], v2 offset:17920\n"
"ds_read_b128 v[164:167], v2 offset:18944\n"
"s_add_u32 m0, s87, 0\n"
"s_add_u32 s95, s68, 0\n"
"buffer_load_dwordx4 v0, s[40:43], s95 offen lds\n"
"buffer_load_dwordx4 v0, s[40:43], s95 offen offset:1024 lds\n"
"s_waitcnt lgkmcnt(1)\n"
"v_mfma_f32_32x32x16_bf16 v[144:159], v[160:163], v[176:179], 0\n"
"s_waitcnt lgkmcnt(0)\n"
"v_mfma_f32_32x32x16_bf16 v[144:159], v[164:167], v[180:183], v[144:159]\n"
"buffer_load_dwordx4 v0, s[40:43], s95 offen offset:2048 lds\n"
"buffer_load_dwordx4 v0, s[40:43], s95 offen offset:3072 lds\n"
"s_nop 9\n"
"v_mov_b32 v240, v156\n"
"v_mov_b32 v241, v156\n"
"s_nop 1\n"
"v_permlane32_swap_b32 v240, v241\n"
"v_add_f32 v241, s58, v241\n"
"v_rcp_f32 v240, v241\n"
"v_rcp_f32 v241, v241\n"
"s_nop 0\n"
"v_pk_mul_f32 v[144:145], v[144:145], v[240:241]\n"
"v_pk_mul_f32 v[146:147], v[146:147], v[240:241]\n"
"v_pk_mul_f32 v[148:149], v[148:149], v[240:241]\n"
"v_pk_mul_f32 v[150:151], v[150:151], v[240:241]\n"
"v_pk_mul_f32 v[152:153], v[152:153], v[240:241]\n"
"v_pk_mul_f32 v[154:155], v[154:155], v[240:241]\n"
"v_pk_mul_f32 v[156:157], v[156:157], v[240:241]\n"
"v_pk_mul_f32 v[158:159], v[158:159], v[240:241]\n"
"v_cvt_pk_bf16_f32 v144, v144, v145\n"
"v_cvt_pk_bf16_f32 v145, v146, v147\n"
"v_cvt_pk_bf16_f32 v146, v148, v149\n"
"v_cvt_pk_bf16_f32 v147, v150, v151\n"
"v_min_u32 v144, -2, v144\n"
"v_min_u32 v145, -2, v145\n"
"v_min_u32 v146, -2, v146\n"
"v_min_u32 v147, -2, v147\n"
"buffer_store_dwordx4 v[144:147], v0, s[44:47], s75 offen sc1\n"
"v_cvt_pk_bf16_f32 v148, v152, v153\n"
"v_cvt_pk_bf16_f32 v149, v154, v155\n"
"v_cvt_pk_bf16_f32 v150, v156, v157\n"
"v_cvt_pk_bf16_f32 v151, v158, v159\n"
"v_min_u32 v148, -2, v148\n"
"v_min_u32 v149, -2, v149\n"
"v_min_u32 v150, -2, v150\n"
"v_min_u32 v151, -2, v151\n"
"buffer_store_dwordx4 v[148:151], v0, s[44:47], s75 offen offset:1024 sc1\n"
"s_add_u32 m0, s87, 4096\n"
"s_add_u32 s95, s68, 4096\n"
"buffer_load_dwordx4 v0, s[40:43], s95 offen lds\n"
"buffer_load_dwordx4 v0, s[40:43], s95 offen offset:1024 lds\n"
"buffer_load_dwordx4 v0, s[40:43], s95 offen offset:2048 lds\n"
"buffer_load_dwordx4 v0, s[40:43], s95 offen offset:3072 lds\n"
"s_add_u32 m0, s87, 8192\n"
"s_add_u32 s95, s68, 8192\n"
"buffer_load_dwordx4 v0, s[40:43], s95 offen lds\n"
"buffer_load_dwordx4 v0, s[40:43], s95 offen offset:1024 lds\n"
"buffer_load_dwordx4 v0, s[40:43], s95 offen offset:2048 lds\n"
"buffer_load_dwordx4 v0, s[40:43], s95 offen offset:3072 lds\n"
"s_cmp_eq_u32 %[act], 0\n"
"s_cbranch_scc1 L_wo_skip_0\n"
"v_lshlrev_b32 v160, 16, v8\n"
"v_and_b32 v161, 0xffff0000, v8\n"
"v_lshlrev_b32 v162, 16, v9\n"
"v_and_b32 v163, 0xffff0000, v9\n"
"v_lshlrev_b32 v164, 16, v10\n"
"v_and_b32 v165, 0xffff0000, v10\n"
"v_lshlrev_b32 v166, 16, v11\n"
"v_and_b32 v167, 0xffff0000, v11\n"
"v_lshlrev_b32 v168, 16, v12\n"
"v_and_b32 v169, 0xffff0000, v12\n"
"v_lshlrev_b32 v170, 16, v13\n"
"v_and_b32 v171, 0xffff0000, v13\n"
"v_lshlrev_b32 v172, 16, v14\n"
"v_and_b32 v173, 0xffff0000, v14\n"
"v_lshlrev_b32 v174, 16, v15\n"
"v_and_b32 v175, 0xffff0000, v15\n"
"v_mov_b32 v128, -1\n"
"v_mov_b32 v129, -1\n"
"v_mov_b32 v130, -1\n"
"v_mov_b32 v131, -1\n"
"s_add_u32 s94, s78, 14614528\n"
"buffer_store_dwordx4 v[128:131], v0, s[44:47], s94 offen sc1\n"
"s_add_u32 s94, s78, 14614528\n"
"buffer_store_dwordx4 v[128:131], v0, s[44:47], s94 offen offset:1024 sc1\n"
"s_nop 1\n"
"v_mov_b32 v131, 0x40000000\n"
"v_cndmask_b32_e64 v131, v131, v4, s[62:63]\n"
"s_add_u32 s94, s79, 16449536\n"
"buffer_store_dwordx2 v[128:129], v131, s[44:47], s94 offen sc1\n"
"s_sleep 6\n"
"L_op_0:\n"
"buffer_load_dword v19, v29, s[44:47], 0 offen sc1\n"
"s_waitcnt vmcnt(0)\n"
"v_cmp_eq_u32 vcc, -1, v19\n"
"s_cbranch_vccnz L_op_s_0\n"
"L_op_go_0:\n"
"v_mov_b32 v19, 0\n"
"buffer_load_dwordx4 v[64:67], v0, s[44:47], s77 offen sc1\n"
"buffer_load_dwordx4 v[68:71], v0, s[44:47], s77 offen offset:1024 sc1\n"
"buffer_load_dwordx4 v[72:75], v0, s[44:47], s77 offen offset:2048 sc1\n"
"buffer_load_dwordx4 v[76:79], v0, s[44:47], s77 offen offset:3072 sc1\n"
"s_add_u32 s94, s77, 4096\n"
"buffer_load_dwordx4 v[80:83], v0, s[44:47], s94 offen sc1\n"
"buffer_load_dwordx4 v[84:87], v0, s[44:47], s94 offen offset:1024 sc1\n"
"buffer_load_dwordx4 v[88:91], v0, s[44:47], s94 offen offset:2048 sc1\n"
"buffer_load_dwordx4 v[92:95], v0, s[44:47], s94 offen offset:3072 sc1\n"
"buffer_load_dwordx4 v[32:35], v0, s[44:47], s76 offen sc1\n"
"buffer_load_dwordx4 v[36:39], v0, s[44:47], s76 offen offset:1024 sc1\n"
"buffer_load_dwordx4 v[40:43], v0, s[44:47], s76 offen offset:2048 sc1\n"
"buffer_load_dwordx4 v[44:47], v0, s[44:47], s76 offen offset:3072 sc1\n"
"s_waitcnt vmcnt(11)\n"
"v_mfma_f32_32x32x16_bf16 v[208:223], a[224:227], v[64:67], 0\n"
"v_max3_u32 v19, v19, v64, v65\n"
"s_add_u32 s94, s76, 4096\n"
"buffer_load_dwordx4 v[48:51], v0, s[44:47], s94 offen sc1\n"
"v_mfma_f32_32x32x16_bf16 v[176:191], v[96:99], v[64:67], 0\n"
"v_max3_u32 v19, v19, v66, v67\n"
"s_waitcnt vmcnt(11)\n"
"v_mfma_f32_32x32x16_bf16 v[208:223], a[228:231], v[68:71], v[208:223]\n"
"v_max3_u32 v19, v19, v68, v69\n"
"buffer_load_dwordx4 v[52:55], v0, s[44:47], s94 offen offset:1024 sc1\n"
"v_mfma_f32_32x32x16_bf16 v[176:191], v[100:103], v[68:71], v[176:191]\n"
"v_max3_u32 v19, v19, v70, v71\n"
"s_waitcnt vmcnt(11)\n"
"v_mfma_f32_32x32x16_bf16 v[208:223], a[232:235], v[72:75], v[208:223]\n"
"v_max3_u32 v19, v19, v72, v73\n"
"buffer_load_dwordx4 v[56:59], v0, s[44:47], s94 offen offset:2048 sc1\n"
"v_mfma_f32_32x32x16_bf16 v[176:191], v[104:107], v[72:75], v[176:191]\n"
"v_max3_u32 v19, v19, v74, v75\n"
"buffer_load_dwordx4 v[60:63], v0, s[44:47], s94 offen offset:3072 sc1\n"
"s_waitcnt vmcnt(12)\n"
"v_mfma_f32_32x32x16_bf16 v[208:223], a[236:239], v[76:79], v[208:223]\n"
"v_max3_u32 v19, v19, v76, v77\n"
"v_mfma_f32_32x32x16_bf16 v[176:191], v[108:111], v[76:79], v[176:191]\n"
"v_max3_u32 v19, v19, v78, v79\n"
"s_add_u32 m0, s87, 12288\n"
"s_add_u32 s95, s68, 12288\n"
"buffer_load_dwordx4 v0, s[40:43], s95 offen lds\n"
"s_waitcnt vmcnt(12)\n"
"v_mfma_f32_32x32x16_bf16 v[208:223], a[240:243], v[80:83], v[208:223]\n"
"v_max3_u32 v19, v19, v80, v81\n"
"v_mfma_f32_32x32x16_bf16 v[176:191], v[112:115], v[80:83], v[176:191]\n"
"v_max3_u32 v19, v19, v82, v83\n"
"buffer_load_dwordx4 v0, s[40:43], s95 offen offset:1024 lds\n"
"s_waitcnt vmcnt(12)\n"
"v_mfma_f32_32x32x16_bf16 v[208:223], a[244:247], v[84:87], v[208:223]\n"
"v_max3_u32 v19, v19, v84, v85\n"
"buffer_load_dwordx4 v0, s[40:43], s95 offen offset:2048 lds\n"
"v_mfma_f32_32x32x16_bf16 v[176:191], v[116:119], v[84:87], v[176:191]\n"
"v_max3_u32 v19, v19, v86, v87\n"
"s_waitcnt vmcnt(12)\n"
"v_mfma_f32_32x32x16_bf16 v[208:223], a[248:251], v[88:91], v[208:223]\n"
"v_max3_u32 v19, v19, v88, v89\n"
"buffer_load_dwordx4 v0, s[40:43], s95 offen offset:3072 lds\n"
"v_mfma_f32_32x32x16_bf16 v[176:191], v[120:123], v[88:91], v[176:191]\n"
"v_max3_u32 v19, v19, v90, v91\n"
"s_waitcnt vmcnt(12)\n"
"v_mfma_f32_32x32x16_bf16 v[208:223], a[252:255], v[92:95], v[208:223]\n"
"v_max3_u32 v19, v19, v92, v93\n"
"s_add_u32 m0, s87, 16384\n"
"s_add_u32 s95, s68, 16384\n"
"buffer_load_dwordx4 v0, s[40:43], s95 offen lds\n"
"v_mfma_f32_32x32x16_bf16 v[176:191], v[124:127], v[92:95], v[176:191]\n"
"v_max3_u32 v19, v19, v94, v95\n"
"buffer_load_dwordx4 v0, s[40:43], s95 offen offset:1024 lds\n"
"s_waitcnt vmcnt(13)\n"
"v_mfma_f32_32x32x16_bf16 v[192:207], a[224:227], v[32:35], 0\n"
"v_max3_u32 v19, v19, v32, v33\n"
"v_max3_u32 v19, v19, v34, v35\n"
"s_waitcnt vmcnt(12)\n"
"v_mfma_f32_32x32x16_bf16 v[192:207], a[228:231], v[36:39], v[192:207]\n"
"v_max3_u32 v19, v19, v36, v37\n"
"v_max3_u32 v19, v19, v38, v39\n"
"buffer_load_dwordx4 v0, s[40:43], s95 offen offset:2048 lds\n"
"s_nop 0\n"
"ds_write_b128 v6, v[176:179]\n"
"s_waitcnt vmcnt(12)\n"
"v_mfma_f32_32x32x16_bf16 v[192:207], a[232:235], v[40:43], v[192:207]\n"
"v_max3_u32 v19, v19, v40, v41\n"
"v_max3_u32 v19, v19, v42, v43\n"
"ds_write_b128 v6, v[180:183] offset:1024\n"
"s_waitcnt vmcnt(11)\n"
"v_mfma_f32_32x32x16_bf16 v[192:207], a[236:239], v[44:47], v[192:207]\n"
"v_max3_u32 v19, v19, v44, v45\n"
"v_max3_u32 v19, v19, v46, v47\n"
"buffer_load_dwordx4 v0, s[40:43], s95 offen offset:3072 lds\n"
"ds_write_b128 v6, v[184:187] offset:2048\n"
"s_waitcnt vmcnt(11)\n"
"v_mfma_f32_32x32x16_bf16 v[192:207], a[240:243], v[48:51], v[192:207]\n"
"v_max3_u32 v19, v19, v48, v49\n"
"v_max3_u32 v19, v19, v50, v51\n"
"s_add_u32 m0, s87, 20480\n"
"s_add_u32 s95, s68, 20480\n"
"buffer_load_dwordx4 v0, s[40:43], s95 offen lds\n"
"s_waitcnt vmcnt(11)\n"
"v_mfma_f32_32x32x16_bf16 v[192:207], a[244:247], v[52:55], v[192:207]\n"
"v_max3_u32 v19, v19, v52, v53\n"
"v_max3_u32 v19, v19, v54, v55\n"
"ds_write_b128 v6, v[188:191] offset:3072\n"
"s_waitcnt vmcnt(10)\n"
"v_mfma_f32_32x32x16_bf16 v[192:207], a[248:251], v[56:59], v[192:207]\n"
"v_max3_u32 v19, v19, v56, v57\n"
"v_max3_u32 v19, v19, v58, v59\n"
"buffer_load_dwordx4 v0, s[40:43], s95 offen offset:1024 lds\n"
"ds_write_b128 v24, v[208:211]\n"
"s_waitcnt vmcnt(10)\n"
"v_mfma_f32_32x32x16_bf16 v[192:207], a[252:255], v[60:63], v[192:207]\n"
"v_max3_u32 v19, v19, v60, v61\n"
"v_max3_u32 v19, v19, v62, v63\n"
"ds_write_b128 v24, v[212:215] offset:1024\n"
"buffer_load_dwordx4 a[0:3], v0, s[40:43], s69 offen\n"
"buffer_load_dwordx4 a[4:7], v0, s[40:43], s70 offen\n"
"ds_write_b128 v24, v[216:219] offset:2048\n"
"ds_write_b128 v24, v[220:223] offset:3072\n"
"v_cmp_eq_u32 vcc, -1, v19\n"
"s_cbranch_vccnz L_ov_s_0\n"
"L_ov_go_0:\n"
"v_mfma_f32_32x32x16_bf16 v[160:175], v[96:99], v[32:35], v[160:175]\n"
"v_mfma_f32_32x32x16_bf16 v[160:175], v[100:103], v[36:39], v[160:175]\n"
"s_nop 0\n"
"ds_write_b128 v7, v[192:195]\n"
"ds_write_b128 v7, v[196:199] offset:1024\n"
"ds_write_b128 v7, v[200:203] offset:2048\n"
"ds_write_b128 v7, v[204:207] offset:3072\n"
"s_waitcnt lgkmcnt(0)\n"
"s_barrier\n"
"ds_read_b128 v[64:67], v5\n"
"ds_read_b128 v[80:83], v5 offset:4096\n"
"ds_read_b128 v[128:131], v5 offset:8192\n"
"ds_read_b128 v[68:71], v5 offset:1024\n"
"ds_read_b128 v[84:87], v5 offset:5120\n"
"ds_read_b128 v[132:135], v5 offset:9216\n"
"ds_read_b128 v[72:75], v5 offset:2048\n"
"ds_read_b128 v[88:91], v5 offset:6144\n"
"ds_read_b128 v[136:139], v5 offset:10240\n"
"ds_read_b128 v[76:79], v5 offset:3072\n"
"ds_read_b128 v[92:95], v5 offset:7168\n"
"ds_read_b128 v[140:143], v5 offset:11264\n"
"v_mfma_f32_32x32x16_bf16 v[160:175], v[104:107], v[40:43], v[160:175]\n"
"v_mfma_f32_32x32x16_bf16 v[160:175], v[108:111], v[44:47], v[160:175]\n"
"v_mfma_f32_32x32x16_bf16 v[160:175], v[112:115], v[48:51], v[160:175]\n"
"s_waitcnt lgkmcnt(10)\n"
"v_add_f32 v64, v64, v80\n"
"v_add_f32 v65, v65, v81\n"
"v_add_f32 v66, v66, v82\n"
"v_add_f32 v67, v67, v83\n"
"s_waitcnt lgkmcnt(9)\n"
"v_add_f32 v64, v64, v128\n"
"v_add_f32 v65, v65, v129\n"
"v_add_f32 v66, v66, v130\n"
"v_add_f32 v67, v67, v131\n"
"s_waitcnt lgkmcnt(7)\n"
"v_add_f32 v68, v68, v84\n"
"v_mfma_f32_32x32x16_bf16 v[160:175], v[116:119], v[52:55], v[160:175]\n"
"v_add_f32 v69, v69, v85\n"
"v_add_f32 v70, v70, v86\n"
"v_add_f32 v71, v71, v87\n"
"s_waitcnt lgkmcnt(6)\n"
"v_add_f32 v68, v68, v132\n"
"v_add_f32 v69, v69, v133\n"
"v_add_f32 v70, v70, v134\n"
"v_add_f32 v71, v71, v135\n"
"s_waitcnt lgkmcnt(4)\n"
"v_add_f32 v72, v72, v88\n"
"v_mfma_f32_32x32x16_bf16 v[160:175], v[120:123], v[56:59], v[160:175]\n"
"v_add_f32 v73, v73, v89\n"
"v_add_f32 v74, v74, v90\n"
"v_add_f32 v75, v75, v91\n"
"s_waitcnt lgkmcnt(3)\n"
"v_add_f32 v72, v72, v136\n"
"v_add_f32 v73, v73, v137\n"
"v_add_f32 v74, v74, v138\n"
"v_add_f32 v75, v75, v139\n"
"s_waitcnt lgkmcnt(1)\n"
"v_add_f32 v76, v76, v92\n"
"v_mfma_f32_32x32x16_bf16 v[160:175], v[124:127], v[60:63], v[160:175]\n"
"v_add_f32 v77, v77, v93\n"
"v_add_f32 v78, v78, v94\n"
"v_add_f32 v79, v79, v95\n"
"s_waitcnt lgkmcnt(0)\n"
"v_add_f32 v76, v76, v140\n"
"v_add_f32 v77, v77, v141\n"
"v_add_f32 v78, v78, v142\n"
"v_add_f32 v79, v79, v143\n"
"s_nop 3\n"
"v_pk_add_f32 v[160:161], v[160:161], v[64:65]\n"
"v_pk_add_f32 v[162:163], v[162:163], v[66:67]\n"
"v_cvt_pk_bf16_f32 v8, v160, v161\n"
"v_cvt_pk_bf16_f32 v9, v162, v163\n"
"v_min_u32 v8, -2, v8\n"
"v_min_u32 v9, -2, v9\n"
"s_add_u32 s60, s78, 2064384\n"
"v_pk_add_f32 v[164:165], v[164:165], v[68:69]\n"
"v_pk_add_f32 v[166:167], v[166:167], v[70:71]\n"
"v_cvt_pk_bf16_f32 v10, v164, v165\n"
"v_cvt_pk_bf16_f32 v11, v166, v167\n"
"v_min_u32 v10, -2, v10\n"
"v_min_u32 v11, -2, v11\n"
"buffer_store_dwordx4 v[8:11], v0, s[44:47], s60 offen sc1\n"
"v_pk_add_f32 v[168:169], v[168:169], v[72:73]\n"
"v_pk_add_f32 v[170:171], v[170:171], v[74:75]\n"
"v_cvt_pk_bf16_f32 v12, v168, v169\n"
"v_cvt_pk_bf16_f32 v13, v170, v171\n"
"v_min_u32 v12, -2, v12\n"
"v_min_u32 v13, -2, v13\n"
"v_pk_add_f32 v[172:173], v[172:173], v[76:77]\n"
"v_pk_add_f32 v[174:175], v[174:175], v[78:79]\n"
"v_cvt_pk_bf16_f32 v14, v172, v173\n"
"v_cvt_pk_bf16_f32 v15, v174, v175\n"
"v_min_u32 v14, -2, v14\n"
"v_min_u32 v15, -2, v15\n"
"buffer_store_dwordx4 v[12:15], v0, s[44:47], s60 offen offset:1024 sc1\n"
"v_mov_b32 v240, 0\n"
"v_mov_b32 v241, 0\n"
"v_dot2c_f32_bf16 v240, 0x3f803f80, v8\n"
"v_dot2c_f32_bf16 v241, v8, v8\n"
"v_dot2c_f32_bf16 v240, 0x3f803f80, v9\n"
"v_dot2c_f32_bf16 v241, v9, v9\n"
"v_dot2c_f32_bf16 v240, 0x3f803f80, v10\n"
"v_dot2c_f32_bf16 v241, v10, v10\n"
"v_dot2c_f32_bf16 v240, 0x3f803f80, v11\n"
"v_dot2c_f32_bf16 v241, v11, v11\n"
"v_dot2c_f32_bf16 v240, 0x3f803f80, v12\n"
"v_dot2c_f32_bf16 v241, v12, v12\n"
"v_dot2c_f32_bf16 v240, 0x3f803f80, v13\n"
"v_dot2c_f32_bf16 v241, v13, v13\n"
"v_dot2c_f32_bf16 v240, 0x3f803f80, v14\n"
"v_dot2c_f32_bf16 v241, v14, v14\n"
"v_dot2c_f32_bf16 v240, 0x3f803f80, v15\n"
"v_dot2c_f32_bf16 v241, v15, v15\n"
"s_nop 1\n"
"v_mov_b32 v242, v240\n"
"v_mov_b32 v243, v241\n"
"s_nop 0\n"
"v_permlane32_swap_b32 v240, v242\n"
"v_permlane32_swap_b32 v241, v243\n"
"v_add_f32 v240, v240, v242\n"
"v_add_f32 v241, v241, v243\n"
"v_min_u32 v240, -2, v240\n"
"v_min_u32 v241, -2, v241\n"
"s_add_u32 s60, s79, 3899392\n"
"v_mov_b32 v242, 0x40000000\n"
"v_cndmask_b32_e64 v242, v242, v4, s[62:63]\n"
"buffer_store_dwordx2 v[240:241], v242, s[44:47], s60 offen sc1\n"
"s_add_u32 s94, s69, 4096\n"
"buffer_load_dwordx4 a[8:11], v0, s[40:43], s94 offen offset:3072\n"
"s_add_u32 s94, s70, 4096\n"
"buffer_load_dwordx4 a[12:15], v0, s[40:43], s94 offen offset:3072\n"
"s_add_u32 s94, s69, 12288\n"
"buffer_load_dwordx4 a[16:19], v0, s[40:43], s94 offen offset:2048\n"
"s_add_u32 s94, s70, 12288\n"
"buffer_load_dwordx4 a[20:23], v0, s[40:43], s94 offen offset:2048\n"
"s_add_u32 s94, s69, 20480\n"
"buffer_load_dwordx4 a[24:27], v0, s[40:43], s94 offen offset:1024\n"
"s_add_u32 s94, s70, 20480\n"
"buffer_load_dwordx4 a[28:31], v0, s[40:43], s94 offen offset:1024\n"
"buffer_load_dwordx4 a[32:35], v0, s[40:43], s69 offen offset:1024\n"
"buffer_load_dwordx4 a[36:39], v0, s[40:43], s70 offen offset:1024\n"
"s_add_u32 s94, s69, 8192\n"
"buffer_load_dwordx4 a[40:43], v0, s[40:43], s94 offen\n"
"s_add_u32 s94, s70, 8192\n"
"buffer_load_dwordx4 a[44:47], v0, s[40:43], s94 offen\n"
"s_add_u32 s94, s69, 12288\n"
"buffer_load_dwordx4 a[48:51], v0, s[40:43], s94 offen offset:3072\n"
"s_add_u32 s94, s70, 12288\n"
"buffer_load_dwordx4 a[52:55], v0, s[40:43], s94 offen offset:3072\n"
"s_add_u32 s94, s69, 20480\n"
"buffer_load_dwordx4 a[56:59], v0, s[40:43], s94 offen offset:2048\n"
"s_add_u32 s94, s70, 20480\n"
"buffer_load_dwordx4 a[60:63], v0, s[40:43], s94 offen offset:2048\n"
"buffer_load_dwordx4 a[64:67], v0, s[40:43], s69 offen offset:2048\n"
"buffer_load_dwordx4 a[68:71], v0, s[40:43], s70 offen offset:2048\n"
"s_add_u32 s94, s69, 8192\n"
"buffer_load_dwordx4 a[72:75], v0, s[40:43], s94 offen offset:1024\n"
"s_add_u32 s94, s70, 8192\n"
"buffer_load_dwordx4 a[76:79], v0, s[40:43], s94 offen offset:1024\n"
"s_add_u32 s94, s69, 16384\n"
"buffer_load_dwordx4 a[80:83], v0, s[40:43], s94 offen\n"
"s_add_u32 s94, s70, 16384\n"
"buffer_load_dwordx4 a[84:87], v0, s[40:43], s94 offen\n"
"s_add_u32 s94, s69, 20480\n"
"buffer_load_dwordx4 a[88:91], v0, s[40:43], s94 offen offset:3072\n"
"s_add_u32 s94, s70, 20480\n"
"buffer_load_dwordx4 a[92:95], v0, s[40:43], s94 offen offset:3072\n"
"s_branch L_wo_done_0\n"
"L_wo_skip_0:\n"
"s_add_u32 m0, s87, 12288\n"
"s_add_u32 s95, s68, 12288\n"
"buffer_load_dwordx4 v0, s[40:43], s95 offen lds\n"
"buffer_load_dwordx4 v0, s[40:43], s95 offen offset:1024 lds\n"
"buffer_load_dwordx4 v0, s[40:43], s95 offen offset:2048 lds\n"
"buffer_load_dwordx4 v0, s[40:43], s95 offen offset:3072 lds\n"
"s_add_u32 m0, s87, 16384\n"
"s_add_u32 s95, s68, 16384\n"
"buffer_load_dwordx4 v0, s[40:43], s95 offen lds\n"
"buffer_load_dwordx4 v0, s[40:43], s95 offen offset:1024 lds\n"
"buffer_load_dwordx4 v0, s[40:43], s95 offen offset:2048 lds\n"
"buffer_load_dwordx4 v0, s[40:43], s95 offen offset:3072 lds\n"
"s_add_u32 m0, s87, 20480\n"
"s_add_u32 s95, s68, 20480\n"
"buffer_load_dwordx4 v0, s[40:43], s95 offen lds\n"
"buffer_load_dwordx4 v0, s[40:43], s95 offen offset:1024 lds\n"
"buffer_load_dwordx4 a[0:3], v0, s[40:43], s69 offen\n"
"buffer_load_dwordx4 a[4:7], v0, s[40:43], s70 offen\n"
"s_add_u32 s94, s69, 4096\n"
"buffer_load_dwordx4 a[8:11], v0, s[40:43], s94 offen offset:3072\n"
"s_add_u32 s94, s70, 4096\n"
"buffer_load_dwordx4 a[12:15], v0, s[40:43], s94 offen offset:3072\n"
"s_add_u32 s94, s69, 12288\n"
"buffer_load_dwordx4 a[16:19], v0, s[40:43], s94 offen offset:2048\n"
"s_add_u32 s94, s70, 12288\n"
"buffer_load_dwordx4 a[20:23], v0, s[40:43], s94 offen offset:2048\n"
"s_add_u32 s94, s69, 20480\n"
"buffer_load_dwordx4 a[24:27], v0, s[40:43], s94 offen offset:1024\n"
"s_add_u32 s94, s70, 20480\n"
"buffer_load_dwordx4 a[28:31], v0, s[40:43], s94 offen offset:1024\n"
"buffer_load_dwordx4 a[32:35], v0, s[40:43], s69 offen offset:1024\n"
"buffer_load_dwordx4 a[36:39], v0, s[40:43], s70 offen offset:1024\n"
"s_add_u32 s94, s69, 8192\n"
"buffer_load_dwordx4 a[40:43], v0, s[40:43], s94 offen\n"
"s_add_u32 s94, s70, 8192\n"
"buffer_load_dwordx4 a[44:47], v0, s[40:43], s94 offen\n"
"s_add_u32 s94, s69, 12288\n"
"buffer_load_dwordx4 a[48:51], v0, s[40:43], s94 offen offset:3072\n"
"s_add_u32 s94, s70, 12288\n"
"buffer_load_dwordx4 a[52:55], v0, s[40:43], s94 offen offset:3072\n"
"s_add_u32 s94, s69, 20480\n"
"buffer_load_dwordx4 a[56:59], v0, s[40:43], s94 offen offset:2048\n"
"s_add_u32 s94, s70, 20480\n"
"buffer_load_dwordx4 a[60:63], v0, s[40:43], s94 offen offset:2048\n"
"buffer_load_dwordx4 a[64:67], v0, s[40:43], s69 offen offset:2048\n"
"buffer_load_dwordx4 a[68:71], v0, s[40:43], s70 offen offset:2048\n"
"s_add_u32 s94, s69, 8192\n"
"buffer_load_dwordx4 a[72:75], v0, s[40:43], s94 offen offset:1024\n"
"s_add_u32 s94, s70, 8192\n"
"buffer_load_dwordx4 a[76:79], v0, s[40:43], s94 offen offset:1024\n"
"s_add_u32 s94, s69, 16384\n"
"buffer_load_dwordx4 a[80:83], v0, s[40:43], s94 offen\n"
"s_add_u32 s94, s70, 16384\n"
"buffer_load_dwordx4 a[84:87], v0, s[40:43], s94 offen\n"
"s_add_u32 s94, s69, 20480\n"
"buffer_load_dwordx4 a[88:91], v0, s[40:43], s94 offen offset:3072\n"
"s_add_u32 s94, s70, 20480\n"
"buffer_load_dwordx4 a[92:95], v0, s[40:43], s94 offen offset:3072\n"
"L_ip_0:\n"
"buffer_load_dword v19, v29, s[44:47], 0 offen sc1\n"
"s_waitcnt vmcnt(0)\n"
"v_cmp_eq_u32 vcc, -1, v19\n"
"s_cbranch_vccnz L_ip_s_0\n"
"L_ip_go_0:\n"
"s_barrier\n"
"v_mov_b32 v32, -1\n"
"v_mov_b32 v33, -1\n"
"v_mov_b32 v34, -1\n"
"v_mov_b32 v35, -1\n"
"s_and_b32 s59, %[blk], 7\n"
"s_lshl_b32 s59, s59, 4\n"
"s_and_b32 s60, %[slw], 15\n"
"s_add_u32 s60, s60, s59\n"
"s_mul_i32 s94, s60, 57344\n"
"s_add_u32 s94, s94, 21889024\n"
"s_mul_i32 s95, s60, 16384\n"
"s_min_u32 s95, s95, 2048000\n"
"s_add_u32 s95, s95, 16678912\n"
"buffer_store_dwordx4 v[32:35], v0, s[44:47], s94 offen sc1\n"
"buffer_store_dwordx4 v[32:35], v0, s[44:47], s94 offen offset:1024 sc1\n"
"buffer_store_dwordx4 v[32:35], v0, s[44:47], s94 offen offset:2048 sc1\n"
"buffer_store_dwordx4 v[32:35], v0, s[44:47], s94 offen offset:3072 sc1\n"
"s_add_u32 s94, s94, 4096\n"
"buffer_store_dwordx4 v[32:35], v0, s[44:47], s94 offen sc1\n"
"buffer_store_dwordx4 v[32:35], v0, s[44:47], s94 offen offset:1024 sc1\n"
"buffer_store_dwordx4 v[32:35], v0, s[44:47], s94 offen offset:2048 sc1\n"
"buffer_store_dwordx4 v[32:35], v0, s[44:47], s94 offen offset:3072 sc1\n"
"s_add_u32 s94, s94, 4096\n"
"buffer_store_dwordx4 v[32:35], v0, s[44:47], s94 offen sc1\n"
"buffer_store_dwordx4 v[32:35], v0, s[44:47], s94 offen offset:1024 sc1\n"
"buffer_store_dwordx4 v[32:35], v0, s[44:47], s94 offen offset:2048 sc1\n"
"buffer_store_dwordx4 v[32:35], v0, s[44:47], s94 offen offset:3072 sc1\n"
"s_add_u32 s94, s94, 4096\n"
"buffer_store_dwordx4 v[32:35], v0, s[44:47], s94 offen sc1\n"
"buffer_store_dwordx4 v[32:35], v0, s[44:47], s94 offen offset:1024 sc1\n"
"buffer_store_dwordx4 v[32:35], v0, s[44:47], s94 offen offset:2048 sc1\n"
"buffer_store_dwordx4 v[32:35], v0, s[44:47], s94 offen offset:3072 sc1\n"
"s_add_u32 s94, s94, 4096\n"
"buffer_store_dwordx4 v[32:35], v0, s[44:47], s94 offen sc1\n"
"buffer_store_dwordx4 v[32:35], v0, s[44:47], s94 offen offset:1024 sc1\n"
"buffer_store_dwordx4 v[32:35], v0, s[44:47], s94 offen offset:2048 sc1\n"
"buffer_store_dwordx4 v[32:35], v0, s[44:47], s94 offen offset:3072 sc1\n"
"s_add_u32 s94, s94, 4096\n"
"buffer_store_dwordx4 v[32:35], v0, s[44:47], s94 offen sc1\n"
"buffer_store_dwordx4 v[32:35], v0, s[44:47], s94 offen offset:1024 sc1\n"
"buffer_store_dwordx4 v[32:35], v0, s[44:47], s94 offen offset:2048 sc1\n"
"buffer_store_dwordx4 v[32:35], v0, s[44:47], s94 offen offset:3072 sc1\n"
"s_add_u32 s94, s94, 4096\n"
"s_add_u32 s94, s94, 4096\n"
"s_add_u32 s94, s94, 4096\n"
"s_add_u32 s94, s94, 4096\n"
"s_add_u32 s94, s94, 4096\n"
"s_add_u32 s94, s94, 4096\n"
"s_add_u32 s94, s94, 4096\n"
"s_add_u32 s94, s94, 4096\n"
"s_add_u32 s95, s95, 4096\n"
"s_add_u32 s95, s95, 4096\n"
"s_add_u32 s95, s95, 4096\n"
"L_wo_done_0:\n"
"s_mov_b32 s56, 0\n"
"s_sleep 4\n"
"L_x1p_0:\n"
"buffer_load_dword v19, v3, s[44:47], s80 offen sc1\n"
"s_waitcnt vmcnt(0)\n"
"v_cmp_eq_u32 vcc, -1, v19\n"
"s_cbranch_vccnz L_x1p_s_0\n"
"L_x1p_go_0:\n"
"v_mov_b32 v19, 0\n"
"buffer_load_dwordx4 v[32:35], v0, s[44:47], s80 offen sc1\n"
"buffer_load_dwordx4 v[36:39], v0, s[44:47], s80 offen offset:1024 sc1\n"
"buffer_load_dwordx4 v[40:43], v0, s[44:47], s80 offen offset:2048 sc1\n"
"buffer_load_dwordx4 v[44:47], v0, s[44:47], s80 offen offset:3072 sc1\n"
"s_add_u32 s94, s80, 4096\n"
"buffer_load_dwordx4 v[48:51], v0, s[44:47], s94 offen sc1\n"
"buffer_load_dwordx4 v[52:55], v0, s[44:47], s94 offen offset:1024 sc1\n"
"buffer_load_dwordx4 v[56:59], v0, s[44:47], s94 offen offset:2048 sc1\n"
"buffer_load_dwordx4 v[60:63], v0, s[44:47], s94 offen offset:3072 sc1\n"
"s_add_u32 s94, s80, 8192\n"
"buffer_load_dwordx4 v[64:67], v0, s[44:47], s94 offen sc1\n"
"buffer_load_dwordx4 v[68:71], v0, s[44:47], s94 offen offset:1024 sc1\n"
"buffer_load_dwordx4 v[72:75], v0, s[44:47], s94 offen offset:2048 sc1\n"
"buffer_load_dwordx4 v[76:79], v0, s[44:47], s94 offen offset:3072 sc1\n"
"buffer_load_dwordx2 v[192:193], v4, s[44:47], s81 offen sc1\n"
"buffer_load_dwordx2 v[194:195], v4, s[44:47], s81 offen offset:512 sc1\n"
"buffer_load_dwordx2 v[196:197], v4, s[44:47], s81 offen offset:1024 sc1\n"
"buffer_load_dwordx2 v[198:199], v4, s[44:47], s81 offen offset:1536 sc1\n"
"buffer_load_dwordx2 v[200:201], v4, s[44:47], s81 offen offset:2048 sc1\n"
"buffer_load_dwordx2 v[202:203], v4, s[44:47], s81 offen offset:2560 sc1\n"
"buffer_load_dwordx2 v[204:205], v4, s[44:47], s81 offen offset:3072 sc1\n"
"s_cmp_lg_u32 s56, 0\n"
"s_cbranch_scc1 L_gb_0\n"
"s_barrier\n"
"s_mov_b32 s56, 1\n"
"L_gb_0:\n"
"ds_read_b128 v[208:211], v0\n"
"ds_read_b128 v[212:215], v0 offset:29696\n"
"ds_read_b128 v[216:219], v0 offset:1024\n"
"ds_read_b128 v[220:223], v0 offset:30720\n"
"ds_read_b128 v[224:227], v0 offset:2048\n"
"ds_read_b128 v[228:231], v0 offset:31744\n"
"s_waitcnt vmcnt(18) lgkmcnt(4)\n"
"v_mfma_f32_32x32x16_bf16 v[144:159], v[208:211], v[32:35], 0\n"
"v_max3_u32 v19, v19, v32, v33\n"
"s_add_u32 s94, s80, 12288\n"
"buffer_load_dwordx4 v[80:83], v0, s[44:47], s94 offen sc1\n"
"v_mfma_f32_32x32x16_bf16 v[160:175], v[212:215], v[32:35], 0\n"
"ds_read_b128 v[208:211], v0 offset:3072\n"
"ds_read_b128 v[212:215], v0 offset:32768\n"
"v_max3_u32 v19, v19, v34, v35\n"
"s_waitcnt vmcnt(18) lgkmcnt(4)\n"
"v_mfma_f32_32x32x16_bf16 v[144:159], v[216:219], v[36:39], v[144:159]\n"
"v_max3_u32 v19, v19, v36, v37\n"
"buffer_load_dwordx4 v[84:87], v0, s[44:47], s94 offen offset:1024 sc1\n"
"v_mfma_f32_32x32x16_bf16 v[160:175], v[220:223], v[36:39], v[160:175]\n"
"ds_read_b128 v[216:219], v0 offset:4096\n"
"ds_read_b128 v[220:223], v0 offset:33792\n"
"v_max3_u32 v19, v19, v38, v39\n"
"s_waitcnt vmcnt(18) lgkmcnt(4)\n"
"v_mfma_f32_32x32x16_bf16 v[144:159], v[224:227], v[40:43], v[144:159]\n"
"v_max3_u32 v19, v19, v40, v41\n"
"buffer_load_dwordx4 v[88:91], v0, s[44:47], s94 offen offset:2048 sc1\n"
"v_mfma_f32_32x32x16_bf16 v[160:175], v[228:231], v[40:43], v[160:175]\n"
"ds_read_b128 v[224:227], v0 offset:5120\n"
"ds_read_b128 v[228:231], v0 offset:34816\n"
"v_max3_u32 v19, v19, v42, v43\n"
"s_waitcnt vmcnt(18) lgkmcnt(4)\n"
"v_mfma_f32_32x32x16_bf16 v[144:159], v[208:211], v[44:47], v[144:159]\n"
"v_max3_u32 v19, v19, v44, v45\n"
"buffer_load_dwordx4 v[92:95], v0, s[44:47], s94 offen offset:3072 sc1\n"
"v_mfma_f32_32x32x16_bf16 v[160:175], v[212:215], v[44:47], v[160:175]\n"
"ds_read_b128 v[208:211], v0 offset:6144\n"
"ds_read_b128 v[212:215], v0 offset:35840\n"
"v_max3_u32 v19, v19, v46, v47\n"
"s_waitcnt vmcnt(18) lgkmcnt(4)\n"
"v_mfma_f32_32x32x16_bf16 v[144:159], v[216:219], v[48:51], v[144:159]\n"
"v_max3_u32 v19, v19, v48, v49\n"
"s_add_u32 s94, s80, 16384\n"
"buffer_load_dwordx4 v[96:99], v0, s[44:47], s94 offen sc1\n"
"v_mfma_f32_32x32x16_bf16 v[160:175], v[220:223], v[48:51], v[160:175]\n"
"ds_read_b128 v[216:219], v0 offset:7168\n"
"ds_read_b128 v[220:223], v0 offset:36864\n"
"v_max3_u32 v19, v19, v50, v51\n"
"s_waitcnt vmcnt(18) lgkmcnt(4)\n"
"v_mfma_f32_32x32x16_bf16 v[144:159], v[224:227], v[52:55], v[144:159]\n"
"v_max3_u32 v19, v19, v52, v53\n"
"buffer_load_dwordx4 v[100:103], v0, s[44:47], s94 offen offset:1024 sc1\n"
"v_mfma_f32_32x32x16_bf16 v[160:175], v[228:231], v[52:55], v[160:175]\n"
"ds_read_b128 v[224:227], v0 offset:8192\n"
"ds_read_b128 v[228:231], v0 offset:37888\n"
"v_max3_u32 v19, v19, v54, v55\n"
"s_waitcnt vmcnt(18) lgkmcnt(4)\n"
"v_mfma_f32_32x32x16_bf16 v[144:159], v[208:211], v[56:59], v[144:159]\n"
"v_max3_u32 v19, v19, v56, v57\n"
"buffer_load_dwordx4 v[104:107], v0, s[44:47], s94 offen offset:2048 sc1\n"
"v_mfma_f32_32x32x16_bf16 v[160:175], v[212:215], v[56:59], v[160:175]\n"
"ds_read_b128 v[208:211], v0 offset:9216\n"
"ds_read_b128 v[212:215], v0 offset:38912\n"
"v_max3_u32 v19, v19, v58, v59\n"
"s_waitcnt vmcnt(18) lgkmcnt(4)\n"
"v_mfma_f32_32x32x16_bf16 v[144:159], v[216:219], v[60:63], v[144:159]\n"
"v_max3_u32 v19, v19, v60, v61\n"
"buffer_load_dwordx4 v[108:111], v0, s[44:47], s94 offen offset:3072 sc1\n"
"v_mfma_f32_32x32x16_bf16 v[160:175], v[220:223], v[60:63], v[160:175]\n"
"ds_read_b128 v[216:219], v0 offset:10240\n"
"ds_read_b128 v[220:223], v0 offset:39936\n"
"v_max3_u32 v19, v19, v62, v63\n"
"s_waitcnt vmcnt(18) lgkmcnt(4)\n"
"v_mfma_f32_32x32x16_bf16 v[144:159], v[224:227], v[64:67], v[144:159]\n"
"v_max3_u32 v19, v19, v64, v65\n"
"s_add_u32 s94, s80, 20480\n"
"buffer_load_dwordx4 v[112:115], v0, s[44:47], s94 offen sc1\n"
"v_mfma_f32_32x32x16_bf16 v[160:175], v[228:231], v[64:67], v[160:175]\n"
"ds_read_b128 v[224:227], v0 offset:11264\n"
"ds_read_b128 v[228:231], v0 offset:40960\n"
"v_max3_u32 v19, v19, v66, v67\n"
"s_waitcnt vmcnt(18) lgkmcnt(4)\n"
"v_mfma_f32_32x32x16_bf16 v[144:159], v[208:211], v[68:71], v[144:159]\n"
"v_max3_u32 v19, v19, v68, v69\n"
"buffer_load_dwordx4 v[116:119], v0, s[44:47], s94 offen offset:1024 sc1\n"
"v_mfma_f32_32x32x16_bf16 v[160:175], v[212:215], v[68:71], v[160:175]\n"
"ds_read_b128 v[208:211], v0 offset:12288\n"
"ds_read_b128 v[212:215], v0 offset:41984\n"
"v_max3_u32 v19, v19, v70, v71\n"
"s_waitcnt vmcnt(18) lgkmcnt(4)\n"
"v_mfma_f32_32x32x16_bf16 v[144:159], v[216:219], v[72:75], v[144:159]\n"
"v_max3_u32 v19, v19, v72, v73\n"
"buffer_load_dwordx4 v[120:123], v0, s[44:47], s94 offen offset:2048 sc1\n"
"v_mfma_f32_32x32x16_bf16 v[160:175], v[220:223], v[72:75], v[160:175]\n"
"ds_read_b128 v[216:219], v0 offset:13312\n"
"ds_read_b128 v[220:223], v0 offset:43008\n"
"v_max3_u32 v19, v19, v74, v75\n"
"s_waitcnt vmcnt(18) lgkmcnt(4)\n"
"v_mfma_f32_32x32x16_bf16 v[144:159], v[224:227], v[76:79], v[144:159]\n"
"v_max3_u32 v19, v19, v76, v77\n"
"buffer_load_dwordx4 v[124:127], v0, s[44:47], s94 offen offset:3072 sc1\n"
"v_mfma_f32_32x32x16_bf16 v[160:175], v[228:231], v[76:79], v[160:175]\n"
"ds_read_b128 v[224:227], v0 offset:14336\n"
"ds_read_b128 v[228:231], v0 offset:44032\n"
"v_max3_u32 v19, v19, v78, v79\n"
"s_waitcnt vmcnt(11) lgkmcnt(4)\n"
"v_mfma_f32_32x32x16_bf16 v[144:159], v[208:211], v[80:83], v[144:159]\n"
"v_max3_u32 v19, v19, v80, v81\n"
"s_add_u32 s94, s80, 24576\n"
"buffer_load_dwordx4 v[128:131], v0, s[44:47], s94 offen sc1\n"
"v_mfma_f32_32x32x16_bf16 v[160:175], v[212:215], v[80:83], v[160:175]\n"
"ds_read_b128 v[208:211], v0 offset:15360\n"
"ds_read_b128 v[212:215], v0 offset:45056\n"
"v_max3_u32 v19, v19, v82, v83\n"
"s_waitcnt vmcnt(11) lgkmcnt(4)\n"
"v_mfma_f32_32x32x16_bf16 v[144:159], v[216:219], v[84:87], v[144:159]\n"
"v_max3_u32 v19, v19, v84, v85\n"
"buffer_load_dwordx4 v[132:135], v0, s[44:47], s94 offen offset:1024 sc1\n"
"v_max3_u32 v19, v19, v192, v193\n"
"v_max3_u32 v19, v19, v194, v195\n"
"v_mfma_f32_32x32x16_bf16 v[160:175], v[220:223], v[84:87], v[160:175]\n"
"ds_read_b128 v[216:219], v0 offset:16384\n"
"ds_read_b128 v[220:223], v0 offset:46080\n"
"v_max3_u32 v19, v19, v86, v87\n"
"v_max3_u32 v19, v19, v196, v197\n"
"v_max3_u32 v19, v19, v198, v199\n"
"v_max3_u32 v19, v19, v200, v201\n"
"s_waitcnt vmcnt(11) lgkmcnt(4)\n"
"v_mfma_f32_32x32x16_bf16 v[144:159], v[224:227], v[88:91], v[144:159]\n"
"v_max3_u32 v19, v19, v88, v89\n"
"buffer_load_dwordx4 v[136:139], v0, s[44:47], s94 offen offset:2048 sc1\n"
"v_max3_u32 v19, v19, v202, v203\n"
"v_mfma_f32_32x32x16_bf16 v[160:175], v[228:231], v[88:91], v[160:175]\n"
"ds_read_b128 v[224:227], v0 offset:17408\n"
"ds_read_b128 v[228:231], v0 offset:47104\n"
"v_max3_u32 v19, v19, v90, v91\n"
"v_max3_u32 v19, v19, v204, v205\n"
"v_add_f32 v192, v192, v194\n"
"v_add_f32 v193, v193, v195\n"
"v_add_f32 v196, v196, v198\n"
"s_waitcnt vmcnt(11) lgkmcnt(4)\n"
"v_mfma_f32_32x32x16_bf16 v[144:159], v[208:211], v[92:95], v[144:159]\n"
"v_max3_u32 v19, v19, v92, v93\n"
"buffer_load_dwordx4 v[140:143], v0, s[44:47], s94 offen offset:3072 sc1\n"
"v_add_f32 v197, v197, v199\n"
"v_mfma_f32_32x32x16_bf16 v[160:175], v[212:215], v[92:95], v[160:175]\n"
"ds_read_b128 v[208:211], v0 offset:18432\n"
"ds_read_b128 v[212:215], v0 offset:48128\n"
"v_max3_u32 v19, v19, v94, v95\n"
"v_add_f32 v200, v200, v202\n"
"v_add_f32 v201, v201, v203\n"
"v_add_f32 v192, v192, v196\n"
"s_waitcnt vmcnt(11) lgkmcnt(4)\n"
"v_mfma_f32_32x32x16_bf16 v[144:159], v[216:219], v[96:99], v[144:159]\n"
"v_max3_u32 v19, v19, v96, v97\n"
"s_add_u32 m0, s89, 89088\n"
"s_add_u32 s95, s88, 89088\n"
"buffer_load_dwordx4 v0, s[40:43], s95 offen lds\n"
"v_add_f32 v193, v193, v197\n"
"v_mfma_f32_32x32x16_bf16 v[160:175], v[220:223], v[96:99], v[160:175]\n"
"ds_read_b128 v[216:219], v0 offset:19456\n"
"ds_read_b128 v[220:223], v0 offset:49152\n"
"v_max3_u32 v19, v19, v98, v99\n"
"v_add_f32 v200, v200, v204\n"
"v_add_f32 v201, v201, v205\n"
"v_add_f32 v192, v192, v200\n"
"v_add_f32 v193, v193, v201\n"
"s_waitcnt vmcnt(11) lgkmcnt(4)\n"
"v_mfma_f32_32x32x16_bf16 v[144:159], v[224:227], v[100:103], v[144:159]\n"
"v_max3_u32 v19, v19, v100, v101\n"
"buffer_load_dwordx4 v0, s[40:43], s95 offen offset:1024 lds\n"
"v_mov_b32 v232, v192\n"
"v_mfma_f32_32x32x16_bf16 v[160:175], v[228:231], v[100:103], v[160:175]\n"
"ds_read_b128 v[224:227], v0 offset:20480\n"
"ds_read_b128 v[228:231], v0 offset:50176\n"
"v_max3_u32 v19, v19, v102, v103\n"
"v_mov_b32 v233, v193\n"
"v_permlane32_swap_b32 v192, v232\n"
"s_nop 0\n"
"v_permlane32_swap_b32 v193, v233\n"
"s_waitcnt vmcnt(11) lgkmcnt(4)\n"
"v_mfma_f32_32x32x16_bf16 v[144:159], v[208:211], v[104:107], v[144:159]\n"
"v_max3_u32 v19, v19, v104, v105\n"
"buffer_load_dwordx4 v0, s[40:43], s95 offen offset:2048 lds\n"
"v_add_f32 v192, v192, v232\n"
"v_mfma_f32_32x32x16_bf16 v[160:175], v[212:215], v[104:107], v[160:175]\n"
"ds_read_b128 v[208:211], v0 offset:21504\n"
"ds_read_b128 v[212:215], v0 offset:51200\n"
"v_max3_u32 v19, v19, v106, v107\n"
"v_add_f32 v193, v193, v233\n"
"v_mul_f32 v192, 0x3b124925, v192\n"
"v_mul_f32 v193, 0x3b124925, v193\n"
"v_fma_f32 v193, -v192, v192, v193\n"
"s_waitcnt vmcnt(11) lgkmcnt(4)\n"
"v_mfma_f32_32x32x16_bf16 v[144:159], v[216:219], v[108:111], v[144:159]\n"
"v_max3_u32 v19, v19, v108, v109\n"
"buffer_load_dwordx4 v0, s[40:43], s95 offen offset:3072 lds\n"
"v_max_f32 v193, 0, v193\n"
"v_mfma_f32_32x32x16_bf16 v[160:175], v[220:223], v[108:111], v[160:175]\n"
"ds_read_b128 v[216:219], v0 offset:22528\n"
"ds_read_b128 v[220:223], v0 offset:52224\n"
"v_max3_u32 v19, v19, v110, v111\n"
"v_cvt_pk_bf16_f32 v193, v193, v193\n"
"v_and_b32 v193, 0xffff0000, v193\n"
"v_add_f32 v193, 0x3727c5ac, v193\n"
"s_waitcnt vmcnt(11) lgkmcnt(4)\n"
"v_mfma_f32_32x32x16_bf16 v[144:159], v[224:227], v[112:115], v[144:159]\n"
"v_max3_u32 v19, v19, v112, v113\n"
"s_add_u32 m0, s89, 93184\n"
"s_add_u32 s95, s88, 93184\n"
"buffer_load_dwordx4 v0, s[40:43], s95 offen lds\n"
"v_cvt_pk_bf16_f32 v193, v193, v193\n"
"v_mfma_f32_32x32x16_bf16 v[160:175], v[228:231], v[112:115], v[160:175]\n"
"ds_read_b128 v[224:227], v0 offset:23552\n"
"ds_read_b128 v[228:231], v0 offset:53248\n"
"v_max3_u32 v19, v19, v114, v115\n"
"v_and_b32 v193, 0xffff0000, v193\n"
"v_sqrt_f32 v193, v193\n"
"s_nop 0\n"
"v_cvt_pk_bf16_f32 v193, v193, v193\n"
"s_waitcnt vmcnt(11) lgkmcnt(4)\n"
"v_mfma_f32_32x32x16_bf16 v[144:159], v[208:211], v[116:119], v[144:159]\n"
"v_max3_u32 v19, v19, v116, v117\n"
"buffer_load_dwordx4 v0, s[40:43], s95 offen offset:1024 lds\n"
"v_and_b32 v193, 0xffff0000, v193\n"
"v_mfma_f32_32x32x16_bf16 v[160:175], v[212:215], v[116:119], v[160:175]\n"
"ds_read_b128 v[208:211], v0 offset:24576\n"
"ds_read_b128 v[212:215], v0 offset:54272\n"
"v_max3_u32 v19, v19, v118, v119\n"
"v_rcp_f32 v16, v193\n"
"v_cvt_pk_bf16_f32 v20, -v192, -v192\n"
"s_waitcnt vmcnt(11) lgkmcnt(4)\n"
"v_mfma_f32_32x32x16_bf16 v[144:159], v[216:219], v[120:123], v[144:159]\n"
"v_max3_u32 v19, v19, v120, v121\n"
"buffer_load_dwordx4 v0, s[40:43], s95 offen offset:2048 lds\n"
"v_lshrrev_b32 v233, 16, v193\n"
"v_mfma_f32_32x32x16_bf16 v[160:175], v[220:223], v[120:123], v[160:175]\n"
"ds_read_b128 v[216:219], v0 offset:25600\n"
"ds_read_b128 v[220:223], v0 offset:55296\n"
"v_max3_u32 v19, v19, v122, v123\n"
"v_mul_f32 v18, v193, v193\n"
"v_mov_b32 v21, v20\n"
"v_mov_b32 v22, v20\n"
"v_cndmask_b32_e64 v23, v233, v20, s[62:63]\n"
"s_waitcnt vmcnt(11) lgkmcnt(4)\n"
"v_mfma_f32_32x32x16_bf16 v[144:159], v[224:227], v[124:127], v[144:159]\n"
"v_max3_u32 v19, v19, v124, v125\n"
"buffer_load_dwordx4 v0, s[40:43], s95 offen offset:3072 lds\n"
"v_mul_f32 v17, 0xbfb8aa3b, v16\n"
"v_mfma_f32_32x32x16_bf16 v[160:175], v[228:231], v[124:127], v[160:175]\n"
"ds_read_b128 v[224:227], v0 offset:26624\n"
"ds_read_b128 v[228:231], v0 offset:56320\n"
"v_max3_u32 v19, v19, v126, v127\n"
"s_waitcnt vmcnt(11) lgkmcnt(4)\n"
"v_mfma_f32_32x32x16_bf16 v[144:159], v[208:211], v[128:131], v[144:159]\n"
"v_max3_u32 v19, v19, v128, v129\n"
"buffer_load_dwordx4 a[96:99], v0, s[40:43], s69 offen offset:3072\n"
"v_mfma_f32_32x32x16_bf16 v[160:175], v[212:215], v[128:131], v[160:175]\n"
"ds_read_b128 v[208:211], v0 offset:27648\n"
"ds_read_b128 v[212:215], v0 offset:57344\n"
"v_max3_u32 v19, v19, v130, v131\n"
"s_waitcnt vmcnt(11) lgkmcnt(4)\n"
"v_mfma_f32_32x32x16_bf16 v[144:159], v[216:219], v[132:135], v[144:159]\n"
"v_max3_u32 v19, v19, v132, v133\n"
"buffer_load_dwordx4 a[100:103], v0, s[40:43], s70 offen offset:3072\n"
"v_mfma_f32_32x32x16_bf16 v[160:175], v[220:223], v[132:135], v[160:175]\n"
"ds_read_b128 v[216:219], v0 offset:28672\n"
"ds_read_b128 v[220:223], v0 offset:58368\n"
"v_max3_u32 v19, v19, v134, v135\n"
"s_waitcnt vmcnt(11) lgkmcnt(4)\n"
"v_mfma_f32_32x32x16_bf16 v[144:159], v[224:227], v[136:139], v[144:159]\n"
"v_max3_u32 v19, v19, v136, v137\n"
"s_add_u32 s94, s69, 8192\n"
"buffer_load_dwordx4 a[104:107], v0, s[40:43], s94 offen offset:2048\n"
"v_mfma_f32_32x32x16_bf16 v[160:175], v[228:231], v[136:139], v[160:175]\n"
"ds_read_b128 v[224:227], v0 offset:59392\n"
"ds_read_b128 v[228:231], v0 offset:60416\n"
"v_max3_u32 v19, v19, v138, v139\n"
"s_waitcnt vmcnt(11) lgkmcnt(4)\n"
"v_mfma_f32_32x32x16_bf16 v[144:159], v[208:211], v[140:143], v[144:159]\n"
"v_max3_u32 v19, v19, v140, v141\n"
"v_mfma_f32_32x32x16_bf16 v[160:175], v[212:215], v[140:143], v[160:175]\n"
"ds_read_b128 v[208:211], v0 offset:61440\n"
"ds_read_b128 v[212:215], v0 offset:62464\n"
"v_max3_u32 v19, v19, v142, v143\n"
"s_waitcnt lgkmcnt(4)\n"
"v_mfma_f32_32x32x16_bf16 v[144:159], v[216:219], v[20:23], v[144:159]\n"
"v_mfma_f32_32x32x16_bf16 v[160:175], v[220:223], v[20:23], v[160:175]\n"
"ds_read_b128 v[216:219], v0 offset:63488\n"
"ds_read_b128 v[220:223], v0 offset:64512\n"
"v_cmp_eq_u32 vcc, -1, v19\n"
"s_cbranch_vccnz L_x1v_s_0\n"
"L_x1v_go_0:\n"
"s_barrier\n"
"v_mov_b32 v252, -1\n"
"v_mov_b32 v253, -1\n"
"v_mov_b32 v254, -1\n"
"v_mov_b32 v255, -1\n"
"s_waitcnt lgkmcnt(4)\n"
"v_mfma_f32_32x32x16_bf16 v[176:191], v[224:227], v[32:35], 0\n"
"s_add_u32 m0, s89, 118784\n"
"s_add_u32 s95, s88, 118784\n"
"buffer_load_dwordx4 v0, s[40:43], s95 offen lds\n"
"v_mfma_f32_32x32x16_bf16 v[176:191], v[228:231], v[36:39], v[176:191]\n"
"ds_read_b128 v[224:227], v1\n"
"ds_read_b128 v[228:231], v1 offset:1024\n"
"v_mul_f32 v232, v17, v144\n"
"v_mul_f32 v233, v17, v145\n"
"v_mul_f32 v234, v17, v146\n"
"v_mul_f32 v235, v17, v147\n"
"s_waitcnt lgkmcnt(4)\n"
"v_mfma_f32_32x32x16_bf16 v[176:191], v[208:211], v[40:43], v[176:191]\n"
"buffer_load_dwordx4 v0, s[40:43], s95 offen offset:1024 lds\n"
"v_exp_f32 v232, v232\n"
"v_mfma_f32_32x32x16_bf16 v[176:191], v[212:215], v[44:47], v[176:191]\n"
"ds_read_b128 v[208:211], v1 offset:2048\n"
"ds_read_b128 v[212:215], v1 offset:3072\n"
"v_exp_f32 v233, v233\n"
"v_exp_f32 v234, v234\n"
"s_waitcnt lgkmcnt(4)\n"
"v_mfma_f32_32x32x16_bf16 v[176:191], v[216:219], v[48:51], v[176:191]\n"
"buffer_load_dwordx4 v0, s[40:43], s95 offen offset:2048 lds\n"
"v_exp_f32 v235, v235\n"
"v_mul_f32 v144, v144, v152\n"
"v_mfma_f32_32x32x16_bf16 v[176:191], v[220:223], v[52:55], v[176:191]\n"
"ds_read_b128 v[216:219], v1 offset:4096\n"
"ds_read_b128 v[220:223], v1 offset:5120\n"
"buffer_load_dwordx4 v0, s[40:43], s95 offen offset:3072 lds\n"
"v_mul_f32 v145, v145, v153\n"
"v_mul_f32 v146, v146, v154\n"
"s_waitcnt lgkmcnt(4)\n"
"v_mfma_f32_32x32x16_bf16 v[176:191], v[224:227], v[56:59], v[176:191]\n"
"v_mul_f32 v147, v147, v155\n"
"v_fma_f32 v232, v232, v18, v18\n"
"v_fma_f32 v233, v233, v18, v18\n"
"v_fma_f32 v234, v234, v18, v18\n"
"v_mfma_f32_32x32x16_bf16 v[176:191], v[228:231], v[60:63], v[176:191]\n"
"ds_read_b128 v[224:227], v1 offset:6144\n"
"ds_read_b128 v[228:231], v1 offset:7168\n"
"s_add_u32 m0, s89, 122880\n"
"s_add_u32 s95, s88, 122880\n"
"buffer_load_dwordx4 v0, s[40:43], s95 offen lds\n"
"v_fma_f32 v235, v235, v18, v18\n"
"s_waitcnt lgkmcnt(4)\n"
"v_mfma_f32_32x32x16_bf16 v[176:191], v[208:211], v[64:67], v[176:191]\n"
"v_rcp_f32 v232, v232\n"
"v_rcp_f32 v233, v233\n"
"v_mfma_f32_32x32x16_bf16 v[176:191], v[212:215], v[68:71], v[176:191]\n"
"ds_read_b128 v[208:211], v1 offset:8192\n"
"ds_read_b128 v[212:215], v1 offset:9216\n"
"buffer_load_dwordx4 v0, s[40:43], s95 offen offset:1024 lds\n"
"v_rcp_f32 v234, v234\n"
"s_waitcnt lgkmcnt(4)\n"
"v_mfma_f32_32x32x16_bf16 v[176:191], v[216:219], v[72:75], v[176:191]\n"
"buffer_load_dwordx4 v0, s[40:43], s95 offen offset:2048 lds\n"
"v_rcp_f32 v235, v235\n"
"v_mul_f32 v144, v144, v232\n"
"v_mfma_f32_32x32x16_bf16 v[176:191], v[220:223], v[76:79], v[176:191]\n"
"ds_read_b128 v[216:219], v1 offset:10240\n"
"ds_read_b128 v[220:223], v1 offset:11264\n"
"v_mul_f32 v145, v145, v233\n"
"v_mul_f32 v146, v146, v234\n"
"v_mul_f32 v147, v147, v235\n"
"v_cvt_pk_bf16_f32 v240, v144, v145\n"
"s_waitcnt lgkmcnt(4)\n"
"v_mfma_f32_32x32x16_bf16 v[176:191], v[224:227], v[80:83], v[176:191]\n"
"buffer_load_dwordx4 v0, s[40:43], s95 offen offset:3072 lds\n"
"v_cvt_pk_bf16_f32 v241, v146, v147\n"
"v_min_u32 v240, -2, v240\n"
"v_mfma_f32_32x32x16_bf16 v[176:191], v[228:231], v[84:87], v[176:191]\n"
"ds_read_b128 v[224:227], v1 offset:12288\n"
"ds_read_b128 v[228:231], v1 offset:13312\n"
"v_min_u32 v241, -2, v241\n"
"v_mul_f32 v236, v17, v148\n"
"v_mul_f32 v237, v17, v149\n"
"v_mul_f32 v238, v17, v150\n"
"v_mul_f32 v239, v17, v151\n"
"s_waitcnt lgkmcnt(4)\n"
"v_mfma_f32_32x32x16_bf16 v[176:191], v[208:211], v[88:91], v[176:191]\n"
"s_add_u32 m0, s89, 0\n"
"s_add_u32 s95, s88, 148480\n"
"buffer_load_dwordx4 v0, s[40:43], s95 offen lds\n"
"v_exp_f32 v236, v236\n"
"v_mfma_f32_32x32x16_bf16 v[176:191], v[212:215], v[92:95], v[176:191]\n"
"ds_read_b128 v[208:211], v1 offset:14336\n"
"ds_read_b128 v[212:215], v1 offset:15360\n"
"buffer_load_dwordx4 v0, s[40:43], s95 offen offset:1024 lds\n"
"v_exp_f32 v237, v237\n"
"s_waitcnt lgkmcnt(4)\n"
"v_mfma_f32_32x32x16_bf16 v[176:191], v[216:219], v[96:99], v[176:191]\n"
"v_exp_f32 v238, v238\n"
"v_exp_f32 v239, v239\n"
"v_mfma_f32_32x32x16_bf16 v[176:191], v[220:223], v[100:103], v[176:191]\n"
"ds_read_b128 v[216:219], v1 offset:16384\n"
"ds_read_b128 v[220:223], v1 offset:17408\n"
"buffer_load_dwordx4 v0, s[40:43], s95 offen offset:2048 lds\n"
"v_mul_f32 v148, v148, v156\n"
"v_mul_f32 v149, v149, v157\n"
"s_waitcnt lgkmcnt(4)\n"
"v_mfma_f32_32x32x16_bf16 v[176:191], v[224:227], v[104:107], v[176:191]\n"
"v_mul_f32 v150, v150, v158\n"
"v_mul_f32 v151, v151, v159\n"
"v_fma_f32 v236, v236, v18, v18\n"
"v_fma_f32 v237, v237, v18, v18\n"
"v_fma_f32 v238, v238, v18, v18\n"
"v_mfma_f32_32x32x16_bf16 v[176:191], v[228:231], v[108:111], v[176:191]\n"
"ds_read_b128 v[224:227], v1 offset:18432\n"
"ds_read_b128 v[228:231], v1 offset:19456\n"
"buffer_load_dwordx4 v0, s[40:43], s95 offen offset:3072 lds\n"
"v_fma_f32 v239, v239, v18, v18\n"
"s_waitcnt lgkmcnt(4)\n"
"v_mfma_f32_32x32x16_bf16 v[176:191], v[208:211], v[112:115], v[176:191]\n"
"s_waitcnt vmcnt(15)\n"
"s_barrier\n"
"s_add_u32 m0, s89, 4096\n"
"s_add_u32 s95, s88, 152576\n"
"buffer_load_dwordx4 v0, s[40:43], s95 offen lds\n"
"v_rcp_f32 v236, v236\n"
"v_mfma_f32_32x32x16_bf16 v[176:191], v[212:215], v[116:119], v[176:191]\n"
"ds_read_b128 v[208:211], v1 offset:20480\n"
"ds_read_b128 v[212:215], v1 offset:21504\n"
"v_rcp_f32 v237, v237\n"
"v_rcp_f32 v238, v238\n"
"s_waitcnt lgkmcnt(4)\n"
"v_mfma_f32_32x32x16_bf16 v[176:191], v[216:219], v[120:123], v[176:191]\n"
"buffer_load_dwordx4 v0, s[40:43], s95 offen offset:1024 lds\n"
"v_rcp_f32 v239, v239\n"
"v_mul_f32 v148, v148, v236\n"
"v_mfma_f32_32x32x16_bf16 v[176:191], v[220:223], v[124:127], v[176:191]\n"
"ds_read_b128 v[216:219], v1 offset:22528\n"
"ds_read_b128 v[220:223], v1 offset:23552\n"
"v_mul_f32 v149, v149, v237\n"
"v_mul_f32 v150, v150, v238\n"
"v_mul_f32 v151, v151, v239\n"
"v_cvt_pk_bf16_f32 v242, v148, v149\n"
"s_waitcnt lgkmcnt(4)\n"
"v_mfma_f32_32x32x16_bf16 v[176:191], v[224:227], v[128:131], v[176:191]\n"
"buffer_load_dwordx4 v0, s[40:43], s95 offen offset:2048 lds\n"
"v_cvt_pk_bf16_f32 v243, v150, v151\n"
"v_min_u32 v242, -2, v242\n"
"v_mfma_f32_32x32x16_bf16 v[176:191], v[228:231], v[132:135], v[176:191]\n"
"ds_read_b128 v[224:227], v1 offset:24576\n"
"ds_read_b128 v[228:231], v1 offset:25600\n"
"buffer_load_dwordx4 v0, s[40:43], s95 offen offset:3072 lds\n"
"v_min_u32 v243, -2, v243\n"
"s_waitcnt lgkmcnt(4)\n"
"v_mfma_f32_32x32x16_bf16 v[176:191], v[208:211], v[136:139], v[176:191]\n"
"buffer_store_dwordx4 v[240:243], v0, s[44:47], s82 offen sc1\n"
"v_mul_f32 v232, v17, v160\n"
"v_mul_f32 v233, v17, v161\n"
"v_mul_f32 v234, v17, v162\n"
"v_mfma_f32_32x32x16_bf16 v[176:191], v[212:215], v[140:143], v[176:191]\n"
"ds_read_b128 v[208:211], v1 offset:26624\n"
"ds_read_b128 v[212:215], v1 offset:27648\n"
"s_add_u32 m0, s89, 29696\n"
"s_add_u32 s95, s88, 178176\n"
"buffer_load_dwordx4 v0, s[40:43], s95 offen lds\n"
"v_mul_f32 v235, v17, v163\n"
"s_waitcnt lgkmcnt(4)\n"
"v_mfma_f32_32x32x16_bf16 v[176:191], v[216:219], v[20:23], v[176:191]\n"
"v_exp_f32 v232, v232\n"
"v_exp_f32 v233, v233\n"
"v_mfma_f32_32x32x16_bf16 v[144:159], v[220:223], v[32:35], 0\n"
"ds_read_b128 v[216:219], v1 offset:28672\n"
"ds_read_b128 v[220:223], v1 offset:29696\n"
"buffer_load_dwordx4 v0, s[40:43], s95 offen offset:1024 lds\n"
"v_exp_f32 v234, v234\n"
"s_waitcnt lgkmcnt(4)\n"
"v_mfma_f32_32x32x16_bf16 v[144:159], v[224:227], v[36:39], v[144:159]\n"
"buffer_load_dwordx4 v0, s[40:43], s95 offen offset:2048 lds\n"
"v_exp_f32 v235, v235\n"
"v_mul_f32 v160, v160, v168\n"
"v_mfma_f32_32x32x16_bf16 v[144:159], v[228:231], v[40:43], v[144:159]\n"
"ds_read_b128 v[224:227], v1 offset:30720\n"
"ds_read_b128 v[228:231], v1 offset:31744\n"
"v_mul_f32 v161, v161, v169\n"
"v_mul_f32 v162, v162, v170\n"
"v_mul_f32 v163, v163, v171\n"
"v_fma_f32 v232, v232, v18, v18\n"
"s_waitcnt lgkmcnt(4)\n"
"v_mfma_f32_32x32x16_bf16 v[144:159], v[208:211], v[44:47], v[144:159]\n"
"buffer_load_dwordx4 v0, s[40:43], s95 offen offset:3072 lds\n"
"v_fma_f32 v233, v233, v18, v18\n"
"v_fma_f32 v234, v234, v18, v18\n"
"v_mfma_f32_32x32x16_bf16 v[144:159], v[212:215], v[48:51], v[144:159]\n"
"ds_read_b128 v[208:211], v1 offset:32768\n"
"ds_read_b128 v[212:215], v1 offset:33792\n"
"v_fma_f32 v235, v235, v18, v18\n"
"v_rcp_f32 v232, v232\n"
"v_rcp_f32 v233, v233\n"
"s_waitcnt lgkmcnt(4)\n"
"v_mfma_f32_32x32x16_bf16 v[144:159], v[216:219], v[52:55], v[144:159]\n"
"s_add_u32 m0, s89, 33792\n"
"s_add_u32 s95, s88, 182272\n"
"buffer_load_dwordx4 v0, s[40:43], s95 offen lds\n"
"v_rcp_f32 v234, v234\n"
"v_mfma_f32_32x32x16_bf16 v[144:159], v[220:223], v[56:59], v[144:159]\n"
"ds_read_b128 v[216:219], v1 offset:34816\n"
"ds_read_b128 v[220:223], v1 offset:35840\n"
"buffer_load_dwordx4 v0, s[40:43], s95 offen offset:1024 lds\n"
"v_rcp_f32 v235, v235\n"
"s_waitcnt lgkmcnt(4)\n"
"v_mfma_f32_32x32x16_bf16 v[144:159], v[224:227], v[60:63], v[144:159]\n"
"v_mul_f32 v160, v160, v232\n"
"v_mul_f32 v161, v161, v233\n"
"v_mul_f32 v162, v162, v234\n"
"v_mul_f32 v163, v163, v235\n"
"v_mfma_f32_32x32x16_bf16 v[144:159], v[228:231], v[64:67], v[144:159]\n"
"ds_read_b128 v[224:227], v1 offset:36864\n"
"ds_read_b128 v[228:231], v1 offset:37888\n"
"buffer_load_dwordx4 v0, s[40:43], s95 offen offset:2048 lds\n"
"v_cvt_pk_bf16_f32 v244, v160, v161\n"
"v_cvt_pk_bf16_f32 v245, v162, v163\n"
"s_waitcnt lgkmcnt(4)\n"
"v_mfma_f32_32x32x16_bf16 v[144:159], v[208:211], v[68:71], v[144:159]\n"
"v_min_u32 v244, -2, v244\n"
"v_min_u32 v245, -2, v245\n"
"v_mul_f32 v236, v17, v164\n"
"v_mul_f32 v237, v17, v165\n"
"v_mul_f32 v238, v17, v166\n"
"v_mfma_f32_32x32x16_bf16 v[144:159], v[212:215], v[72:75], v[144:159]\n"
"ds_read_b128 v[208:211], v1 offset:38912\n"
"ds_read_b128 v[212:215], v1 offset:39936\n"
"buffer_load_dwordx4 v0, s[40:43], s95 offen offset:3072 lds\n"
"v_mul_f32 v239, v17, v167\n"
"s_waitcnt lgkmcnt(4)\n"
"v_mfma_f32_32x32x16_bf16 v[144:159], v[216:219], v[76:79], v[144:159]\n"
"s_add_u32 s94, s75, 14614528\n"
"buffer_store_dwordx4 v[252:255], v0, s[44:47], s94 offen sc1\n"
"v_exp_f32 v236, v236\n"
"v_mfma_f32_32x32x16_bf16 v[144:159], v[220:223], v[80:83], v[144:159]\n"
"ds_read_b128 v[216:219], v1 offset:40960\n"
"ds_read_b128 v[220:223], v1 offset:41984\n"
"v_exp_f32 v237, v237\n"
"v_exp_f32 v238, v238\n"
"s_waitcnt lgkmcnt(4)\n"
"v_mfma_f32_32x32x16_bf16 v[144:159], v[224:227], v[84:87], v[144:159]\n"
"s_add_u32 s94, s75, 14614528\n"
"buffer_store_dwordx4 v[252:255], v0, s[44:47], s94 offen offset:1024 sc1\n"
"v_exp_f32 v239, v239\n"
"v_mul_f32 v164, v164, v172\n"
"v_mfma_f32_32x32x16_bf16 v[144:159], v[228:231], v[88:91], v[144:159]\n"
"ds_read_b128 v[224:227], v1 offset:43008\n"
"ds_read_b128 v[228:231], v1 offset:44032\n"
"v_mul_f32 v165, v165, v173\n"
"v_mul_f32 v166, v166, v174\n"
"v_mul_f32 v167, v167, v175\n"
"v_fma_f32 v236, v236, v18, v18\n"
"s_waitcnt lgkmcnt(4)\n"
"v_mfma_f32_32x32x16_bf16 v[144:159], v[208:211], v[92:95], v[144:159]\n"
"s_add_u32 s94, s70, 8192\n"
"buffer_load_dwordx4 a[108:111], v0, s[40:43], s94 offen offset:2048\n"
"v_fma_f32 v237, v237, v18, v18\n"
"v_fma_f32 v238, v238, v18, v18\n"
"v_mfma_f32_32x32x16_bf16 v[144:159], v[212:215], v[96:99], v[144:159]\n"
"ds_read_b128 v[208:211], v1 offset:45056\n"
"ds_read_b128 v[212:215], v1 offset:46080\n"
"s_add_u32 s94, s69, 16384\n"
"buffer_load_dwordx4 a[112:115], v0, s[40:43], s94 offen offset:1024\n"
"v_fma_f32 v239, v239, v18, v18\n"
"s_waitcnt lgkmcnt(4)\n"
"v_mfma_f32_32x32x16_bf16 v[144:159], v[216:219], v[100:103], v[144:159]\n"
"v_rcp_f32 v236, v236\n"
"v_rcp_f32 v237, v237\n"
"v_mfma_f32_32x32x16_bf16 v[144:159], v[220:223], v[104:107], v[144:159]\n"
"ds_read_b128 v[216:219], v1 offset:47104\n"
"ds_read_b128 v[220:223], v1 offset:48128\n"
"s_add_u32 s94, s70, 16384\n"
"buffer_load_dwordx4 a[116:119], v0, s[40:43], s94 offen offset:1024\n"
"v_rcp_f32 v238, v238\n"
"s_waitcnt lgkmcnt(4)\n"
"v_mfma_f32_32x32x16_bf16 v[144:159], v[224:227], v[108:111], v[144:159]\n"
"v_rcp_f32 v239, v239\n"
"v_mul_f32 v164, v164, v236\n"
"v_mul_f32 v165, v165, v237\n"
"v_mul_f32 v166, v166, v238\n"
"v_mfma_f32_32x32x16_bf16 v[144:159], v[228:231], v[112:115], v[144:159]\n"
"ds_read_b128 v[224:227], v1 offset:49152\n"
"ds_read_b128 v[228:231], v1 offset:50176\n"
"s_add_u32 s94, s69, 24576\n"
"buffer_load_dwordx4 a[120:123], v0, s[40:43], s94 offen\n"
"v_mul_f32 v167, v167, v239\n"
"v_cvt_pk_bf16_f32 v246, v164, v165\n"
"s_waitcnt lgkmcnt(4)\n"
"v_mfma_f32_32x32x16_bf16 v[144:159], v[208:211], v[116:119], v[144:159]\n"
"s_waitcnt vmcnt(23)\n"
"s_barrier\n"
"s_add_u32 s94, s70, 24576\n"
"buffer_load_dwordx4 a[124:127], v0, s[40:43], s94 offen\n"
"v_cvt_pk_bf16_f32 v247, v166, v167\n"
"v_min_u32 v246, -2, v246\n"
"v_mfma_f32_32x32x16_bf16 v[144:159], v[212:215], v[120:123], v[144:159]\n"
"ds_read_b128 v[208:211], v1 offset:51200\n"
"ds_read_b128 v[212:215], v1 offset:52224\n"
"v_min_u32 v247, -2, v247\n"
"buffer_store_dwordx4 v[244:247], v0, s[44:47], s82 offen offset:1024 sc1\n"
"v_mul_f32 v232, v17, v176\n"
"s_waitcnt lgkmcnt(4)\n"
"v_mfma_f32_32x32x16_bf16 v[144:159], v[216:219], v[124:127], v[144:159]\n"
"s_add_u32 s94, s69, 4096\n"
"buffer_load_dwordx4 a[128:131], v0, s[40:43], s94 offen\n"
"v_mul_f32 v233, v17, v177\n"
"v_mul_f32 v234, v17, v178\n"
"v_mfma_f32_32x32x16_bf16 v[144:159], v[220:223], v[128:131], v[144:159]\n"
"ds_read_b128 v[216:219], v1 offset:53248\n"
"ds_read_b128 v[220:223], v1 offset:54272\n"
"v_mul_f32 v235, v17, v179\n"
"v_exp_f32 v232, v232\n"
"v_exp_f32 v233, v233\n"
"s_waitcnt lgkmcnt(4)\n"
"v_mfma_f32_32x32x16_bf16 v[144:159], v[224:227], v[132:135], v[144:159]\n"
"s_add_u32 s94, s70, 4096\n"
"buffer_load_dwordx4 a[132:135], v0, s[40:43], s94 offen\n"
"v_exp_f32 v234, v234\n"
"v_mfma_f32_32x32x16_bf16 v[144:159], v[228:231], v[136:139], v[144:159]\n"
"ds_read_b128 v[224:227], v1 offset:55296\n"
"ds_read_b128 v[228:231], v1 offset:56320\n"
"s_add_u32 s94, s69, 8192\n"
"buffer_load_dwordx4 a[136:139], v0, s[40:43], s94 offen offset:3072\n"
"v_exp_f32 v235, v235\n"
"s_waitcnt lgkmcnt(4)\n"
"v_mfma_f32_32x32x16_bf16 v[144:159], v[208:211], v[140:143], v[144:159]\n"
"v_mul_f32 v176, v176, v184\n"
"v_mul_f32 v177, v177, v185\n"
"v_mul_f32 v178, v178, v186\n"
"v_mul_f32 v179, v179, v187\n"
"v_mfma_f32_32x32x16_bf16 v[144:159], v[212:215], v[20:23], v[144:159]\n"
"ds_read_b128 v[208:211], v1 offset:57344\n"
"ds_read_b128 v[212:215], v1 offset:58368\n"
"s_add_u32 s94, s70, 8192\n"
"buffer_load_dwordx4 a[140:143], v0, s[40:43], s94 offen offset:3072\n"
"v_fma_f32 v232, v232, v18, v18\n"
"v_fma_f32 v233, v233, v18, v18\n"
"s_waitcnt lgkmcnt(4)\n"
"v_mfma_f32_32x32x16_bf16 v[160:175], v[216:219], v[32:35], 0\n"
"v_fma_f32 v234, v234, v18, v18\n"
"v_fma_f32 v235, v235, v18, v18\n"
"v_rcp_f32 v232, v232\n"
"v_mfma_f32_32x32x16_bf16 v[160:175], v[220:223], v[36:39], v[160:175]\n"
"ds_read_b128 v[216:219], v1 offset:59392\n"
"ds_read_b128 v[220:223], v1 offset:60416\n"
"s_add_u32 s94, s69, 16384\n"
"buffer_load_dwordx4 a[144:147], v0, s[40:43], s94 offen offset:2048\n"
"v_rcp_f32 v233, v233\n"
"s_waitcnt lgkmcnt(4)\n"
"v_mfma_f32_32x32x16_bf16 v[160:175], v[224:227], v[40:43], v[160:175]\n"
"s_add_u32 s94, s70, 16384\n"
"buffer_load_dwordx4 a[148:151], v0, s[40:43], s94 offen offset:2048\n"
"v_rcp_f32 v234, v234\n"
"v_mfma_f32_32x32x16_bf16 v[160:175], v[228:231], v[44:47], v[160:175]\n"
"ds_read_b128 v[224:227], v1 offset:61440\n"
"ds_read_b128 v[228:231], v1 offset:62464\n"
"v_rcp_f32 v235, v235\n"
"v_mul_f32 v176, v176, v232\n"
"v_mul_f32 v177, v177, v233\n"
"v_mul_f32 v178, v178, v234\n"
"s_waitcnt lgkmcnt(4)\n"
"v_mfma_f32_32x32x16_bf16 v[160:175], v[208:211], v[48:51], v[160:175]\n"
"s_add_u32 s94, s69, 24576\n"
"buffer_load_dwordx4 a[152:155], v0, s[40:43], s94 offen offset:1024\n"
"v_mul_f32 v179, v179, v235\n"
"v_cvt_pk_bf16_f32 v240, v176, v177\n"
"v_mfma_f32_32x32x16_bf16 v[160:175], v[212:215], v[52:55], v[160:175]\n"
"ds_read_b128 v[208:211], v1 offset:63488\n"
"ds_read_b128 v[212:215], v1 offset:64512\n"
"v_cvt_pk_bf16_f32 v241, v178, v179\n"
"v_min_u32 v240, -2, v240\n"
"v_min_u32 v241, -2, v241\n"
"v_mul_f32 v236, v17, v180\n"
"v_mul_f32 v237, v17, v181\n"
"s_waitcnt lgkmcnt(4)\n"
"v_mfma_f32_32x32x16_bf16 v[160:175], v[216:219], v[56:59], v[160:175]\n"
"s_add_u32 s94, s70, 24576\n"
"buffer_load_dwordx4 a[156:159], v0, s[40:43], s94 offen offset:1024\n"
"v_mul_f32 v238, v17, v182\n"
"v_mul_f32 v239, v17, v183\n"
"v_mfma_f32_32x32x16_bf16 v[160:175], v[220:223], v[60:63], v[160:175]\n"
"ds_read_b128 v[216:219], v2\n"
"ds_read_b128 v[220:223], v2 offset:1024\n"
"s_add_u32 s94, s69, 4096\n"
"buffer_load_dwordx4 a[160:163], v0, s[40:43], s94 offen offset:1024\n"
"v_exp_f32 v236, v236\n"
"s_waitcnt lgkmcnt(4)\n"
"v_mfma_f32_32x32x16_bf16 v[160:175], v[224:227], v[64:67], v[160:175]\n"
"v_exp_f32 v237, v237\n"
"v_exp_f32 v238, v238\n"
"v_mfma_f32_32x32x16_bf16 v[160:175], v[228:231], v[68:71], v[160:175]\n"
"ds_read_b128 v[224:227], v2 offset:2048\n"
"ds_read_b128 v[228:231], v2 offset:3072\n"
"s_add_u32 s94, s70, 4096\n"
"buffer_load_dwordx4 a[164:167], v0, s[40:43], s94 offen offset:1024\n"
"v_exp_f32 v239, v239\n"
"s_waitcnt lgkmcnt(4)\n"
"v_mfma_f32_32x32x16_bf16 v[160:175], v[208:211], v[72:75], v[160:175]\n"
"v_mul_f32 v180, v180, v188\n"
"v_mul_f32 v181, v181, v189\n"
"v_mul_f32 v182, v182, v190\n"
"v_mul_f32 v183, v183, v191\n"
"v_fma_f32 v236, v236, v18, v18\n"
"v_mfma_f32_32x32x16_bf16 v[160:175], v[212:215], v[76:79], v[160:175]\n"
"ds_read_b128 v[208:211], v2 offset:4096\n"
"ds_read_b128 v[212:215], v2 offset:5120\n"
"s_add_u32 s94, s69, 12288\n"
"buffer_load_dwordx4 a[168:171], v0, s[40:43], s94 offen\n"
"v_fma_f32 v237, v237, v18, v18\n"
"v_fma_f32 v238, v238, v18, v18\n"
"s_waitcnt lgkmcnt(4)\n"
"v_mfma_f32_32x32x16_bf16 v[160:175], v[216:219], v[80:83], v[160:175]\n"
"s_add_u32 s94, s70, 12288\n"
"buffer_load_dwordx4 a[172:175], v0, s[40:43], s94 offen\n"
"v_fma_f32 v239, v239, v18, v18\n"
"v_mfma_f32_32x32x16_bf16 v[160:175], v[220:223], v[84:87], v[160:175]\n"
"ds_read_b128 v[216:219], v2 offset:6144\n"
"ds_read_b128 v[220:223], v2 offset:7168\n"
"v_rcp_f32 v236, v236\n"
"v_rcp_f32 v237, v237\n"
"s_waitcnt lgkmcnt(4)\n"
"v_mfma_f32_32x32x16_bf16 v[160:175], v[224:227], v[88:91], v[160:175]\n"
"s_add_u32 s94, s69, 16384\n"
"buffer_load_dwordx4 a[176:179], v0, s[40:43], s94 offen offset:3072\n"
"v_rcp_f32 v238, v238\n"
"v_mfma_f32_32x32x16_bf16 v[160:175], v[228:231], v[92:95], v[160:175]\n"
"ds_read_b128 v[224:227], v2 offset:8192\n"
"ds_read_b128 v[228:231], v2 offset:9216\n"
"v_rcp_f32 v239, v239\n"
"v_mul_f32 v180, v180, v236\n"
"v_mul_f32 v181, v181, v237\n"
"v_mul_f32 v182, v182, v238\n"
"s_waitcnt lgkmcnt(4)\n"
"v_mfma_f32_32x32x16_bf16 v[160:175], v[208:211], v[96:99], v[160:175]\n"
"s_add_u32 s94, s70, 16384\n"
"buffer_load_dwordx4 a[180:183], v0, s[40:43], s94 offen offset:3072\n"
"v_mul_f32 v183, v183, v239\n"
"v_cvt_pk_bf16_f32 v242, v180, v181\n"
"v_mfma_f32_32x32x16_bf16 v[160:175], v[212:215], v[100:103], v[160:175]\n"
"ds_read_b128 v[208:211], v2 offset:10240\n"
"ds_read_b128 v[212:215], v2 offset:11264\n"
"s_add_u32 s94, s69, 24576\n"
"buffer_load_dwordx4 a[184:187], v0, s[40:43], s94 offen offset:2048\n"
"v_cvt_pk_bf16_f32 v243, v182, v183\n"
"v_min_u32 v242, -2, v242\n"
"s_waitcnt lgkmcnt(4)\n"
"v_mfma_f32_32x32x16_bf16 v[160:175], v[216:219], v[104:107], v[160:175]\n"
"v_min_u32 v243, -2, v243\n"
"buffer_store_dwordx4 v[240:243], v0, s[44:47], s82 offen offset:2048 sc1\n"
"v_mul_f32 v232, v17, v144\n"
"v_mfma_f32_32x32x16_bf16 v[160:175], v[220:223], v[108:111], v[160:175]\n"
"ds_read_b128 v[216:219], v2 offset:12288\n"
"ds_read_b128 v[220:223], v2 offset:13312\n"
"s_add_u32 s94, s70, 24576\n"
"buffer_load_dwordx4 a[188:191], v0, s[40:43], s94 offen offset:2048\n"
"v_mul_f32 v233, v17, v145\n"
"v_mul_f32 v234, v17, v146\n"
"s_waitcnt lgkmcnt(4)\n"
"v_mfma_f32_32x32x16_bf16 v[160:175], v[224:227], v[112:115], v[160:175]\n"
"s_waitcnt vmcnt(34)\n"
"s_waitcnt vmcnt(25)\n"
"s_barrier\n"
"v_mul_f32 v235, v17, v147\n"
"v_exp_f32 v232, v232\n"
"v_exp_f32 v233, v233\n"
"v_mfma_f32_32x32x16_bf16 v[160:175], v[228:231], v[116:119], v[160:175]\n"
"ds_read_b128 v[224:227], v2 offset:14336\n"
"ds_read_b128 v[228:231], v2 offset:15360\n"
"s_add_u32 s94, s69, 4096\n"
"buffer_load_dwordx4 a[192:195], v0, s[40:43], s94 offen offset:2048\n"
"v_exp_f32 v234, v234\n"
"s_waitcnt lgkmcnt(4)\n"
"v_mfma_f32_32x32x16_bf16 v[160:175], v[208:211], v[120:123], v[160:175]\n"
"s_add_u32 s94, s70, 4096\n"
"buffer_load_dwordx4 a[196:199], v0, s[40:43], s94 offen offset:2048\n"
"v_exp_f32 v235, v235\n"
"v_mfma_f32_32x32x16_bf16 v[160:175], v[212:215], v[124:127], v[160:175]\n"
"ds_read_b128 v[208:211], v2 offset:16384\n"
"ds_read_b128 v[212:215], v0\n"
"v_mul_f32 v144, v144, v152\n"
"v_mul_f32 v145, v145, v153\n"
"v_mul_f32 v146, v146, v154\n"
"v_mul_f32 v147, v147, v155\n"
"s_waitcnt lgkmcnt(4)\n"
"v_mfma_f32_32x32x16_bf16 v[160:175], v[216:219], v[128:131], v[160:175]\n"
"s_add_u32 s94, s69, 12288\n"
"buffer_load_dwordx4 a[200:203], v0, s[40:43], s94 offen offset:1024\n"
"v_fma_f32 v232, v232, v18, v18\n"
"v_fma_f32 v233, v233, v18, v18\n"
"v_mfma_f32_32x32x16_bf16 v[160:175], v[220:223], v[132:135], v[160:175]\n"
"ds_read_b128 v[216:219], v0 offset:1024\n"
"ds_read_b128 v[220:223], v0 offset:2048\n"
"v_fma_f32 v234, v234, v18, v18\n"
"v_fma_f32 v235, v235, v18, v18\n"
"v_rcp_f32 v232, v232\n"
"s_waitcnt lgkmcnt(4)\n"
"v_mfma_f32_32x32x16_bf16 v[160:175], v[224:227], v[136:139], v[160:175]\n"
"s_add_u32 s94, s70, 12288\n"
"buffer_load_dwordx4 a[204:207], v0, s[40:43], s94 offen offset:1024\n"
"v_rcp_f32 v233, v233\n"
"v_mfma_f32_32x32x16_bf16 v[160:175], v[228:231], v[140:143], v[160:175]\n"
"ds_read_b128 v[224:227], v0 offset:3072\n"
"ds_read_b128 v[228:231], v0 offset:4096\n"
"s_add_u32 s94, s69, 20480\n"
"buffer_load_dwordx4 a[208:211], v0, s[40:43], s94 offen\n"
"v_rcp_f32 v234, v234\n"
"s_waitcnt lgkmcnt(4)\n"
"v_mfma_f32_32x32x16_bf16 v[160:175], v[208:211], v[20:23], v[160:175]\n"
"v_rcp_f32 v235, v235\n"
"v_mul_f32 v144, v144, v232\n"
"v_mul_f32 v145, v145, v233\n"
"v_mul_f32 v146, v146, v234\n"
"v_mfma_f32_32x32x16_bf16 v[176:191], v[212:215], v[32:35], 0\n"
"ds_read_b128 v[208:211], v0 offset:5120\n"
"ds_read_b128 v[212:215], v0 offset:6144\n"
"s_add_u32 s94, s70, 20480\n"
"buffer_load_dwordx4 a[212:215], v0, s[40:43], s94 offen\n"
"v_mul_f32 v147, v147, v235\n"
"v_cvt_pk_bf16_f32 v244, v144, v145\n"
"s_waitcnt lgkmcnt(4)\n"
"v_mfma_f32_32x32x16_bf16 v[176:191], v[216:219], v[36:39], v[176:191]\n"
"v_cvt_pk_bf16_f32 v245, v146, v147\n"
"v_min_u32 v244, -2, v244\n"
"v_min_u32 v245, -2, v245\n"
"v_mul_f32 v236, v17, v148\n"
"v_mul_f32 v237, v17, v149\n"
"v_mfma_f32_32x32x16_bf16 v[176:191], v[220:223], v[40:43], v[176:191]\n"
"ds_read_b128 v[216:219], v0 offset:7168\n"
"ds_read_b128 v[220:223], v0 offset:8192\n"
"s_add_u32 s94, s69, 24576\n"
"buffer_load_dwordx4 a[216:219], v0, s[40:43], s94 offen offset:3072\n"
"v_mul_f32 v238, v17, v150\n"
"v_mul_f32 v239, v17, v151\n"
"s_waitcnt lgkmcnt(4)\n"
"v_mfma_f32_32x32x16_bf16 v[176:191], v[224:227], v[44:47], v[176:191]\n"
"s_add_u32 s94, s70, 24576\n"
"buffer_load_dwordx4 a[220:223], v0, s[40:43], s94 offen offset:3072\n"
"v_exp_f32 v236, v236\n"
"v_mfma_f32_32x32x16_bf16 v[176:191], v[228:231], v[48:51], v[176:191]\n"
"ds_read_b128 v[224:227], v0 offset:9216\n"
"ds_read_b128 v[228:231], v0 offset:10240\n"
"v_exp_f32 v237, v237\n"
"v_exp_f32 v238, v238\n"
"s_waitcnt lgkmcnt(4)\n"
"v_mfma_f32_32x32x16_bf16 v[176:191], v[208:211], v[52:55], v[176:191]\n"
"v_exp_f32 v239, v239\n"
"v_mul_f32 v148, v148, v156\n"
"v_mul_f32 v149, v149, v157\n"
"v_mul_f32 v150, v150, v158\n"
"v_mfma_f32_32x32x16_bf16 v[176:191], v[212:215], v[56:59], v[176:191]\n"
"ds_read_b128 v[208:211], v0 offset:11264\n"
"ds_read_b128 v[212:215], v0 offset:12288\n"
"v_mul_f32 v151, v151, v159\n"
"v_fma_f32 v236, v236, v18, v18\n"
"v_fma_f32 v237, v237, v18, v18\n"
"v_fma_f32 v238, v238, v18, v18\n"
"s_waitcnt lgkmcnt(4)\n"
"v_mfma_f32_32x32x16_bf16 v[176:191], v[216:219], v[60:63], v[176:191]\n"
"v_fma_f32 v239, v239, v18, v18\n"
"v_rcp_f32 v236, v236\n"
"v_rcp_f32 v237, v237\n"
"v_mfma_f32_32x32x16_bf16 v[176:191], v[220:223], v[64:67], v[176:191]\n"
"ds_read_b128 v[216:219], v0 offset:13312\n"
"ds_read_b128 v[220:223], v0 offset:14336\n"
"v_rcp_f32 v238, v238\n"
"v_rcp_f32 v239, v239\n"
"s_waitcnt lgkmcnt(4)\n"
"v_mfma_f32_32x32x16_bf16 v[176:191], v[224:227], v[68:71], v[176:191]\n"
"v_mul_f32 v148, v148, v236\n"
"v_mul_f32 v149, v149, v237\n"
"v_mul_f32 v150, v150, v238\n"
"v_mul_f32 v151, v151, v239\n"
"v_cvt_pk_bf16_f32 v246, v148, v149\n"
"v_mfma_f32_32x32x16_bf16 v[176:191], v[228:231], v[72:75], v[176:191]\n"
"ds_read_b128 v[224:227], v0 offset:15360\n"
"ds_read_b128 v[228:231], v0 offset:16384\n"
"v_cvt_pk_bf16_f32 v247, v150, v151\n"
"v_min_u32 v246, -2, v246\n"
"v_min_u32 v247, -2, v247\n"
"s_waitcnt lgkmcnt(4)\n"
"v_mfma_f32_32x32x16_bf16 v[176:191], v[208:211], v[76:79], v[176:191]\n"
"buffer_store_dwordx4 v[244:247], v0, s[44:47], s82 offen offset:3072 sc1\n"
"v_mul_f32 v232, v17, v160\n"
"v_mul_f32 v233, v17, v161\n"
"v_mul_f32 v234, v17, v162\n"
"v_mfma_f32_32x32x16_bf16 v[176:191], v[212:215], v[80:83], v[176:191]\n"
"ds_read_b128 v[208:211], v0 offset:17408\n"
"ds_read_b128 v[212:215], v0 offset:18432\n"
"v_mul_f32 v235, v17, v163\n"
"v_exp_f32 v232, v232\n"
"s_waitcnt lgkmcnt(4)\n"
"v_mfma_f32_32x32x16_bf16 v[176:191], v[216:219], v[84:87], v[176:191]\n"
"s_waitcnt vmcnt(32)\n"
"buffer_load_dwordx4 v[192:195], v0, s[44:47], s83 offen sc1\n"
"v_exp_f32 v233, v233\n"
"v_mfma_f32_32x32x16_bf16 v[176:191], v[220:223], v[88:91], v[176:191]\n"
"ds_read_b128 v[216:219], v0 offset:19456\n"
"ds_read_b128 v[220:223], v0 offset:20480\n"
"buffer_load_dwordx4 v[196:199], v0, s[44:47], s84 offen sc1\n"
"v_exp_f32 v234, v234\n"
"s_waitcnt lgkmcnt(4)\n"
"v_mfma_f32_32x32x16_bf16 v[176:191], v[224:227], v[92:95], v[176:191]\n"
"v_exp_f32 v235, v235\n"
"v_mul_f32 v160, v160, v168\n"
"v_mul_f32 v161, v161, v169\n"
"v_mul_f32 v162, v162, v170\n"
"v_mfma_f32_32x32x16_bf16 v[176:191], v[228:231], v[96:99], v[176:191]\n"
"ds_read_b128 v[224:227], v0 offset:21504\n"
"ds_read_b128 v[228:231], v0 offset:22528\n"
"s_add_u32 s94, s83, 4096\n"
"buffer_load_dwordx4 v[200:203], v0, s[44:47], s94 offen offset:3072 sc1\n"
"v_mul_f32 v163, v163, v171\n"
"v_fma_f32 v232, v232, v18, v18\n"
"s_waitcnt lgkmcnt(4)\n"
"v_mfma_f32_32x32x16_bf16 v[176:191], v[208:211], v[100:103], v[176:191]\n"
"v_fma_f32 v233, v233, v18, v18\n"
"v_fma_f32 v234, v234, v18, v18\n"
"v_fma_f32 v235, v235, v18, v18\n"
"v_rcp_f32 v232, v232\n"
"v_mfma_f32_32x32x16_bf16 v[176:191], v[212:215], v[104:107], v[176:191]\n"
"ds_read_b128 v[208:211], v0 offset:23552\n"
"ds_read_b128 v[212:215], v0 offset:24576\n"
"s_add_u32 s94, s84, 4096\n"
"buffer_load_dwordx4 v[204:207], v0, s[44:47], s94 offen offset:3072 sc1\n"
"v_rcp_f32 v233, v233\n"
"s_waitcnt lgkmcnt(4)\n"
"v_mfma_f32_32x32x16_bf16 v[176:191], v[216:219], v[108:111], v[176:191]\n"
"v_rcp_f32 v234, v234\n"
"v_rcp_f32 v235, v235\n"
"v_mfma_f32_32x32x16_bf16 v[176:191], v[220:223], v[112:115], v[176:191]\n"
"ds_read_b128 v[216:219], v0 offset:25600\n"
"ds_read_b128 v[220:223], v0 offset:26624\n"
"v_mul_f32 v160, v160, v232\n"
"v_mul_f32 v161, v161, v233\n"
"v_mul_f32 v162, v162, v234\n"
"v_mul_f32 v163, v163, v235\n"
"v_cvt_pk_bf16_f32 v240, v160, v161\n"
"s_waitcnt lgkmcnt(4)\n"
"v_mfma_f32_32x32x16_bf16 v[176:191], v[224:227], v[116:119], v[176:191]\n"
"v_cvt_pk_bf16_f32 v241, v162, v163\n"
"v_min_u32 v240, -2, v240\n"
"v_min_u32 v241, -2, v241\n"
"v_mul_f32 v236, v17, v164\n"
"v_mfma_f32_32x32x16_bf16 v[176:191], v[228:231], v[120:123], v[176:191]\n"
"ds_read_b128 v[224:227], v0 offset:27648\n"
"ds_read_b128 v[228:231], v0 offset:28672\n"
"v_mul_f32 v237, v17, v165\n"
"v_mul_f32 v238, v17, v166\n"
"v_mul_f32 v239, v17, v167\n"
"v_exp_f32 v236, v236\n"
"s_waitcnt lgkmcnt(4)\n"
"v_mfma_f32_32x32x16_bf16 v[176:191], v[208:211], v[124:127], v[176:191]\n"
"v_exp_f32 v237, v237\n"
"v_exp_f32 v238, v238\n"
"v_mfma_f32_32x32x16_bf16 v[176:191], v[212:215], v[128:131], v[176:191]\n"
"ds_read_b128 v[208:211], v0 offset:29696\n"
"ds_read_b128 v[212:215], v0 offset:30720\n"
"v_exp_f32 v239, v239\n"
"v_mul_f32 v164, v164, v172\n"
"v_mul_f32 v165, v165, v173\n"
"v_mul_f32 v166, v166, v174\n"
"s_waitcnt lgkmcnt(4)\n"
"v_mfma_f32_32x32x16_bf16 v[176:191], v[216:219], v[132:135], v[176:191]\n"
"v_mul_f32 v167, v167, v175\n"
"v_fma_f32 v236, v236, v18, v18\n"
"v_fma_f32 v237, v237, v18, v18\n"
"v_fma_f32 v238, v238, v18, v18\n"
"v_mfma_f32_32x32x16_bf16 v[176:191], v[220:223], v[136:139], v[176:191]\n"
"ds_read_b128 v[216:219], v0 offset:31744\n"
"ds_read_b128 v[220:223], v0 offset:32768\n"
"v_fma_f32 v239, v239, v18, v18\n"
"v_rcp_f32 v236, v236\n"
"v_rcp_f32 v237, v237\n"
"s_waitcnt lgkmcnt(4)\n"
"v_mfma_f32_32x32x16_bf16 v[176:191], v[224:227], v[140:143], v[176:191]\n"
"v_rcp_f32 v238, v238\n"
"v_rcp_f32 v239, v239\n"
"v_mfma_f32_32x32x16_bf16 v[176:191], v[228:231], v[20:23], v[176:191]\n"
"ds_read_b128 v[224:227], v0 offset:33792\n"
"ds_read_b128 v[228:231], v0 offset:34816\n"
"v_mul_f32 v164, v164, v236\n"
"v_mul_f32 v165, v165, v237\n"
"v_mul_f32 v166, v166, v238\n"
"v_mul_f32 v167, v167, v239\n"
"v_cvt_pk_bf16_f32 v242, v164, v165\n"
"s_waitcnt lgkmcnt(4)\n"
"v_mfma_f32_32x32x16_bf16 v[144:159], v[208:211], v[32:35], 0\n"
"v_cvt_pk_bf16_f32 v243, v166, v167\n"
"v_min_u32 v242, -2, v242\n"
"v_min_u32 v243, -2, v243\n"
"s_add_u32 s59, s82, 4096\n"
"v_mfma_f32_32x32x16_bf16 v[144:159], v[212:215], v[36:39], v[144:159]\n"
"ds_read_b128 v[208:211], v0 offset:35840\n"
"ds_read_b128 v[212:215], v0 offset:36864\n"
"s_add_u32 s94, s83, 12288\n"
"buffer_load_dwordx4 v[32:35], v0, s[44:47], s94 offen offset:2048 sc1\n"
"buffer_store_dwordx4 v[240:243], v0, s[44:47], s59 offen sc1\n"
"s_waitcnt lgkmcnt(4)\n"
"v_mfma_f32_32x32x16_bf16 v[144:159], v[216:219], v[40:43], v[144:159]\n"
"s_add_u32 s94, s84, 12288\n"
"buffer_load_dwordx4 v[36:39], v0, s[44:47], s94 offen offset:2048 sc1\n"
"v_mul_f32 v232, v17, v176\n"
"v_mul_f32 v233, v17, v177\n"
"v_mfma_f32_32x32x16_bf16 v[144:159], v[220:223], v[44:47], v[144:159]\n"
"ds_read_b128 v[216:219], v0 offset:37888\n"
"ds_read_b128 v[220:223], v0 offset:38912\n"
"v_mul_f32 v234, v17, v178\n"
"v_mul_f32 v235, v17, v179\n"
"v_exp_f32 v232, v232\n"
"s_waitcnt lgkmcnt(4)\n"
"v_mfma_f32_32x32x16_bf16 v[144:159], v[224:227], v[48:51], v[144:159]\n"
"s_add_u32 s94, s83, 20480\n"
"buffer_load_dwordx4 v[40:43], v0, s[44:47], s94 offen offset:1024 sc1\n"
"v_exp_f32 v233, v233\n"
"v_mfma_f32_32x32x16_bf16 v[144:159], v[228:231], v[52:55], v[144:159]\n"
"ds_read_b128 v[224:227], v0 offset:39936\n"
"ds_read_b128 v[228:231], v0 offset:40960\n"
"v_exp_f32 v234, v234\n"
"v_exp_f32 v235, v235\n"
"v_mul_f32 v176, v176, v184\n"
"s_waitcnt lgkmcnt(4)\n"
"v_mfma_f32_32x32x16_bf16 v[144:159], v[208:211], v[56:59], v[144:159]\n"
"s_add_u32 s94, s84, 20480\n"
"buffer_load_dwordx4 v[44:47], v0, s[44:47], s94 offen offset:1024 sc1\n"
"v_mul_f32 v177, v177, v185\n"
"v_mul_f32 v178, v178, v186\n"
"v_mfma_f32_32x32x16_bf16 v[144:159], v[212:215], v[60:63], v[144:159]\n"
"ds_read_b128 v[208:211], v0 offset:41984\n"
"ds_read_b128 v[212:215], v0 offset:43008\n"
"buffer_load_dwordx4 v[48:51], v0, s[44:47], s83 offen offset:1024 sc1\n"
"v_mul_f32 v179, v179, v187\n"
"v_fma_f32 v232, v232, v18, v18\n"
"s_waitcnt lgkmcnt(4)\n"
"v_mfma_f32_32x32x16_bf16 v[144:159], v[216:219], v[64:67], v[144:159]\n"
"v_fma_f32 v233, v233, v18, v18\n"
"v_fma_f32 v234, v234, v18, v18\n"
"v_fma_f32 v235, v235, v18, v18\n"
"v_mfma_f32_32x32x16_bf16 v[144:159], v[220:223], v[68:71], v[144:159]\n"
"ds_read_b128 v[216:219], v0 offset:44032\n"
"ds_read_b128 v[220:223], v0 offset:45056\n"
"buffer_load_dwordx4 v[52:55], v0, s[44:47], s84 offen offset:1024 sc1\n"
"v_rcp_f32 v232, v232\n"
"s_waitcnt lgkmcnt(4)\n"
"v_mfma_f32_32x32x16_bf16 v[144:159], v[224:227], v[72:75], v[144:159]\n"
"v_rcp_f32 v233, v233\n"
"v_rcp_f32 v234, v234\n"
"v_mfma_f32_32x32x16_bf16 v[144:159], v[228:231], v[76:79], v[144:159]\n"
"ds_read_b128 v[224:227], v0 offset:46080\n"
"ds_read_b128 v[228:231], v0 offset:47104\n"
"s_add_u32 s94, s83, 8192\n"
"buffer_load_dwordx4 v[56:59], v0, s[44:47], s94 offen sc1\n"
"v_rcp_f32 v235, v235\n"
"v_mul_f32 v176, v176, v232\n"
"s_waitcnt lgkmcnt(4)\n"
"v_mfma_f32_32x32x16_bf16 v[144:159], v[208:211], v[80:83], v[144:159]\n"
"s_add_u32 s94, s84, 8192\n"
"buffer_load_dwordx4 v[60:63], v0, s[44:47], s94 offen sc1\n"
"v_mul_f32 v177, v177, v233\n"
"v_mul_f32 v178, v178, v234\n"
"v_mfma_f32_32x32x16_bf16 v[144:159], v[212:215], v[84:87], v[144:159]\n"
"ds_read_b128 v[208:211], v0 offset:48128\n"
"ds_read_b128 v[212:215], v0 offset:49152\n"
"v_mul_f32 v179, v179, v235\n"
"v_cvt_pk_bf16_f32 v244, v176, v177\n"
"v_cvt_pk_bf16_f32 v245, v178, v179\n"
"v_min_u32 v244, -2, v244\n"
"s_waitcnt lgkmcnt(4)\n"
"v_mfma_f32_32x32x16_bf16 v[144:159], v[216:219], v[88:91], v[144:159]\n"
"s_add_u32 s94, s83, 12288\n"
"buffer_load_dwordx4 v[64:67], v0, s[44:47], s94 offen offset:3072 sc1\n"
"v_min_u32 v245, -2, v245\n"
"v_mul_f32 v236, v17, v180\n"
"v_mfma_f32_32x32x16_bf16 v[144:159], v[220:223], v[92:95], v[144:159]\n"
"ds_read_b128 v[216:219], v0 offset:50176\n"
"ds_read_b128 v[220:223], v0 offset:51200\n"
"v_mul_f32 v237, v17, v181\n"
"v_mul_f32 v238, v17, v182\n"
"v_mul_f32 v239, v17, v183\n"
"v_exp_f32 v236, v236\n"
"s_waitcnt lgkmcnt(4)\n"
"v_mfma_f32_32x32x16_bf16 v[144:159], v[224:227], v[96:99], v[144:159]\n"
"s_add_u32 s94, s84, 12288\n"
"buffer_load_dwordx4 v[68:71], v0, s[44:47], s94 offen offset:3072 sc1\n"
"v_exp_f32 v237, v237\n"
"v_mfma_f32_32x32x16_bf16 v[144:159], v[228:231], v[100:103], v[144:159]\n"
"ds_read_b128 v[224:227], v0 offset:52224\n"
"ds_read_b128 v[228:231], v0 offset:53248\n"
"s_add_u32 s94, s83, 20480\n"
"buffer_load_dwordx4 v[72:75], v0, s[44:47], s94 offen offset:2048 sc1\n"
"v_exp_f32 v238, v238\n"
"s_waitcnt lgkmcnt(4)\n"
"v_mfma_f32_32x32x16_bf16 v[144:159], v[208:211], v[104:107], v[144:159]\n"
"v_exp_f32 v239, v239\n"
"v_mul_f32 v180, v180, v188\n"
"v_mul_f32 v181, v181, v189\n"
"v_mfma_f32_32x32x16_bf16 v[144:159], v[212:215], v[108:111], v[144:159]\n"
"ds_read_b128 v[208:211], v0 offset:54272\n"
"ds_read_b128 v[212:215], v0 offset:55296\n"
"s_add_u32 s94, s84, 20480\n"
"buffer_load_dwordx4 v[76:79], v0, s[44:47], s94 offen offset:2048 sc1\n"
"v_mul_f32 v182, v182, v190\n"
"v_mul_f32 v183, v183, v191\n"
"s_waitcnt lgkmcnt(4)\n"
"v_mfma_f32_32x32x16_bf16 v[144:159], v[216:219], v[112:115], v[144:159]\n"
"v_fma_f32 v236, v236, v18, v18\n"
"v_fma_f32 v237, v237, v18, v18\n"
"v_fma_f32 v238, v238, v18, v18\n"
"v_fma_f32 v239, v239, v18, v18\n"
"v_mfma_f32_32x32x16_bf16 v[144:159], v[220:223], v[116:119], v[144:159]\n"
"ds_read_b128 v[216:219], v0 offset:56320\n"
"ds_read_b128 v[220:223], v0 offset:57344\n"
"buffer_load_dwordx4 v[80:83], v0, s[44:47], s83 offen offset:2048 sc1\n"
"v_rcp_f32 v236, v236\n"
"s_waitcnt lgkmcnt(4)\n"
"v_mfma_f32_32x32x16_bf16 v[144:159], v[224:227], v[120:123], v[144:159]\n"
"buffer_load_dwordx4 v[84:87], v0, s[44:47], s84 offen offset:2048 sc1\n"
"v_rcp_f32 v237, v237\n"
"v_mfma_f32_32x32x16_bf16 v[144:159], v[228:231], v[124:127], v[144:159]\n"
"ds_read_b128 v[224:227], v0 offset:58368\n"
"v_rcp_f32 v238, v238\n"
"v_rcp_f32 v239, v239\n"
"v_mul_f32 v180, v180, v236\n"
"s_waitcnt lgkmcnt(3)\n"
"v_mfma_f32_32x32x16_bf16 v[144:159], v[208:211], v[128:131], v[144:159]\n"
"s_add_u32 s94, s83, 8192\n"
"buffer_load_dwordx4 v[88:91], v0, s[44:47], s94 offen offset:1024 sc1\n"
"v_mul_f32 v181, v181, v237\n"
"v_mul_f32 v182, v182, v238\n"
"v_mfma_f32_32x32x16_bf16 v[144:159], v[212:215], v[132:135], v[144:159]\n"
"v_mul_f32 v183, v183, v239\n"
"v_cvt_pk_bf16_f32 v246, v180, v181\n"
"v_cvt_pk_bf16_f32 v247, v182, v183\n"
"v_min_u32 v246, -2, v246\n"
"v_min_u32 v247, -2, v247\n"
"s_waitcnt lgkmcnt(1)\n"
"v_mfma_f32_32x32x16_bf16 v[144:159], v[216:219], v[136:139], v[144:159]\n"
"s_add_u32 s94, s84, 8192\n"
"buffer_load_dwordx4 v[92:95], v0, s[44:47], s94 offen offset:1024 sc1\n"
"s_add_u32 s59, s82, 4096\n"
"v_mfma_f32_32x32x16_bf16 v[144:159], v[220:223], v[140:143], v[144:159]\n"
"s_add_u32 s94, s83, 16384\n"
"buffer_load_dwordx4 v[96:99], v0, s[44:47], s94 offen sc1\n"
"buffer_store_dwordx4 v[244:247], v0, s[44:47], s59 offen offset:1024 sc1\n"
"s_waitcnt lgkmcnt(0)\n"
"v_mfma_f32_32x32x16_bf16 v[144:159], v[224:227], v[20:23], v[144:159]\n"
"v_lshlrev_b32 v160, 16, v8\n"
"v_and_b32 v161, 0xffff0000, v8\n"
"v_lshlrev_b32 v162, 16, v9\n"
"v_and_b32 v163, 0xffff0000, v9\n"
"v_lshlrev_b32 v164, 16, v10\n"
"v_and_b32 v165, 0xffff0000, v10\n"
"v_lshlrev_b32 v166, 16, v11\n"
"v_and_b32 v167, 0xffff0000, v11\n"
"v_lshlrev_b32 v168, 16, v12\n"
"v_and_b32 v169, 0xffff0000, v12\n"
"v_lshlrev_b32 v170, 16, v13\n"
"v_and_b32 v171, 0xffff0000, v13\n"
"v_lshlrev_b32 v172, 16, v14\n"
"v_and_b32 v173, 0xffff0000, v14\n"
"v_lshlrev_b32 v174, 16, v15\n"
"v_and_b32 v175, 0xffff0000, v15\n"
"s_cmp_eq_u32 %[act], 0\n"
"s_cbranch_scc1 L_dn_skip_0\n"
"s_waitcnt vmcnt(22)\n"
"v_max3_u32 v19, v192, v193, v194\n"
"v_max_u32 v19, v19, v195\n"
"s_waitcnt vmcnt(21)\n"
"v_max3_u32 v19, v19, v196, v197\n"
"v_max3_u32 v19, v19, v198, v199\n"
"s_waitcnt vmcnt(20)\n"
"v_max3_u32 v19, v19, v200, v201\n"
"v_max3_u32 v19, v19, v202, v203\n"
"s_waitcnt vmcnt(19)\n"
"v_max3_u32 v19, v19, v204, v205\n"
"v_max3_u32 v19, v19, v206, v207\n"
"s_waitcnt vmcnt(18)\n"
"v_max3_u32 v19, v19, v32, v33\n"
"v_max3_u32 v19, v19, v34, v35\n"
"s_waitcnt vmcnt(16)\n"
"v_max3_u32 v19, v19, v36, v37\n"
"v_max3_u32 v19, v19, v38, v39\n"
"s_waitcnt vmcnt(15)\n"
"v_max3_u32 v19, v19, v40, v41\n"
"v_max3_u32 v19, v19, v42, v43\n"
"s_waitcnt vmcnt(14)\n"
"v_max3_u32 v19, v19, v44, v45\n"
"v_max3_u32 v19, v19, v46, v47\n"
"L_hc0_0:\n"
"v_cmp_eq_u32 vcc, -1, v19\n"
"s_cbranch_vccnz L_h0_s_0\n"
"L_h0_go_0:\n"
"v_mfma_f32_32x32x16_bf16 v[160:175], a[0:3], v[192:195], v[160:175]\n"
"s_add_u32 s94, s84, 16384\n"
"buffer_load_dwordx4 v[100:103], v0, s[44:47], s94 offen sc1\n"
"v_mul_f32 v232, v17, v144\n"
"v_mul_f32 v233, v17, v145\n"
"v_mul_f32 v234, v17, v146\n"
"v_mfma_f32_32x32x16_bf16 v[176:191], a[0:3], v[196:199], 0\n"
"v_mul_f32 v235, v17, v147\n"
"v_exp_f32 v232, v232\n"
"v_exp_f32 v233, v233\n"
"v_mfma_f32_32x32x16_bf16 a[224:239], a[4:7], v[192:195], 0\n"
"s_add_u32 s94, s83, 20480\n"
"buffer_load_dwordx4 v[104:107], v0, s[44:47], s94 offen offset:3072 sc1\n"
"v_exp_f32 v234, v234\n"
"v_exp_f32 v235, v235\n"
"v_mfma_f32_32x32x16_bf16 a[240:255], a[4:7], v[196:199], 0\n"
"v_mul_f32 v144, v144, v152\n"
"v_mul_f32 v145, v145, v153\n"
"v_mul_f32 v146, v146, v154\n"
"v_mul_f32 v147, v147, v155\n"
"v_fma_f32 v232, v232, v18, v18\n"
"v_fma_f32 v233, v233, v18, v18\n"
"v_mfma_f32_32x32x16_bf16 v[160:175], a[8:11], v[200:203], v[160:175]\n"
"s_add_u32 s94, s84, 20480\n"
"buffer_load_dwordx4 v[108:111], v0, s[44:47], s94 offen offset:3072 sc1\n"
"v_fma_f32 v234, v234, v18, v18\n"
"v_fma_f32 v235, v235, v18, v18\n"
"v_rcp_f32 v232, v232\n"
"v_mfma_f32_32x32x16_bf16 v[176:191], a[8:11], v[204:207], v[176:191]\n"
"v_rcp_f32 v233, v233\n"
"v_rcp_f32 v234, v234\n"
"v_rcp_f32 v235, v235\n"
"v_mfma_f32_32x32x16_bf16 a[224:239], a[12:15], v[200:203], a[224:239]\n"
"buffer_load_dwordx4 v[112:115], v0, s[44:47], s83 offen offset:3072 sc1\n"
"v_mul_f32 v144, v144, v232\n"
"v_mul_f32 v145, v145, v233\n"
"v_mul_f32 v146, v146, v234\n"
"v_mfma_f32_32x32x16_bf16 a[240:255], a[12:15], v[204:207], a[240:255]\n"
"v_mul_f32 v147, v147, v235\n"
"v_cvt_pk_bf16_f32 v240, v144, v145\n"
"v_cvt_pk_bf16_f32 v241, v146, v147\n"
"v_min_u32 v240, -2, v240\n"
"v_min_u32 v241, -2, v241\n"
"v_mul_f32 v236, v17, v148\n"
"v_mfma_f32_32x32x16_bf16 v[160:175], a[16:19], v[32:35], v[160:175]\n"
"buffer_load_dwordx4 v[116:119], v0, s[44:47], s84 offen offset:3072 sc1\n"
"v_mul_f32 v237, v17, v149\n"
"v_mul_f32 v238, v17, v150\n"
"v_mul_f32 v239, v17, v151\n"
"v_mfma_f32_32x32x16_bf16 v[176:191], a[16:19], v[36:39], v[176:191]\n"
"s_add_u32 s94, s83, 8192\n"
"buffer_load_dwordx4 v[120:123], v0, s[44:47], s94 offen offset:2048 sc1\n"
"v_exp_f32 v236, v236\n"
"v_exp_f32 v237, v237\n"
"v_mfma_f32_32x32x16_bf16 a[224:239], a[20:23], v[32:35], a[224:239]\n"
"v_exp_f32 v238, v238\n"
"v_exp_f32 v239, v239\n"
"v_mul_f32 v148, v148, v156\n"
"v_mul_f32 v149, v149, v157\n"
"v_mfma_f32_32x32x16_bf16 a[240:255], a[20:23], v[36:39], a[240:255]\n"
"s_add_u32 s94, s84, 8192\n"
"buffer_load_dwordx4 v[124:127], v0, s[44:47], s94 offen offset:2048 sc1\n"
"v_mul_f32 v150, v150, v158\n"
"v_mul_f32 v151, v151, v159\n"
"v_fma_f32 v236, v236, v18, v18\n"
"v_fma_f32 v237, v237, v18, v18\n"
"v_mfma_f32_32x32x16_bf16 v[160:175], a[24:27], v[40:43], v[160:175]\n"
"s_waitcnt vmcnt(20)\n"
"v_max3_u32 v19, v48, v49, v50\n"
"v_max_u32 v19, v19, v51\n"
"s_waitcnt vmcnt(19)\n"
"v_max3_u32 v19, v19, v52, v53\n"
"v_max3_u32 v19, v19, v54, v55\n"
"v_fma_f32 v238, v238, v18, v18\n"
"v_fma_f32 v239, v239, v18, v18\n"
"v_rcp_f32 v236, v236\n"
"v_rcp_f32 v237, v237\n"
"v_mfma_f32_32x32x16_bf16 v[176:191], a[24:27], v[44:47], v[176:191]\n"
"s_waitcnt vmcnt(18)\n"
"v_max3_u32 v19, v19, v56, v57\n"
"v_max3_u32 v19, v19, v58, v59\n"
"s_waitcnt vmcnt(17)\n"
"v_max3_u32 v19, v19, v60, v61\n"
"v_max3_u32 v19, v19, v62, v63\n"
"s_add_u32 s94, s83, 16384\n"
"buffer_load_dwordx4 v[128:131], v0, s[44:47], s94 offen offset:1024 sc1\n"
"v_rcp_f32 v238, v238\n"
"v_mfma_f32_32x32x16_bf16 a[224:239], a[28:31], v[40:43], a[224:239]\n"
"s_waitcnt vmcnt(17)\n"
"v_max3_u32 v19, v19, v64, v65\n"
"v_max3_u32 v19, v19, v66, v67\n"
"s_waitcnt vmcnt(16)\n"
"v_max3_u32 v19, v19, v68, v69\n"
"v_max3_u32 v19, v19, v70, v71\n"
"v_rcp_f32 v239, v239\n"
"v_mul_f32 v148, v148, v236\n"
"v_mul_f32 v149, v149, v237\n"
"v_mul_f32 v150, v150, v238\n"
"v_mul_f32 v151, v151, v239\n"
"v_cvt_pk_bf16_f32 v242, v148, v149\n"
"v_mfma_f32_32x32x16_bf16 a[240:255], a[28:31], v[44:47], a[240:255]\n"
"s_waitcnt vmcnt(15)\n"
"v_max3_u32 v19, v19, v72, v73\n"
"v_max3_u32 v19, v19, v74, v75\n"
"s_waitcnt vmcnt(14)\n"
"v_max3_u32 v19, v19, v76, v77\n"
"v_max3_u32 v19, v19, v78, v79\n"
"L_hc1_0:\n"
"v_cmp_eq_u32 vcc, -1, v19\n"
"s_cbranch_vccnz L_h1_s_0\n"
"L_h1_go_0:\n"
"s_add_u32 s94, s84, 16384\n"
"buffer_load_dwordx4 v[132:135], v0, s[44:47], s94 offen offset:1024 sc1\n"
"v_cvt_pk_bf16_f32 v243, v150, v151\n"
"v_min_u32 v242, -2, v242\n"
"v_min_u32 v243, -2, v243\n"
"v_mfma_f32_32x32x16_bf16 v[160:175], a[32:35], v[48:51], v[160:175]\n"
"s_add_u32 s59, s82, 4096\n"
"buffer_store_dwordx4 v[240:243], v0, s[44:47], s59 offen offset:2048 sc1\n"
"v_mfma_f32_32x32x16_bf16 v[176:191], a[32:35], v[52:55], v[176:191]\n"
"s_add_u32 s94, s83, 24576\n"
"buffer_load_dwordx4 v[136:139], v0, s[44:47], s94 offen sc1\n"
"v_mfma_f32_32x32x16_bf16 a[224:239], a[36:39], v[48:51], a[224:239]\n"
"v_mfma_f32_32x32x16_bf16 a[240:255], a[36:39], v[52:55], a[240:255]\n"
"s_add_u32 s94, s84, 24576\n"
"buffer_load_dwordx4 v[140:143], v0, s[44:47], s94 offen sc1\n"
"v_mfma_f32_32x32x16_bf16 v[160:175], a[40:43], v[56:59], v[160:175]\n"
"s_barrier\n"
"s_add_u32 s94, s83, 4096\n"
"buffer_load_dwordx4 v[208:211], v0, s[44:47], s94 offen sc1\n"
"v_mfma_f32_32x32x16_bf16 v[176:191], a[40:43], v[60:63], v[176:191]\n"
"v_mfma_f32_32x32x16_bf16 a[224:239], a[44:47], v[56:59], a[224:239]\n"
"s_add_u32 s94, s84, 4096\n"
"buffer_load_dwordx4 v[212:215], v0, s[44:47], s94 offen sc1\n"
"v_mfma_f32_32x32x16_bf16 a[240:255], a[44:47], v[60:63], a[240:255]\n"
"v_mfma_f32_32x32x16_bf16 v[160:175], a[48:51], v[64:67], v[160:175]\n"
"s_add_u32 s94, s83, 8192\n"
"buffer_load_dwordx4 v[216:219], v0, s[44:47], s94 offen offset:3072 sc1\n"
"v_mfma_f32_32x32x16_bf16 v[176:191], a[48:51], v[68:71], v[176:191]\n"
"v_mfma_f32_32x32x16_bf16 a[224:239], a[52:55], v[64:67], a[224:239]\n"
"s_add_u32 s94, s84, 8192\n"
"buffer_load_dwordx4 v[220:223], v0, s[44:47], s94 offen offset:3072 sc1\n"
"v_mfma_f32_32x32x16_bf16 a[240:255], a[52:55], v[68:71], a[240:255]\n"
"v_mfma_f32_32x32x16_bf16 v[160:175], a[56:59], v[72:75], v[160:175]\n"
"s_waitcnt vmcnt(21)\n"
"v_max3_u32 v19, v80, v81, v82\n"
"v_max_u32 v19, v19, v83\n"
"s_waitcnt vmcnt(20)\n"
"v_max3_u32 v19, v19, v84, v85\n"
"v_max3_u32 v19, v19, v86, v87\n"
"s_add_u32 s94, s83, 16384\n"
"buffer_load_dwordx4 v[224:227], v0, s[44:47], s94 offen offset:2048 sc1\n"
"v_mfma_f32_32x32x16_bf16 v[176:191], a[56:59], v[76:79], v[176:191]\n"
"s_waitcnt vmcnt(20)\n"
"v_max3_u32 v19, v19, v88, v89\n"
"v_max3_u32 v19, v19, v90, v91\n"
"s_waitcnt vmcnt(19)\n"
"v_max3_u32 v19, v19, v92, v93\n"
"v_max3_u32 v19, v19, v94, v95\n"
"v_mfma_f32_32x32x16_bf16 a[224:239], a[60:63], v[72:75], a[224:239]\n"
"s_waitcnt vmcnt(18)\n"
"v_max3_u32 v19, v19, v96, v97\n"
"v_max3_u32 v19, v19, v98, v99\n"
"s_waitcnt vmcnt(16)\n"
"v_max3_u32 v19, v19, v100, v101\n"
"v_max3_u32 v19, v19, v102, v103\n"
"s_add_u32 s94, s84, 16384\n"
"buffer_load_dwordx4 v[228:231], v0, s[44:47], s94 offen offset:2048 sc1\n"
"v_mfma_f32_32x32x16_bf16 a[240:255], a[60:63], v[76:79], a[240:255]\n"
"s_waitcnt vmcnt(16)\n"
"v_max3_u32 v19, v19, v104, v105\n"
"v_max3_u32 v19, v19, v106, v107\n"
"s_waitcnt vmcnt(15)\n"
"v_max3_u32 v19, v19, v108, v109\n"
"v_max3_u32 v19, v19, v110, v111\n"
"L_hc2_0:\n"
"v_cmp_eq_u32 vcc, -1, v19\n"
"s_cbranch_vccnz L_h2_s_0\n"
"L_h2_go_0:\n"
"s_add_u32 s94, s83, 24576\n"
"buffer_load_dwordx4 v[232:235], v0, s[44:47], s94 offen offset:1024 sc1\n"
"v_mfma_f32_32x32x16_bf16 v[160:175], a[64:67], v[80:83], v[160:175]\n"
"v_mfma_f32_32x32x16_bf16 v[176:191], a[64:67], v[84:87], v[176:191]\n"
"s_add_u32 s94, s84, 24576\n"
"buffer_load_dwordx4 v[236:239], v0, s[44:47], s94 offen offset:1024 sc1\n"
"v_mfma_f32_32x32x16_bf16 a[224:239], a[68:71], v[80:83], a[224:239]\n"
"v_mfma_f32_32x32x16_bf16 a[240:255], a[68:71], v[84:87], a[240:255]\n"
"s_add_u32 s94, s83, 4096\n"
"buffer_load_dwordx4 v[240:243], v0, s[44:47], s94 offen offset:1024 sc1\n"
"v_mfma_f32_32x32x16_bf16 v[160:175], a[72:75], v[88:91], v[160:175]\n"
"v_mfma_f32_32x32x16_bf16 v[176:191], a[72:75], v[92:95], v[176:191]\n"
"s_add_u32 s94, s84, 4096\n"
"buffer_load_dwordx4 v[244:247], v0, s[44:47], s94 offen offset:1024 sc1\n"
"v_mfma_f32_32x32x16_bf16 a[224:239], a[76:79], v[88:91], a[224:239]\n"
"v_mfma_f32_32x32x16_bf16 a[240:255], a[76:79], v[92:95], a[240:255]\n"
"s_add_u32 s94, s83, 12288\n"
"buffer_load_dwordx4 v[248:251], v0, s[44:47], s94 offen sc1\n"
"v_mfma_f32_32x32x16_bf16 v[160:175], a[80:83], v[96:99], v[160:175]\n"
"s_add_u32 s94, s84, 12288\n"
"buffer_load_dwordx4 v[252:255], v0, s[44:47], s94 offen sc1\n"
"v_mfma_f32_32x32x16_bf16 v[176:191], a[80:83], v[100:103], v[176:191]\n"
"v_mfma_f32_32x32x16_bf16 a[224:239], a[84:87], v[96:99], a[224:239]\n"
"s_add_u32 s94, s83, 16384\n"
"buffer_load_dwordx4 v[144:147], v0, s[44:47], s94 offen offset:3072 sc1\n"
"v_mfma_f32_32x32x16_bf16 a[240:255], a[84:87], v[100:103], a[240:255]\n"
"v_mfma_f32_32x32x16_bf16 v[160:175], a[88:91], v[104:107], v[160:175]\n"
"s_waitcnt vmcnt(21)\n"
"v_max3_u32 v19, v112, v113, v114\n"
"v_max_u32 v19, v19, v115\n"
"s_waitcnt vmcnt(20)\n"
"v_max3_u32 v19, v19, v116, v117\n"
"v_max3_u32 v19, v19, v118, v119\n"
"s_add_u32 s94, s84, 16384\n"
"buffer_load_dwordx4 v[148:151], v0, s[44:47], s94 offen offset:3072 sc1\n"
"v_mfma_f32_32x32x16_bf16 v[176:191], a[88:91], v[108:111], v[176:191]\n"
"s_waitcnt vmcnt(20)\n"
"v_max3_u32 v19, v19, v120, v121\n"
"v_max3_u32 v19, v19, v122, v123\n"
"s_waitcnt vmcnt(19)\n"
"v_max3_u32 v19, v19, v124, v125\n"
"v_max3_u32 v19, v19, v126, v127\n"
"v_mfma_f32_32x32x16_bf16 a[224:239], a[92:95], v[104:107], a[224:239]\n"
"s_waitcnt vmcnt(18)\n"
"v_max3_u32 v19, v19, v128, v129\n"
"v_max3_u32 v19, v19, v130, v131\n"
"s_waitcnt vmcnt(17)\n"
"v_max3_u32 v19, v19, v132, v133\n"
"v_max3_u32 v19, v19, v134, v135\n"
"s_add_u32 s94, s83, 24576\n"
"buffer_load_dwordx4 v[152:155], v0, s[44:47], s94 offen offset:2048 sc1\n"
"v_mfma_f32_32x32x16_bf16 a[240:255], a[92:95], v[108:111], a[240:255]\n"
"s_waitcnt vmcnt(16)\n"
"v_max3_u32 v19, v19, v136, v137\n"
"v_max3_u32 v19, v19, v138, v139\n"
"s_waitcnt vmcnt(15)\n"
"v_max3_u32 v19, v19, v140, v141\n"
"v_max3_u32 v19, v19, v142, v143\n"
"L_hc3_0:\n"
"v_cmp_eq_u32 vcc, -1, v19\n"
"s_cbranch_vccnz L_h3_s_0\n"
"L_h3_go_0:\n"
"v_mfma_f32_32x32x16_bf16 v[160:175], a[96:99], v[112:115], v[160:175]\n"
"s_add_u32 s94, s84, 24576\n"
"buffer_load_dwordx4 v[156:159], v0, s[44:47], s94 offen offset:2048 sc1\n"
"v_mfma_f32_32x32x16_bf16 v[176:191], a[96:99], v[116:119], v[176:191]\n"
"v_mfma_f32_32x32x16_bf16 a[224:239], a[100:103], v[112:115], a[224:239]\n"
"v_mfma_f32_32x32x16_bf16 a[240:255], a[100:103], v[116:119], a[240:255]\n"
"v_mfma_f32_32x32x16_bf16 v[160:175], a[104:107], v[120:123], v[160:175]\n"
"v_mfma_f32_32x32x16_bf16 v[176:191], a[104:107], v[124:127], v[176:191]\n"
"s_add_u32 s94, s83, 4096\n"
"buffer_load_dwordx4 v[192:195], v0, s[44:47], s94 offen offset:2048 sc1\n"
"v_mfma_f32_32x32x16_bf16 a[224:239], a[108:111], v[120:123], a[224:239]\n"
"s_add_u32 s94, s84, 4096\n"
"buffer_load_dwordx4 v[196:199], v0, s[44:47], s94 offen offset:2048 sc1\n"
"v_mfma_f32_32x32x16_bf16 a[240:255], a[108:111], v[124:127], a[240:255]\n"
"v_mfma_f32_32x32x16_bf16 v[160:175], a[112:115], v[128:131], v[160:175]\n"
"s_add_u32 s94, s83, 12288\n"
"buffer_load_dwordx4 v[200:203], v0, s[44:47], s94 offen offset:1024 sc1\n"
"v_mfma_f32_32x32x16_bf16 v[176:191], a[112:115], v[132:135], v[176:191]\n"
"v_mfma_f32_32x32x16_bf16 a[224:239], a[116:119], v[128:131], a[224:239]\n"
"s_add_u32 s94, s84, 12288\n"
"buffer_load_dwordx4 v[204:207], v0, s[44:47], s94 offen offset:1024 sc1\n"
"v_mfma_f32_32x32x16_bf16 a[240:255], a[116:119], v[132:135], a[240:255]\n"
"v_mfma_f32_32x32x16_bf16 v[160:175], a[120:123], v[136:139], v[160:175]\n"
"s_waitcnt vmcnt(19)\n"
"v_max3_u32 v19, v208, v209, v210\n"
"v_max_u32 v19, v19, v211\n"
"s_waitcnt vmcnt(18)\n"
"v_max3_u32 v19, v19, v212, v213\n"
"v_max3_u32 v19, v19, v214, v215\n"
"s_add_u32 s94, s83, 20480\n"
"buffer_load_dwordx4 v[32:35], v0, s[44:47], s94 offen sc1\n"
"v_mfma_f32_32x32x16_bf16 v[176:191], a[120:123], v[140:143], v[176:191]\n"
"s_waitcnt vmcnt(18)\n"
"v_max3_u32 v19, v19, v216, v217\n"
"v_max3_u32 v19, v19, v218, v219\n"
"s_waitcnt vmcnt(17)\n"
"v_max3_u32 v19, v19, v220, v221\n"
"v_max3_u32 v19, v19, v222, v223\n"
"v_mfma_f32_32x32x16_bf16 a[224:239], a[124:127], v[136:139], a[224:239]\n"
"s_waitcnt vmcnt(16)\n"
"v_max3_u32 v19, v19, v224, v225\n"
"v_max3_u32 v19, v19, v226, v227\n"
"s_waitcnt vmcnt(15)\n"
"v_max3_u32 v19, v19, v228, v229\n"
"v_max3_u32 v19, v19, v230, v231\n"
"s_add_u32 s94, s84, 20480\n"
"buffer_load_dwordx4 v[36:39], v0, s[44:47], s94 offen sc1\n"
"v_mfma_f32_32x32x16_bf16 a[240:255], a[124:127], v[140:143], a[240:255]\n"
"s_waitcnt vmcnt(15)\n"
"v_max3_u32 v19, v19, v232, v233\n"
"v_max3_u32 v19, v19, v234, v235\n"
"s_waitcnt vmcnt(14)\n"
"v_max3_u32 v19, v19, v236, v237\n"
"v_max3_u32 v19, v19, v238, v239\n"
"L_hc4_0:\n"
"v_cmp_eq_u32 vcc, -1, v19\n"
"s_cbranch_vccnz L_h4_s_0\n"
"L_h4_go_0:\n"
"s_add_u32 s94, s83, 24576\n"
"buffer_load_dwordx4 v[40:43], v0, s[44:47], s94 offen offset:3072 sc1\n"
"v_mfma_f32_32x32x16_bf16 v[176:191], a[128:131], v[212:215], v[176:191]\n"
"v_mfma_f32_32x32x16_bf16 a[224:239], a[132:135], v[208:211], a[224:239]\n"
"s_add_u32 s94, s84, 24576\n"
"buffer_load_dwordx4 v[44:47], v0, s[44:47], s94 offen offset:3072 sc1\n"
"v_mfma_f32_32x32x16_bf16 a[240:255], a[132:135], v[212:215], a[240:255]\n"
"v_mfma_f32_32x32x16_bf16 v[176:191], a[136:139], v[220:223], v[176:191]\n"
"s_add_u32 m0, s92, 0\n"
"s_add_u32 s95, s71, 0\n"
"buffer_load_dwordx4 v0, s[40:43], s95 offen lds\n"
"v_mfma_f32_32x32x16_bf16 a[224:239], a[140:143], v[216:219], a[224:239]\n"
"v_mfma_f32_32x32x16_bf16 a[240:255], a[140:143], v[220:223], a[240:255]\n"
"buffer_load_dwordx4 v0, s[40:43], s95 offen offset:1024 lds\n"
"v_mfma_f32_32x32x16_bf16 v[176:191], a[144:147], v[228:231], v[176:191]\n"
"v_mfma_f32_32x32x16_bf16 a[224:239], a[148:151], v[224:227], a[224:239]\n"
"buffer_load_dwordx4 v0, s[40:43], s95 offen offset:2048 lds\n"
"v_mfma_f32_32x32x16_bf16 a[240:255], a[148:151], v[228:231], a[240:255]\n"
"s_waitcnt vmcnt(18)\n"
"v_max3_u32 v19, v240, v241, v242\n"
"v_max_u32 v19, v19, v243\n"
"s_waitcnt vmcnt(17)\n"
"v_max3_u32 v19, v19, v244, v245\n"
"v_max3_u32 v19, v19, v246, v247\n"
"buffer_load_dwordx4 v0, s[40:43], s95 offen offset:3072 lds\n"
"v_mfma_f32_32x32x16_bf16 v[176:191], a[152:155], v[236:239], v[176:191]\n"
"s_waitcnt vmcnt(17)\n"
"v_max3_u32 v19, v19, v248, v249\n"
"v_max3_u32 v19, v19, v250, v251\n"
"s_waitcnt vmcnt(16)\n"
"v_max3_u32 v19, v19, v252, v253\n"
"v_max3_u32 v19, v19, v254, v255\n"
"v_mfma_f32_32x32x16_bf16 a[224:239], a[156:159], v[232:235], a[224:239]\n"
"s_waitcnt vmcnt(15)\n"
"v_max3_u32 v19, v19, v144, v145\n"
"v_max3_u32 v19, v19, v146, v147\n"
"s_waitcnt vmcnt(14)\n"
"v_max3_u32 v19, v19, v148, v149\n"
"v_max3_u32 v19, v19, v150, v151\n"
"s_add_u32 m0, s92, 4096\n"
"s_add_u32 s95, s71, 4096\n"
"buffer_load_dwordx4 v0, s[40:43], s95 offen lds\n"
"v_mfma_f32_32x32x16_bf16 a[240:255], a[156:159], v[236:239], a[240:255]\n"
"s_waitcnt vmcnt(14)\n"
"v_max3_u32 v19, v19, v152, v153\n"
"v_max3_u32 v19, v19, v154, v155\n"
"s_waitcnt vmcnt(13)\n"
"v_max3_u32 v19, v19, v156, v157\n"
"v_max3_u32 v19, v19, v158, v159\n"
"L_hc5_0:\n"
"v_cmp_eq_u32 vcc, -1, v19\n"
"s_cbranch_vccnz L_h5_s_0\n"
"L_h5_go_0:\n"
"v_mfma_f32_32x32x16_bf16 v[176:191], a[160:163], v[244:247], v[176:191]\n"
"buffer_load_dwordx4 v0, s[40:43], s95 offen offset:1024 lds\n"
"v_mfma_f32_32x32x16_bf16 a[224:239], a[164:167], v[240:243], a[224:239]\n"
"v_mfma_f32_32x32x16_bf16 a[240:255], a[164:167], v[244:247], a[240:255]\n"
"buffer_load_dwordx4 v0, s[40:43], s95 offen offset:2048 lds\n"
"v_mfma_f32_32x32x16_bf16 v[176:191], a[168:171], v[252:255], v[176:191]\n"
"v_mfma_f32_32x32x16_bf16 a[224:239], a[172:175], v[248:251], a[224:239]\n"
"buffer_load_dwordx4 v0, s[40:43], s95 offen offset:3072 lds\n"
"v_mfma_f32_32x32x16_bf16 a[240:255], a[172:175], v[252:255], a[240:255]\n"
"v_mfma_f32_32x32x16_bf16 v[176:191], a[176:179], v[148:151], v[176:191]\n"
"v_mfma_f32_32x32x16_bf16 a[224:239], a[180:183], v[144:147], a[224:239]\n"
"v_mfma_f32_32x32x16_bf16 a[240:255], a[180:183], v[148:151], a[240:255]\n"
"s_waitcnt vmcnt(15)\n"
"v_max3_u32 v19, v192, v193, v194\n"
"v_max_u32 v19, v19, v195\n"
"s_waitcnt vmcnt(14)\n"
"v_max3_u32 v19, v19, v196, v197\n"
"v_max3_u32 v19, v19, v198, v199\n"
"v_mfma_f32_32x32x16_bf16 v[176:191], a[184:187], v[156:159], v[176:191]\n"
"s_waitcnt vmcnt(13)\n"
"v_max3_u32 v19, v19, v200, v201\n"
"v_max3_u32 v19, v19, v202, v203\n"
"s_waitcnt vmcnt(12)\n"
"v_max3_u32 v19, v19, v204, v205\n"
"v_max3_u32 v19, v19, v206, v207\n"
"v_mfma_f32_32x32x16_bf16 a[224:239], a[188:191], v[152:155], a[224:239]\n"
"s_waitcnt vmcnt(11)\n"
"v_max3_u32 v19, v19, v32, v33\n"
"v_max3_u32 v19, v19, v34, v35\n"
"s_waitcnt vmcnt(10)\n"
"v_max3_u32 v19, v19, v36, v37\n"
"v_max3_u32 v19, v19, v38, v39\n"
"v_mfma_f32_32x32x16_bf16 a[240:255], a[188:191], v[156:159], a[240:255]\n"
"s_waitcnt vmcnt(9)\n"
"v_max3_u32 v19, v19, v40, v41\n"
"v_max3_u32 v19, v19, v42, v43\n"
"s_waitcnt vmcnt(8)\n"
"v_max3_u32 v19, v19, v44, v45\n"
"v_max3_u32 v19, v19, v46, v47\n"
"L_hc6_0:\n"
"v_cmp_eq_u32 vcc, -1, v19\n"
"s_cbranch_vccnz L_h6_s_0\n"
"L_h6_go_0:\n"
"v_mfma_f32_32x32x16_bf16 a[240:255], a[196:199], v[196:199], a[240:255]\n"
"v_mfma_f32_32x32x16_bf16 a[240:255], a[204:207], v[204:207], a[240:255]\n"
"v_mfma_f32_32x32x16_bf16 a[240:255], a[212:215], v[36:39], a[240:255]\n"
"v_mfma_f32_32x32x16_bf16 a[240:255], a[220:223], v[44:47], a[240:255]\n"
"v_mfma_f32_32x32x16_bf16 a[224:239], a[196:199], v[192:195], a[224:239]\n"
"v_mfma_f32_32x32x16_bf16 a[224:239], a[204:207], v[200:203], a[224:239]\n"
"v_mfma_f32_32x32x16_bf16 a[224:239], a[212:215], v[32:35], a[224:239]\n"
"v_mfma_f32_32x32x16_bf16 a[224:239], a[220:223], v[40:43], a[224:239]\n"
"s_nop 7\n"
"ds_write_b128 v24, a[240:243]\n"
"v_mfma_f32_32x32x16_bf16 v[176:191], a[192:195], v[196:199], v[176:191]\n"
"ds_write_b128 v24, a[244:247] offset:1024\n"
"v_mfma_f32_32x32x16_bf16 v[176:191], a[200:203], v[204:207], v[176:191]\n"
"ds_write_b128 v24, a[248:251] offset:2048\n"
"v_mfma_f32_32x32x16_bf16 v[176:191], a[208:211], v[36:39], v[176:191]\n"
"ds_write_b128 v24, a[252:255] offset:3072\n"
"v_mfma_f32_32x32x16_bf16 v[176:191], a[216:219], v[44:47], v[176:191]\n"
"ds_write_b128 v7, a[224:227]\n"
"v_mfma_f32_32x32x16_bf16 v[160:175], a[128:131], v[208:211], v[160:175]\n"
"ds_write_b128 v7, a[228:231] offset:1024\n"
"v_mfma_f32_32x32x16_bf16 v[160:175], a[136:139], v[216:219], v[160:175]\n"
"ds_write_b128 v7, a[232:235] offset:2048\n"
"v_mfma_f32_32x32x16_bf16 v[160:175], a[144:147], v[224:227], v[160:175]\n"
"ds_write_b128 v7, a[236:239] offset:3072\n"
"v_mfma_f32_32x32x16_bf16 v[160:175], a[152:155], v[232:235], v[160:175]\n"
"s_nop 3\n"
"ds_write_b128 v6, v[176:179]\n"
"ds_write_b128 v6, v[180:183] offset:1024\n"
"ds_write_b128 v6, v[184:187] offset:2048\n"
"ds_write_b128 v6, v[188:191] offset:3072\n"
"s_waitcnt lgkmcnt(0)\n"
"s_barrier\n"
"ds_read_b128 v[48:51], v5\n"
"ds_read_b128 v[64:67], v5 offset:4096\n"
"ds_read_b128 v[80:83], v5 offset:8192\n"
"ds_read_b128 v[52:55], v5 offset:1024\n"
"ds_read_b128 v[68:71], v5 offset:5120\n"
"ds_read_b128 v[84:87], v5 offset:9216\n"
"ds_read_b128 v[56:59], v5 offset:2048\n"
"ds_read_b128 v[72:75], v5 offset:6144\n"
"ds_read_b128 v[88:91], v5 offset:10240\n"
"ds_read_b128 v[60:63], v5 offset:3072\n"
"ds_read_b128 v[76:79], v5 offset:7168\n"
"ds_read_b128 v[92:95], v5 offset:11264\n"
"v_mfma_f32_32x32x16_bf16 v[160:175], a[160:163], v[240:243], v[160:175]\n"
"v_mfma_f32_32x32x16_bf16 v[160:175], a[168:171], v[248:251], v[160:175]\n"
"v_mfma_f32_32x32x16_bf16 v[160:175], a[176:179], v[144:147], v[160:175]\n"
"s_waitcnt lgkmcnt(10)\n"
"v_add_f32 v48, v48, v64\n"
"v_add_f32 v49, v49, v65\n"
"v_add_f32 v50, v50, v66\n"
"v_add_f32 v51, v51, v67\n"
"s_waitcnt lgkmcnt(9)\n"
"v_add_f32 v48, v48, v80\n"
"v_add_f32 v49, v49, v81\n"
"v_add_f32 v50, v50, v82\n"
"v_add_f32 v51, v51, v83\n"
"s_waitcnt lgkmcnt(7)\n"
"v_add_f32 v52, v52, v68\n"
"v_mfma_f32_32x32x16_bf16 v[160:175], a[184:187], v[152:155], v[160:175]\n"
"v_add_f32 v53, v53, v69\n"
"v_add_f32 v54, v54, v70\n"
"v_add_f32 v55, v55, v71\n"
"s_waitcnt lgkmcnt(6)\n"
"v_add_f32 v52, v52, v84\n"
"v_add_f32 v53, v53, v85\n"
"v_add_f32 v54, v54, v86\n"
"v_add_f32 v55, v55, v87\n"
"s_waitcnt lgkmcnt(4)\n"
"v_add_f32 v56, v56, v72\n"
"v_mfma_f32_32x32x16_bf16 v[160:175], a[192:195], v[192:195], v[160:175]\n"
"v_add_f32 v57, v57, v73\n"
"v_add_f32 v58, v58, v74\n"
"v_add_f32 v59, v59, v75\n"
"s_waitcnt lgkmcnt(3)\n"
"v_add_f32 v56, v56, v88\n"
"v_add_f32 v57, v57, v89\n"
"v_add_f32 v58, v58, v90\n"
"v_add_f32 v59, v59, v91\n"
"s_waitcnt lgkmcnt(1)\n"
"v_add_f32 v60, v60, v76\n"
"v_mfma_f32_32x32x16_bf16 v[160:175], a[200:203], v[200:203], v[160:175]\n"
"v_add_f32 v61, v61, v77\n"
"v_add_f32 v62, v62, v78\n"
"v_add_f32 v63, v63, v79\n"
"s_waitcnt lgkmcnt(0)\n"
"v_add_f32 v60, v60, v92\n"
"v_add_f32 v61, v61, v93\n"
"v_add_f32 v62, v62, v94\n"
"v_add_f32 v63, v63, v95\n"
"v_mfma_f32_32x32x16_bf16 v[160:175], a[208:211], v[32:35], v[160:175]\n"
"v_mfma_f32_32x32x16_bf16 v[160:175], a[216:219], v[40:43], v[160:175]\n"
"s_nop 11\n"
"v_pk_add_f32 v[160:161], v[160:161], v[48:49]\n"
"v_pk_add_f32 v[162:163], v[162:163], v[50:51]\n"
"v_cvt_pk_bf16_f32 v8, v160, v161\n"
"v_cvt_pk_bf16_f32 v9, v162, v163\n"
"v_min_u32 v8, -2, v8\n"
"v_min_u32 v9, -2, v9\n"
"s_add_u32 s60, s78, 14614528\n"
"v_pk_add_f32 v[164:165], v[164:165], v[52:53]\n"
"v_pk_add_f32 v[166:167], v[166:167], v[54:55]\n"
"v_cvt_pk_bf16_f32 v10, v164, v165\n"
"v_cvt_pk_bf16_f32 v11, v166, v167\n"
"v_min_u32 v10, -2, v10\n"
"v_min_u32 v11, -2, v11\n"
"buffer_store_dwordx4 v[8:11], v0, s[44:47], s60 offen sc1\n"
"v_pk_add_f32 v[168:169], v[168:169], v[56:57]\n"
"v_pk_add_f32 v[170:171], v[170:171], v[58:59]\n"
"v_cvt_pk_bf16_f32 v12, v168, v169\n"
"v_cvt_pk_bf16_f32 v13, v170, v171\n"
"v_min_u32 v12, -2, v12\n"
"v_min_u32 v13, -2, v13\n"
"v_pk_add_f32 v[172:173], v[172:173], v[60:61]\n"
"v_pk_add_f32 v[174:175], v[174:175], v[62:63]\n"
"v_cvt_pk_bf16_f32 v14, v172, v173\n"
"v_cvt_pk_bf16_f32 v15, v174, v175\n"
"v_min_u32 v14, -2, v14\n"
"v_min_u32 v15, -2, v15\n"
"buffer_store_dwordx4 v[12:15], v0, s[44:47], s60 offen offset:1024 sc1\n"
"v_mov_b32 v240, 0\n"
"v_mov_b32 v241, 0\n"
"v_dot2c_f32_bf16 v240, 0x3f803f80, v8\n"
"v_dot2c_f32_bf16 v241, v8, v8\n"
"v_dot2c_f32_bf16 v240, 0x3f803f80, v9\n"
"v_dot2c_f32_bf16 v241, v9, v9\n"
"v_dot2c_f32_bf16 v240, 0x3f803f80, v10\n"
"v_dot2c_f32_bf16 v241, v10, v10\n"
"v_dot2c_f32_bf16 v240, 0x3f803f80, v11\n"
"v_dot2c_f32_bf16 v241, v11, v11\n"
"v_dot2c_f32_bf16 v240, 0x3f803f80, v12\n"
"v_dot2c_f32_bf16 v241, v12, v12\n"
"v_dot2c_f32_bf16 v240, 0x3f803f80, v13\n"
"v_dot2c_f32_bf16 v241, v13, v13\n"
"v_dot2c_f32_bf16 v240, 0x3f803f80, v14\n"
"v_dot2c_f32_bf16 v241, v14, v14\n"
"v_dot2c_f32_bf16 v240, 0x3f803f80, v15\n"
"v_dot2c_f32_bf16 v241, v15, v15\n"
"s_nop 1\n"
"v_mov_b32 v242, v240\n"
"v_mov_b32 v243, v241\n"
"s_nop 0\n"
"v_permlane32_swap_b32 v240, v242\n"
"v_permlane32_swap_b32 v241, v243\n"
"v_add_f32 v240, v240, v242\n"
"v_add_f32 v241, v241, v243\n"
"v_min_u32 v240, -2, v240\n"
"v_min_u32 v241, -2, v241\n"
"s_add_u32 s60, s79, 16449536\n"
"v_mov_b32 v242, 0x40000000\n"
"v_cndmask_b32_e64 v242, v242, v4, s[62:63]\n"
"buffer_store_dwordx2 v[240:241], v242, s[44:47], s60 offen sc1\n"
"s_add_u32 m0, s92, 8192\n"
"s_add_u32 s95, s71, 8192\n"
"buffer_load_dwordx4 v0, s[40:43], s95 offen lds\n"
"buffer_load_dwordx4 v0, s[40:43], s95 offen offset:1024 lds\n"
"buffer_load_dwordx4 v0, s[40:43], s95 offen offset:2048 lds\n"
"buffer_load_dwordx4 v0, s[40:43], s95 offen offset:3072 lds\n"
"s_add_u32 m0, s92, 12288\n"
"s_add_u32 s95, s71, 12288\n"
"buffer_load_dwordx4 v0, s[40:43], s95 offen lds\n"
"buffer_load_dwordx4 v0, s[40:43], s95 offen offset:1024 lds\n"
"buffer_load_dwordx4 v0, s[40:43], s95 offen offset:2048 lds\n"
"s_branch L_dn_done_0\n"
"L_dn_skip_0:\n"
"v_mul_f32 v232, v17, v144\n"
"v_mul_f32 v233, v17, v145\n"
"v_mul_f32 v234, v17, v146\n"
"v_mul_f32 v235, v17, v147\n"
"v_exp_f32 v232, v232\n"
"v_exp_f32 v233, v233\n"
"v_exp_f32 v234, v234\n"
"v_exp_f32 v235, v235\n"
"v_mul_f32 v144, v144, v152\n"
"v_mul_f32 v145, v145, v153\n"
"v_mul_f32 v146, v146, v154\n"
"v_mul_f32 v147, v147, v155\n"
"v_fma_f32 v232, v232, v18, v18\n"
"v_fma_f32 v233, v233, v18, v18\n"
"v_fma_f32 v234, v234, v18, v18\n"
"v_fma_f32 v235, v235, v18, v18\n"
"v_rcp_f32 v232, v232\n"
"v_rcp_f32 v233, v233\n"
"v_rcp_f32 v234, v234\n"
"v_rcp_f32 v235, v235\n"
"v_mul_f32 v144, v144, v232\n"
"v_mul_f32 v145, v145, v233\n"
"v_mul_f32 v146, v146, v234\n"
"v_mul_f32 v147, v147, v235\n"
"v_cvt_pk_bf16_f32 v240, v144, v145\n"
"v_cvt_pk_bf16_f32 v241, v146, v147\n"
"v_min_u32 v240, -2, v240\n"
"v_min_u32 v241, -2, v241\n"
"v_mul_f32 v236, v17, v148\n"
"v_mul_f32 v237, v17, v149\n"
"v_mul_f32 v238, v17, v150\n"
"v_mul_f32 v239, v17, v151\n"
"v_exp_f32 v236, v236\n"
"v_exp_f32 v237, v237\n"
"v_exp_f32 v238, v238\n"
"v_exp_f32 v239, v239\n"
"v_mul_f32 v148, v148, v156\n"
"v_mul_f32 v149, v149, v157\n"
"v_mul_f32 v150, v150, v158\n"
"v_mul_f32 v151, v151, v159\n"
"v_fma_f32 v236, v236, v18, v18\n"
"v_fma_f32 v237, v237, v18, v18\n"
"v_fma_f32 v238, v238, v18, v18\n"
"v_fma_f32 v239, v239, v18, v18\n"
"v_rcp_f32 v236, v236\n"
"v_rcp_f32 v237, v237\n"
"v_rcp_f32 v238, v238\n"
"v_rcp_f32 v239, v239\n"
"v_mul_f32 v148, v148, v236\n"
"v_mul_f32 v149, v149, v237\n"
"v_mul_f32 v150, v150, v238\n"
"v_mul_f32 v151, v151, v239\n"
"v_cvt_pk_bf16_f32 v242, v148, v149\n"
"v_cvt_pk_bf16_f32 v243, v150, v151\n"
"v_min_u32 v242, -2, v242\n"
"v_min_u32 v243, -2, v243\n"
"s_add_u32 s59, s82, 4096\n"
"buffer_store_dwordx4 v[240:243], v0, s[44:47], s59 offen offset:2048 sc1\n"
"s_barrier\n"
"s_add_u32 m0, s92, 0\n"
"s_add_u32 s95, s71, 0\n"
"buffer_load_dwordx4 v0, s[40:43], s95 offen lds\n"
"buffer_load_dwordx4 v0, s[40:43], s95 offen offset:1024 lds\n"
"buffer_load_dwordx4 v0, s[40:43], s95 offen offset:2048 lds\n"
"buffer_load_dwordx4 v0, s[40:43], s95 offen offset:3072 lds\n"
"s_add_u32 m0, s92, 4096\n"
"s_add_u32 s95, s71, 4096\n"
"buffer_load_dwordx4 v0, s[40:43], s95 offen lds\n"
"buffer_load_dwordx4 v0, s[40:43], s95 offen offset:1024 lds\n"
"s_waitcnt vmcnt(58)\n"
"buffer_load_dwordx4 v0, s[40:43], s95 offen offset:2048 lds\n"
"buffer_load_dwordx4 v0, s[40:43], s95 offen offset:3072 lds\n"
"s_add_u32 m0, s92, 8192\n"
"s_add_u32 s95, s71, 8192\n"
"buffer_load_dwordx4 v0, s[40:43], s95 offen lds\n"
"buffer_load_dwordx4 v0, s[40:43], s95 offen offset:1024 lds\n"
"s_waitcnt vmcnt(58)\n"
"buffer_load_dwordx4 v0, s[40:43], s95 offen offset:2048 lds\n"
"buffer_load_dwordx4 v0, s[40:43], s95 offen offset:3072 lds\n"
"s_add_u32 m0, s92, 12288\n"
"s_add_u32 s95, s71, 12288\n"
"buffer_load_dwordx4 v0, s[40:43], s95 offen lds\n"
"buffer_load_dwordx4 v0, s[40:43], s95 offen offset:1024 lds\n"
"s_waitcnt vmcnt(58)\n"
"buffer_load_dwordx4 v0, s[40:43], s95 offen offset:2048 lds\n"
"s_waitcnt vmcnt(34)\n"
"v_mov_b32 v32, -1\n"
"v_mov_b32 v33, -1\n"
"v_mov_b32 v34, -1\n"
"v_mov_b32 v35, -1\n"
"s_and_b32 s59, %[blk], 7\n"
"s_lshl_b32 s59, s59, 4\n"
"s_and_b32 s60, %[slw], 15\n"
"s_add_u32 s60, s60, s59\n"
"s_mul_i32 s94, s60, 57344\n"
"s_add_u32 s94, s94, 21889024\n"
"s_mul_i32 s95, s60, 16384\n"
"s_min_u32 s95, s95, 2048000\n"
"s_add_u32 s95, s95, 16678912\n"
"s_add_u32 s94, s94, 4096\n"
"s_add_u32 s94, s94, 4096\n"
"s_add_u32 s94, s94, 4096\n"
"s_add_u32 s94, s94, 4096\n"
"s_add_u32 s94, s94, 4096\n"
"s_add_u32 s94, s94, 4096\n"
"buffer_store_dwordx4 v[32:35], v0, s[44:47], s94 offen sc1\n"
"s_sleep 1\n"
"buffer_store_dwordx4 v[32:35], v0, s[44:47], s94 offen offset:1024 sc1\n"
"s_sleep 1\n"
"buffer_store_dwordx4 v[32:35], v0, s[44:47], s94 offen offset:2048 sc1\n"
"s_sleep 1\n"
"buffer_store_dwordx4 v[32:35], v0, s[44:47], s94 offen offset:3072 sc1\n"
"s_sleep 1\n"
"s_add_u32 s94, s94, 4096\n"
"buffer_store_dwordx4 v[32:35], v0, s[44:47], s94 offen sc1\n"
"s_sleep 1\n"
"buffer_store_dwordx4 v[32:35], v0, s[44:47], s94 offen offset:1024 sc1\n"
"s_sleep 1\n"
"buffer_store_dwordx4 v[32:35], v0, s[44:47], s94 offen offset:2048 sc1\n"
"s_sleep 1\n"
"buffer_store_dwordx4 v[32:35], v0, s[44:47], s94 offen offset:3072 sc1\n"
"s_sleep 1\n"
"s_add_u32 s94, s94, 4096\n"
"buffer_store_dwordx4 v[32:35], v0, s[44:47], s94 offen sc1\n"
"s_sleep 1\n"
"buffer_store_dwordx4 v[32:35], v0, s[44:47], s94 offen offset:1024 sc1\n"
"s_sleep 1\n"
"buffer_store_dwordx4 v[32:35], v0, s[44:47], s94 offen offset:2048 sc1\n"
"s_sleep 1\n"
"buffer_store_dwordx4 v[32:35], v0, s[44:47], s94 offen offset:3072 sc1\n"
"s_sleep 1\n"
"s_add_u32 s94, s94, 4096\n"
"buffer_store_dwordx4 v[32:35], v0, s[44:47], s94 offen sc1\n"
"s_sleep 1\n"
"buffer_store_dwordx4 v[32:35], v0, s[44:47], s94 offen offset:1024 sc1\n"
"s_sleep 1\n"
"buffer_store_dwordx4 v[32:35], v0, s[44:47], s94 offen offset:2048 sc1\n"
"s_sleep 1\n"
"buffer_store_dwordx4 v[32:35], v0, s[44:47], s94 offen offset:3072 sc1\n"
"s_sleep 1\n"
"s_add_u32 s94, s94, 4096\n"
"buffer_store_dwordx4 v[32:35], v0, s[44:47], s94 offen sc1\n"
"s_sleep 1\n"
"buffer_store_dwordx4 v[32:35], v0, s[44:47], s94 offen offset:1024 sc1\n"
"s_sleep 1\n"
"buffer_store_dwordx4 v[32:35], v0, s[44:47], s94 offen offset:2048 sc1\n"
"s_sleep 1\n"
"buffer_store_dwordx4 v[32:35], v0, s[44:47], s94 offen offset:3072 sc1\n"
"s_sleep 1\n"
"s_add_u32 s94, s94, 4096\n"
"buffer_store_dwordx4 v[32:35], v0, s[44:47], s94 offen sc1\n"
"s_sleep 1\n"
"buffer_store_dwordx4 v[32:35], v0, s[44:47], s94 offen offset:1024 sc1\n"
"s_sleep 1\n"
"buffer_store_dwordx4 v[32:35], v0, s[44:47], s94 offen offset:2048 sc1\n"
"s_sleep 1\n"
"buffer_store_dwordx4 v[32:35], v0, s[44:47], s94 offen offset:3072 sc1\n"
"s_sleep 1\n"
"s_add_u32 s94, s94, 4096\n"
"buffer_store_dwordx4 v[32:35], v0, s[44:47], s94 offen sc1\n"
"s_sleep 1\n"
"buffer_store_dwordx4 v[32:35], v0, s[44:47], s94 offen offset:1024 sc1\n"
"s_sleep 1\n"
"buffer_store_dwordx4 v[32:35], v0, s[44:47], s94 offen offset:2048 sc1\n"
"s_sleep 1\n"
"buffer_store_dwordx4 v[32:35], v0, s[44:47], s94 offen offset:3072 sc1\n"
"s_sleep 1\n"
"s_add_u32 s94, s94, 4096\n"
"s_waitcnt vmcnt(58)\n"
"buffer_store_dwordx4 v[32:35], v0, s[44:47], s94 offen sc1\n"
"s_sleep 1\n"
"buffer_store_dwordx4 v[32:35], v0, s[44:47], s94 offen offset:1024 sc1\n"
"s_sleep 1\n"
"buffer_store_dwordx4 v[32:35], v0, s[44:47], s94 offen offset:2048 sc1\n"
"s_sleep 1\n"
"buffer_store_dwordx4 v[32:35], v0, s[44:47], s94 offen offset:3072 sc1\n"
"s_sleep 1\n"
"s_waitcnt vmcnt(58)\n"
"buffer_store_dwordx4 v[32:35], v0, s[44:47], s95 offen sc1\n"
"s_sleep 1\n"
"buffer_store_dwordx4 v[32:35], v0, s[44:47], s95 offen offset:1024 sc1\n"
"s_sleep 1\n"
"buffer_store_dwordx4 v[32:35], v0, s[44:47], s95 offen offset:2048 sc1\n"
"s_sleep 1\n"
"buffer_store_dwordx4 v[32:35], v0, s[44:47], s95 offen offset:3072 sc1\n"
"s_sleep 1\n"
"s_add_u32 s95, s95, 4096\n"
"s_waitcnt vmcnt(58)\n"
"buffer_store_dwordx4 v[32:35], v0, s[44:47], s95 offen sc1\n"
"s_sleep 1\n"
"buffer_store_dwordx4 v[32:35], v0, s[44:47], s95 offen offset:1024 sc1\n"
"s_sleep 1\n"
"buffer_store_dwordx4 v[32:35], v0, s[44:47], s95 offen offset:2048 sc1\n"
"s_sleep 1\n"
"buffer_store_dwordx4 v[32:35], v0, s[44:47], s95 offen offset:3072 sc1\n"
"s_sleep 1\n"
"s_add_u32 s95, s95, 4096\n"
"s_waitcnt vmcnt(58)\n"
"buffer_store_dwordx4 v[32:35], v0, s[44:47], s95 offen sc1\n"
"s_sleep 1\n"
"buffer_store_dwordx4 v[32:35], v0, s[44:47], s95 offen offset:1024 sc1\n"
"s_sleep 1\n"
"buffer_store_dwordx4 v[32:35], v0, s[44:47], s95 offen offset:2048 sc1\n"
"s_sleep 1\n"
"buffer_store_dwordx4 v[32:35], v0, s[44:47], s95 offen offset:3072 sc1\n"
"s_sleep 1\n"
"s_add_u32 s95, s95, 4096\n"
"s_waitcnt vmcnt(58)\n"
"buffer_store_dwordx4 v[32:35], v0, s[44:47], s95 offen sc1\n"
"s_sleep 1\n"
"buffer_store_dwordx4 v[32:35], v0, s[44:47], s95 offen offset:1024 sc1\n"
"s_sleep 1\n"
"buffer_store_dwordx4 v[32:35], v0, s[44:47], s95 offen offset:2048 sc1\n"
"s_sleep 1\n"
"buffer_store_dwordx4 v[32:35], v0, s[44:47], s95 offen offset:3072 sc1\n"
"s_sleep 1\n"
"s_waitcnt vmcnt(0)\n"
"s_sleep 20\n"
"L_dn_done_0:\n"
"s_add_u32 s44, s44, 14614528\n"
"s_addc_u32 s45, s45, 0\n"
"s_mov_b32 s46, 14614528\n"
"s_mov_b32 s98, 32\n"
"s_waitcnt vmcnt(0)\n"
"s_barrier\n"
"s_sleep 4\n"
"L_xp_1:\n"
"buffer_load_dword v19, v3, s[44:47], s64 offen sc1\n"
"s_waitcnt vmcnt(0)\n"
"v_cmp_eq_u32 vcc, -1, v19\n"
"s_cbranch_vccnz L_xp_s_1\n"
"L_xp_go_1:\n"
"v_mov_b32 v19, 0\n"
"buffer_load_dwordx4 v[32:35], v0, s[44:47], s64 offen sc1\n"
"buffer_load_dwordx4 v[36:39], v0, s[44:47], s64 offen offset:1024 sc1\n"
"buffer_load_dwordx4 v[40:43], v0, s[44:47], s64 offen offset:2048 sc1\n"
"buffer_load_dwordx4 v[44:47], v0, s[44:47], s64 offen offset:3072 sc1\n"
"s_add_u32 s94, s64, 4096\n"
"buffer_load_dwordx4 v[48:51], v0, s[44:47], s94 offen sc1\n"
"buffer_load_dwordx4 v[52:55], v0, s[44:47], s94 offen offset:1024 sc1\n"
"buffer_load_dwordx4 v[56:59], v0, s[44:47], s94 offen offset:2048 sc1\n"
"buffer_load_dwordx4 v[60:63], v0, s[44:47], s94 offen offset:3072 sc1\n"
"s_add_u32 s94, s64, 8192\n"
"buffer_load_dwordx4 v[64:67], v0, s[44:47], s94 offen sc1\n"
"buffer_load_dwordx4 v[68:71], v0, s[44:47], s94 offen offset:1024 sc1\n"
"buffer_load_dwordx4 v[72:75], v0, s[44:47], s94 offen offset:2048 sc1\n"
"buffer_load_dwordx4 v[76:79], v0, s[44:47], s94 offen offset:3072 sc1\n"
"buffer_load_dwordx2 v[224:225], v4, s[44:47], s65 offen sc1\n"
"buffer_load_dwordx2 v[226:227], v4, s[44:47], s65 offen offset:512 sc1\n"
"buffer_load_dwordx2 v[228:229], v4, s[44:47], s65 offen offset:1024 sc1\n"
"buffer_load_dwordx2 v[230:231], v4, s[44:47], s65 offen offset:1536 sc1\n"
"buffer_load_dwordx2 v[232:233], v4, s[44:47], s65 offen offset:2048 sc1\n"
"buffer_load_dwordx2 v[234:235], v4, s[44:47], s65 offen offset:2560 sc1\n"
"buffer_load_dwordx2 v[236:237], v4, s[44:47], s65 offen offset:3072 sc1\n"
"ds_read_b128 v[192:195], v0\n"
"ds_read_b128 v[196:199], v0 offset:29696\n"
"ds_read_b128 v[200:203], v0 offset:1024\n"
"ds_read_b128 v[204:207], v0 offset:30720\n"
"ds_read_b128 v[208:211], v0 offset:2048\n"
"ds_read_b128 v[212:215], v0 offset:31744\n"
"ds_read_b128 v[216:219], v0 offset:3072\n"
"ds_read_b128 v[220:223], v0 offset:32768\n"
"s_waitcnt vmcnt(18) lgkmcnt(6)\n"
"v_mfma_f32_32x32x16_bf16 v[144:159], v[32:35], v[192:195], 0\n"
"v_max3_u32 v19, v19, v32, v33\n"
"s_add_u32 s94, s64, 12288\n"
"buffer_load_dwordx4 v[80:83], v0, s[44:47], s94 offen sc1\n"
"v_mfma_f32_32x32x16_bf16 v[160:175], v[32:35], v[196:199], 0\n"
"ds_read_b128 v[192:195], v0 offset:4096\n"
"ds_read_b128 v[196:199], v0 offset:33792\n"
"v_max3_u32 v19, v19, v34, v35\n"
"s_waitcnt vmcnt(18) lgkmcnt(6)\n"
"v_mfma_f32_32x32x16_bf16 v[144:159], v[36:39], v[200:203], v[144:159]\n"
"v_max3_u32 v19, v19, v36, v37\n"
"buffer_load_dwordx4 v[84:87], v0, s[44:47], s94 offen offset:1024 sc1\n"
"v_mfma_f32_32x32x16_bf16 v[160:175], v[36:39], v[204:207], v[160:175]\n"
"ds_read_b128 v[200:203], v0 offset:5120\n"
"ds_read_b128 v[204:207], v0 offset:34816\n"
"v_max3_u32 v19, v19, v38, v39\n"
"s_waitcnt vmcnt(18) lgkmcnt(6)\n"
"v_mfma_f32_32x32x16_bf16 v[144:159], v[40:43], v[208:211], v[144:159]\n"
"v_max3_u32 v19, v19, v40, v41\n"
"buffer_load_dwordx4 v[88:91], v0, s[44:47], s94 offen offset:2048 sc1\n"
"v_mfma_f32_32x32x16_bf16 v[160:175], v[40:43], v[212:215], v[160:175]\n"
"ds_read_b128 v[208:211], v0 offset:6144\n"
"ds_read_b128 v[212:215], v0 offset:35840\n"
"v_max3_u32 v19, v19, v42, v43\n"
"s_waitcnt vmcnt(18) lgkmcnt(6)\n"
"v_mfma_f32_32x32x16_bf16 v[144:159], v[44:47], v[216:219], v[144:159]\n"
"v_max3_u32 v19, v19, v44, v45\n"
"buffer_load_dwordx4 v[92:95], v0, s[44:47], s94 offen offset:3072 sc1\n"
"v_mfma_f32_32x32x16_bf16 v[160:175], v[44:47], v[220:223], v[160:175]\n"
"ds_read_b128 v[216:219], v0 offset:7168\n"
"ds_read_b128 v[220:223], v0 offset:36864\n"
"v_max3_u32 v19, v19, v46, v47\n"
"s_waitcnt vmcnt(18) lgkmcnt(6)\n"
"v_mfma_f32_32x32x16_bf16 v[144:159], v[48:51], v[192:195], v[144:159]\n"
"v_max3_u32 v19, v19, v48, v49\n"
"s_add_u32 s94, s64, 16384\n"
"buffer_load_dwordx4 v[96:99], v0, s[44:47], s94 offen sc1\n"
"v_mfma_f32_32x32x16_bf16 v[160:175], v[48:51], v[196:199], v[160:175]\n"
"ds_read_b128 v[192:195], v0 offset:8192\n"
"ds_read_b128 v[196:199], v0 offset:37888\n"
"v_max3_u32 v19, v19, v50, v51\n"
"s_waitcnt vmcnt(18) lgkmcnt(6)\n"
"v_mfma_f32_32x32x16_bf16 v[144:159], v[52:55], v[200:203], v[144:159]\n"
"v_max3_u32 v19, v19, v52, v53\n"
"buffer_load_dwordx4 v[100:103], v0, s[44:47], s94 offen offset:1024 sc1\n"
"v_mfma_f32_32x32x16_bf16 v[160:175], v[52:55], v[204:207], v[160:175]\n"
"ds_read_b128 v[200:203], v0 offset:9216\n"
"ds_read_b128 v[204:207], v0 offset:38912\n"
"v_max3_u32 v19, v19, v54, v55\n"
"s_waitcnt vmcnt(18) lgkmcnt(6)\n"
"v_mfma_f32_32x32x16_bf16 v[144:159], v[56:59], v[208:211], v[144:159]\n"
"v_max3_u32 v19, v19, v56, v57\n"
"buffer_load_dwordx4 v[104:107], v0, s[44:47], s94 offen offset:2048 sc1\n"
"v_mfma_f32_32x32x16_bf16 v[160:175], v[56:59], v[212:215], v[160:175]\n"
"ds_read_b128 v[208:211], v0 offset:10240\n"
"ds_read_b128 v[212:215], v0 offset:39936\n"
"v_max3_u32 v19, v19, v58, v59\n"
"s_waitcnt vmcnt(18) lgkmcnt(6)\n"
"v_mfma_f32_32x32x16_bf16 v[144:159], v[60:63], v[216:219], v[144:159]\n"
"v_max3_u32 v19, v19, v60, v61\n"
"buffer_load_dwordx4 v[108:111], v0, s[44:47], s94 offen offset:3072 sc1\n"
"v_mfma_f32_32x32x16_bf16 v[160:175], v[60:63], v[220:223], v[160:175]\n"
"ds_read_b128 v[216:219], v0 offset:11264\n"
"ds_read_b128 v[220:223], v0 offset:40960\n"
"v_max3_u32 v19, v19, v62, v63\n"
"s_waitcnt vmcnt(18) lgkmcnt(6)\n"
"v_mfma_f32_32x32x16_bf16 v[144:159], v[64:67], v[192:195], v[144:159]\n"
"v_max3_u32 v19, v19, v64, v65\n"
"s_add_u32 s94, s64, 20480\n"
"buffer_load_dwordx4 v[112:115], v0, s[44:47], s94 offen sc1\n"
"v_mfma_f32_32x32x16_bf16 v[160:175], v[64:67], v[196:199], v[160:175]\n"
"ds_read_b128 v[192:195], v0 offset:12288\n"
"ds_read_b128 v[196:199], v0 offset:41984\n"
"v_max3_u32 v19, v19, v66, v67\n"
"s_waitcnt vmcnt(18) lgkmcnt(6)\n"
"v_mfma_f32_32x32x16_bf16 v[144:159], v[68:71], v[200:203], v[144:159]\n"
"v_max3_u32 v19, v19, v68, v69\n"
"buffer_load_dwordx4 v[116:119], v0, s[44:47], s94 offen offset:1024 sc1\n"
"v_mfma_f32_32x32x16_bf16 v[160:175], v[68:71], v[204:207], v[160:175]\n"
"ds_read_b128 v[200:203], v0 offset:13312\n"
"ds_read_b128 v[204:207], v0 offset:43008\n"
"v_max3_u32 v19, v19, v70, v71\n"
"s_waitcnt vmcnt(18) lgkmcnt(6)\n"
"v_mfma_f32_32x32x16_bf16 v[144:159], v[72:75], v[208:211], v[144:159]\n"
"v_max3_u32 v19, v19, v72, v73\n"
"buffer_load_dwordx4 v[120:123], v0, s[44:47], s94 offen offset:2048 sc1\n"
"v_mfma_f32_32x32x16_bf16 v[160:175], v[72:75], v[212:215], v[160:175]\n"
"ds_read_b128 v[208:211], v0 offset:14336\n"
"ds_read_b128 v[212:215], v0 offset:44032\n"
"v_max3_u32 v19, v19, v74, v75\n"
"s_waitcnt vmcnt(18) lgkmcnt(6)\n"
"v_mfma_f32_32x32x16_bf16 v[144:159], v[76:79], v[216:219], v[144:159]\n"
"v_max3_u32 v19, v19, v76, v77\n"
"buffer_load_dwordx4 v[124:127], v0, s[44:47], s94 offen offset:3072 sc1\n"
"v_mfma_f32_32x32x16_bf16 v[160:175], v[76:79], v[220:223], v[160:175]\n"
"ds_read_b128 v[216:219], v0 offset:15360\n"
"ds_read_b128 v[220:223], v0 offset:45056\n"
"v_max3_u32 v19, v19, v78, v79\n"
"s_waitcnt vmcnt(11) lgkmcnt(6)\n"
"v_mfma_f32_32x32x16_bf16 v[144:159], v[80:83], v[192:195], v[144:159]\n"
"v_max3_u32 v19, v19, v80, v81\n"
"s_add_u32 s94, s64, 24576\n"
"buffer_load_dwordx4 v[128:131], v0, s[44:47], s94 offen sc1\n"
"v_mfma_f32_32x32x16_bf16 v[160:175], v[80:83], v[196:199], v[160:175]\n"
"ds_read_b128 v[192:195], v0 offset:16384\n"
"ds_read_b128 v[196:199], v0 offset:46080\n"
"v_max3_u32 v19, v19, v82, v83\n"
"s_waitcnt vmcnt(11) lgkmcnt(6)\n"
"v_mfma_f32_32x32x16_bf16 v[144:159], v[84:87], v[200:203], v[144:159]\n"
"v_max3_u32 v19, v19, v84, v85\n"
"buffer_load_dwordx4 v[132:135], v0, s[44:47], s94 offen offset:1024 sc1\n"
"v_max3_u32 v19, v19, v224, v225\n"
"v_max3_u32 v19, v19, v226, v227\n"
"v_mfma_f32_32x32x16_bf16 v[160:175], v[84:87], v[204:207], v[160:175]\n"
"ds_read_b128 v[200:203], v0 offset:17408\n"
"ds_read_b128 v[204:207], v0 offset:47104\n"
"v_max3_u32 v19, v19, v86, v87\n"
"v_max3_u32 v19, v19, v228, v229\n"
"v_max3_u32 v19, v19, v230, v231\n"
"v_max3_u32 v19, v19, v232, v233\n"
"s_waitcnt vmcnt(11) lgkmcnt(6)\n"
"v_mfma_f32_32x32x16_bf16 v[144:159], v[88:91], v[208:211], v[144:159]\n"
"v_max3_u32 v19, v19, v88, v89\n"
"buffer_load_dwordx4 v[136:139], v0, s[44:47], s94 offen offset:2048 sc1\n"
"v_max3_u32 v19, v19, v234, v235\n"
"v_mfma_f32_32x32x16_bf16 v[160:175], v[88:91], v[212:215], v[160:175]\n"
"ds_read_b128 v[208:211], v0 offset:18432\n"
"ds_read_b128 v[212:215], v0 offset:48128\n"
"v_max3_u32 v19, v19, v90, v91\n"
"v_max3_u32 v19, v19, v236, v237\n"
"v_add_f32 v224, v224, v226\n"
"v_add_f32 v225, v225, v227\n"
"v_add_f32 v228, v228, v230\n"
"s_waitcnt vmcnt(11) lgkmcnt(6)\n"
"v_mfma_f32_32x32x16_bf16 v[144:159], v[92:95], v[216:219], v[144:159]\n"
"v_max3_u32 v19, v19, v92, v93\n"
"buffer_load_dwordx4 v[140:143], v0, s[44:47], s94 offen offset:3072 sc1\n"
"v_add_f32 v229, v229, v231\n"
"v_mfma_f32_32x32x16_bf16 v[160:175], v[92:95], v[220:223], v[160:175]\n"
"ds_read_b128 v[216:219], v0 offset:19456\n"
"ds_read_b128 v[220:223], v0 offset:49152\n"
"v_max3_u32 v19, v19, v94, v95\n"
"v_add_f32 v232, v232, v234\n"
"v_add_f32 v233, v233, v235\n"
"v_add_f32 v224, v224, v228\n"
"s_waitcnt vmcnt(11) lgkmcnt(6)\n"
"v_mfma_f32_32x32x16_bf16 v[144:159], v[96:99], v[192:195], v[144:159]\n"
"v_max3_u32 v19, v19, v96, v97\n"
"s_add_u32 m0, s89, 59392\n"
"s_add_u32 s95, s86, 59392\n"
"buffer_load_dwordx4 v0, s[40:43], s95 offen lds\n"
"v_add_f32 v225, v225, v229\n"
"v_mfma_f32_32x32x16_bf16 v[160:175], v[96:99], v[196:199], v[160:175]\n"
"ds_read_b128 v[192:195], v0 offset:20480\n"
"ds_read_b128 v[196:199], v0 offset:50176\n"
"v_max3_u32 v19, v19, v98, v99\n"
"v_add_f32 v232, v232, v236\n"
"v_add_f32 v233, v233, v237\n"
"v_add_f32 v224, v224, v232\n"
"v_add_f32 v225, v225, v233\n"
"s_waitcnt vmcnt(11) lgkmcnt(6)\n"
"v_mfma_f32_32x32x16_bf16 v[144:159], v[100:103], v[200:203], v[144:159]\n"
"v_max3_u32 v19, v19, v100, v101\n"
"buffer_load_dwordx4 v0, s[40:43], s95 offen offset:1024 lds\n"
"v_mov_b32 v240, v224\n"
"v_mfma_f32_32x32x16_bf16 v[160:175], v[100:103], v[204:207], v[160:175]\n"
"ds_read_b128 v[200:203], v0 offset:21504\n"
"ds_read_b128 v[204:207], v0 offset:51200\n"
"v_max3_u32 v19, v19, v102, v103\n"
"v_mov_b32 v241, v225\n"
"v_permlane32_swap_b32 v224, v240\n"
"s_nop 0\n"
"v_permlane32_swap_b32 v225, v241\n"
"s_waitcnt vmcnt(11) lgkmcnt(6)\n"
"v_mfma_f32_32x32x16_bf16 v[144:159], v[104:107], v[208:211], v[144:159]\n"
"v_max3_u32 v19, v19, v104, v105\n"
"buffer_load_dwordx4 v0, s[40:43], s95 offen offset:2048 lds\n"
"v_add_f32 v224, v224, v240\n"
"v_mfma_f32_32x32x16_bf16 v[160:175], v[104:107], v[212:215], v[160:175]\n"
"ds_read_b128 v[208:211], v0 offset:22528\n"
"ds_read_b128 v[212:215], v0 offset:52224\n"
"v_max3_u32 v19, v19, v106, v107\n"
"v_add_f32 v225, v225, v241\n"
"v_mul_f32 v224, 0x3b124925, v224\n"
"v_mul_f32 v225, 0x3b124925, v225\n"
"v_fma_f32 v225, -v224, v224, v225\n"
"s_waitcnt vmcnt(11) lgkmcnt(6)\n"
"v_mfma_f32_32x32x16_bf16 v[144:159], v[108:111], v[216:219], v[144:159]\n"
"v_max3_u32 v19, v19, v108, v109\n"
"buffer_load_dwordx4 v0, s[40:43], s95 offen offset:3072 lds\n"
"v_max_f32 v225, 0, v225\n"
"v_mfma_f32_32x32x16_bf16 v[160:175], v[108:111], v[220:223], v[160:175]\n"
"ds_read_b128 v[216:219], v0 offset:23552\n"
"ds_read_b128 v[220:223], v0 offset:53248\n"
"v_max3_u32 v19, v19, v110, v111\n"
"v_cvt_pk_bf16_f32 v225, v225, v225\n"
"v_and_b32 v225, 0xffff0000, v225\n"
"v_add_f32 v225, 0x3727c5ac, v225\n"
"s_waitcnt vmcnt(11) lgkmcnt(6)\n"
"v_mfma_f32_32x32x16_bf16 v[144:159], v[112:115], v[192:195], v[144:159]\n"
"v_max3_u32 v19, v19, v112, v113\n"
"s_add_u32 m0, s89, 63488\n"
"s_add_u32 s95, s86, 63488\n"
"buffer_load_dwordx4 v0, s[40:43], s95 offen lds\n"
"v_cvt_pk_bf16_f32 v225, v225, v225\n"
"v_mfma_f32_32x32x16_bf16 v[160:175], v[112:115], v[196:199], v[160:175]\n"
"ds_read_b128 v[192:195], v0 offset:24576\n"
"ds_read_b128 v[196:199], v0 offset:54272\n"
"v_max3_u32 v19, v19, v114, v115\n"
"v_and_b32 v225, 0xffff0000, v225\n"
"v_sqrt_f32 v225, v225\n"
"s_nop 0\n"
"v_cvt_pk_bf16_f32 v225, v225, v225\n"
"s_waitcnt vmcnt(11) lgkmcnt(6)\n"
"v_mfma_f32_32x32x16_bf16 v[144:159], v[116:119], v[200:203], v[144:159]\n"
"v_max3_u32 v19, v19, v116, v117\n"
"buffer_load_dwordx4 v0, s[40:43], s95 offen offset:1024 lds\n"
"v_and_b32 v225, 0xffff0000, v225\n"
"v_mfma_f32_32x32x16_bf16 v[160:175], v[116:119], v[204:207], v[160:175]\n"
"ds_read_b128 v[200:203], v0 offset:25600\n"
"ds_read_b128 v[204:207], v0 offset:55296\n"
"v_max3_u32 v19, v19, v118, v119\n"
"v_rcp_f32 v16, v225\n"
"v_cvt_pk_bf16_f32 v20, -v224, -v224\n"
"s_waitcnt vmcnt(11) lgkmcnt(6)\n"
"v_mfma_f32_32x32x16_bf16 v[144:159], v[120:123], v[208:211], v[144:159]\n"
"v_max3_u32 v19, v19, v120, v121\n"
"buffer_load_dwordx4 v0, s[40:43], s95 offen offset:2048 lds\n"
"v_lshrrev_b32 v241, 16, v225\n"
"v_mfma_f32_32x32x16_bf16 v[160:175], v[120:123], v[212:215], v[160:175]\n"
"ds_read_b128 v[208:211], v0 offset:26624\n"
"ds_read_b128 v[212:215], v0 offset:56320\n"
"v_max3_u32 v19, v19, v122, v123\n"
"v_mul_f32 v18, v225, v225\n"
"v_mov_b32 v21, v20\n"
"v_mov_b32 v22, v20\n"
"v_cndmask_b32_e64 v23, v241, v20, s[62:63]\n"
"s_waitcnt vmcnt(11) lgkmcnt(6)\n"
"v_mfma_f32_32x32x16_bf16 v[144:159], v[124:127], v[216:219], v[144:159]\n"
"v_max3_u32 v19, v19, v124, v125\n"
"buffer_load_dwordx4 v0, s[40:43], s95 offen offset:3072 lds\n"
"v_mul_f32 v17, 0xbfb8aa3b, v16\n"
"v_mfma_f32_32x32x16_bf16 v[160:175], v[124:127], v[220:223], v[160:175]\n"
"ds_read_b128 v[216:219], v0 offset:27648\n"
"ds_read_b128 v[220:223], v0 offset:57344\n"
"v_max3_u32 v19, v19, v126, v127\n"
"ds_write_b32 v25, v16\n"
"ds_read_b128 v[224:227], v26\n"
"ds_read_b128 v[228:231], v26 offset:32\n"
"s_waitcnt vmcnt(11) lgkmcnt(9)\n"
"v_mfma_f32_32x32x16_bf16 v[144:159], v[128:131], v[192:195], v[144:159]\n"
"v_max3_u32 v19, v19, v128, v129\n"
"buffer_load_dwordx4 a[224:227], v0, s[40:43], s67 offen\n"
"ds_read_b128 v[232:235], v26 offset:64\n"
"v_mfma_f32_32x32x16_bf16 v[160:175], v[128:131], v[196:199], v[160:175]\n"
"ds_read_b128 v[192:195], v0 offset:28672\n"
"ds_read_b128 v[196:199], v0 offset:58368\n"
"v_max3_u32 v19, v19, v130, v131\n"
"ds_read_b128 v[236:239], v26 offset:96\n"
"s_waitcnt vmcnt(11) lgkmcnt(11)\n"
"v_mfma_f32_32x32x16_bf16 v[144:159], v[132:135], v[200:203], v[144:159]\n"
"v_max3_u32 v19, v19, v132, v133\n"
"buffer_load_dwordx4 a[228:231], v0, s[40:43], s67 offen offset:1024\n"
"v_mfma_f32_32x32x16_bf16 v[160:175], v[132:135], v[204:207], v[160:175]\n"
"v_max3_u32 v19, v19, v134, v135\n"
"s_waitcnt vmcnt(11) lgkmcnt(9)\n"
"v_mfma_f32_32x32x16_bf16 v[144:159], v[136:139], v[208:211], v[144:159]\n"
"v_max3_u32 v19, v19, v136, v137\n"
"buffer_load_dwordx4 a[232:235], v0, s[40:43], s67 offen offset:2048\n"
"v_mfma_f32_32x32x16_bf16 v[160:175], v[136:139], v[212:215], v[160:175]\n"
"v_max3_u32 v19, v19, v138, v139\n"
"s_waitcnt vmcnt(11) lgkmcnt(7)\n"
"v_mfma_f32_32x32x16_bf16 v[144:159], v[140:143], v[216:219], v[144:159]\n"
"v_max3_u32 v19, v19, v140, v141\n"
"buffer_load_dwordx4 a[236:239], v0, s[40:43], s67 offen offset:3072\n"
"v_mfma_f32_32x32x16_bf16 v[160:175], v[140:143], v[220:223], v[160:175]\n"
"v_max3_u32 v19, v19, v142, v143\n"
"s_waitcnt lgkmcnt(1)\n"
"v_mfma_f32_32x32x16_bf16 v[144:159], v[20:23], v[192:195], v[144:159]\n"
"v_mfma_f32_32x32x16_bf16 v[160:175], v[20:23], v[196:199], v[160:175]\n"
"v_cmp_eq_u32 vcc, -1, v19\n"
"s_cbranch_vccnz L_xv_s_1\n"
"L_xv_go_1:\n"
"v_mov_b32 v248, s58\n"
"v_mov_b32 v249, s58\n"
"v_mov_b32 v250, s61\n"
"v_mov_b32 v251, s61\n"
"v_mov_b32 v252, v16\n"
"v_mov_b32 v253, v16\n"
"v_add_u32 v244, s90, v0\n"
"v_add_u32 v245, s91, v0\n"
"s_nop 0\n"
"v_pk_fma_f32 v[144:145], v[144:145], v[224:225], v[248:249]\n"
"v_pk_fma_f32 v[146:147], v[146:147], v[226:227], v[248:249]\n"
"v_pk_mul_f32 v[240:241], v[144:145], v[250:251]\n"
"v_pk_mul_f32 v[242:243], v[146:147], v[250:251]\n"
"v_exp_f32_e64 v240, v240 clamp\n"
"v_exp_f32_e64 v241, v241 clamp\n"
"v_exp_f32_e64 v242, v242 clamp\n"
"v_exp_f32_e64 v243, v243 clamp\n"
"s_add_u32 s94, s67, 4096\n"
"buffer_load_dwordx4 a[240:243], v0, s[40:43], s94 offen\n"
"v_max_f32 v144, s58, v144\n"
"v_max_f32 v145, s58, v145\n"
"v_max_f32 v146, s58, v146\n"
"v_max_f32 v147, s58, v147\n"
"v_pk_add_f32 v[144:145], v[144:145], v[240:241]\n"
"v_pk_add_f32 v[146:147], v[146:147], v[242:243]\n"
"v_pk_fma_f32 v[148:149], v[148:149], v[228:229], v[248:249]\n"
"v_pk_fma_f32 v[150:151], v[150:151], v[230:231], v[248:249]\n"
"buffer_load_dwordx4 a[244:247], v0, s[40:43], s94 offen offset:1024\n"
"v_pk_mul_f32 v[240:241], v[148:149], v[250:251]\n"
"v_pk_mul_f32 v[242:243], v[150:151], v[250:251]\n"
"v_exp_f32_e64 v240, v240 clamp\n"
"v_exp_f32_e64 v241, v241 clamp\n"
"v_exp_f32_e64 v242, v242 clamp\n"
"v_exp_f32_e64 v243, v243 clamp\n"
"v_max_f32 v148, s58, v148\n"
"v_max_f32 v149, s58, v149\n"
"buffer_load_dwordx4 a[248:251], v0, s[40:43], s94 offen offset:2048\n"
"v_max_f32 v150, s58, v150\n"
"v_max_f32 v151, s58, v151\n"
"v_pk_add_f32 v[148:149], v[148:149], v[240:241]\n"
"v_pk_add_f32 v[150:151], v[150:151], v[242:243]\n"
"v_pk_fma_f32 v[152:153], v[152:153], v[232:233], v[248:249]\n"
"v_pk_fma_f32 v[154:155], v[154:155], v[234:235], v[248:249]\n"
"v_pk_mul_f32 v[240:241], v[152:153], v[250:251]\n"
"v_pk_mul_f32 v[242:243], v[154:155], v[250:251]\n"
"buffer_load_dwordx4 a[252:255], v0, s[40:43], s94 offen offset:3072\n"
"v_exp_f32_e64 v240, v240 clamp\n"
"v_exp_f32_e64 v241, v241 clamp\n"
"v_exp_f32_e64 v242, v242 clamp\n"
"v_exp_f32_e64 v243, v243 clamp\n"
"v_max_f32 v152, s58, v152\n"
"v_max_f32 v153, s58, v153\n"
"v_max_f32 v154, s58, v154\n"
"v_max_f32 v155, s58, v155\n"
"v_pk_add_f32 v[152:153], v[152:153], v[240:241]\n"
"v_pk_add_f32 v[154:155], v[154:155], v[242:243]\n"
"s_waitcnt lgkmcnt(0)\n"
"v_pk_fma_f32 v[156:157], v[156:157], v[236:237], v[248:249]\n"
"v_pk_fma_f32 v[158:159], v[158:159], v[238:239], v[248:249]\n"
"v_pk_mul_f32 v[240:241], v[156:157], v[250:251]\n"
"v_pk_mul_f32 v[242:243], v[158:159], v[250:251]\n"
"v_exp_f32_e64 v240, v240 clamp\n"
"v_exp_f32_e64 v241, v241 clamp\n"
"v_exp_f32_e64 v242, v242 clamp\n"
"v_exp_f32_e64 v243, v243 clamp\n"
"v_max_f32 v156, s58, v156\n"
"v_max_f32 v157, s58, v157\n"
"v_max_f32 v158, s58, v158\n"
"v_max_f32 v159, s58, v159\n"
"v_pk_add_f32 v[156:157], v[156:157], v[240:241]\n"
"v_pk_add_f32 v[158:159], v[158:159], v[242:243]\n"
"v_cvt_pk_bf16_f32 v144, v144, v145\n"
"v_cvt_pk_bf16_f32 v145, v146, v147\n"
"v_cvt_pk_bf16_f32 v146, v148, v149\n"
"v_cvt_pk_bf16_f32 v147, v150, v151\n"
"v_cvt_pk_bf16_f32 v148, v152, v153\n"
"v_cvt_pk_bf16_f32 v149, v154, v155\n"
"v_cvt_pk_bf16_f32 v150, v156, v157\n"
"v_cvt_pk_bf16_f32 v151, v158, v159\n"
"v_pk_mul_f32 v[160:161], v[160:161], v[224:225]\n"
"v_pk_mul_f32 v[162:163], v[162:163], v[226:227]\n"
"v_pk_mul_f32 v[164:165], v[164:165], v[228:229]\n"
"v_pk_mul_f32 v[166:167], v[166:167], v[230:231]\n"
"v_pk_mul_f32 v[168:169], v[168:169], v[232:233]\n"
"v_pk_mul_f32 v[170:171], v[170:171], v[234:235]\n"
"v_pk_mul_f32 v[172:173], v[172:173], v[236:237]\n"
"v_pk_mul_f32 v[174:175], v[174:175], v[238:239]\n"
"v_cvt_pk_bf16_f32 v160, v160, v161\n"
"v_cvt_pk_bf16_f32 v161, v162, v163\n"
"v_cvt_pk_bf16_f32 v162, v164, v165\n"
"v_cvt_pk_bf16_f32 v163, v166, v167\n"
"v_cvt_pk_bf16_f32 v164, v168, v169\n"
"v_cvt_pk_bf16_f32 v165, v170, v171\n"
"v_cvt_pk_bf16_f32 v166, v172, v173\n"
"v_cvt_pk_bf16_f32 v167, v174, v175\n"
"v_mfma_f32_32x32x16_bf16 v[224:239], v[144:147], v[160:163], 0\n"
"s_nop 0\n"
"v_mfma_f32_32x32x16_bf16 v[224:239], v[148:151], v[164:167], v[224:239]\n"
"s_nop 11\n"
"ds_write_b128 v244, v[224:227]\n"
"ds_write_b128 v244, v[228:231] offset:1024\n"
"ds_write_b128 v244, v[232:235] offset:2048\n"
"ds_write_b128 v244, v[236:239] offset:3072\n"
"s_waitcnt vmcnt(8)\n"
"s_waitcnt lgkmcnt(0)\n"
"s_barrier\n"
"ds_read_b128 v[160:163], v245\n"
"ds_read_b128 v[164:167], v245 offset:4096\n"
"ds_read_b128 v[168:171], v245 offset:8192\n"
"ds_read_b128 v[172:175], v245 offset:12288\n"
"ds_read_b128 v[200:203], v0 offset:59392\n"
"ds_read_b128 v[204:207], v0 offset:60416\n"
"ds_read_b128 v[208:211], v0 offset:61440\n"
"ds_read_b128 v[212:215], v0 offset:62464\n"
"ds_read_b128 v[216:219], v0 offset:63488\n"
"ds_read_b128 v[220:223], v0 offset:64512\n"
"ds_read_b128 v[192:195], v1\n"
"ds_read_b128 v[196:199], v1 offset:1024\n"
"s_waitcnt lgkmcnt(10)\n"
"v_pk_add_f32 v[160:161], v[160:161], v[164:165]\n"
"v_pk_add_f32 v[162:163], v[162:163], v[166:167]\n"
"s_waitcnt lgkmcnt(8)\n"
"v_pk_add_f32 v[168:169], v[168:169], v[172:173]\n"
"v_pk_add_f32 v[170:171], v[170:171], v[174:175]\n"
"v_pk_add_f32 v[160:161], v[160:161], v[168:169]\n"
"v_pk_add_f32 v[162:163], v[162:163], v[170:171]\n"
"v_min_u32 v160, -2, v160\n"
"v_min_u32 v161, -2, v161\n"
"v_min_u32 v162, -2, v162\n"
"v_min_u32 v163, -2, v163\n"
"buffer_store_dwordx4 v[160:163], v0, s[44:47], s72 offen sc1\n"
"s_waitcnt lgkmcnt(6)\n"
"v_mfma_f32_32x32x16_bf16 v[176:191], v[200:203], v[32:35], 0\n"
"v_mfma_f32_32x32x16_bf16 v[176:191], v[204:207], v[36:39], v[176:191]\n"
"ds_read_b128 v[200:203], v1 offset:2048\n"
"ds_read_b128 v[204:207], v1 offset:3072\n"
"s_waitcnt lgkmcnt(6)\n"
"v_mfma_f32_32x32x16_bf16 v[176:191], v[208:211], v[40:43], v[176:191]\n"
"v_mfma_f32_32x32x16_bf16 v[176:191], v[212:215], v[44:47], v[176:191]\n"
"ds_read_b128 v[208:211], v1 offset:4096\n"
"ds_read_b128 v[212:215], v1 offset:5120\n"
"s_waitcnt lgkmcnt(6)\n"
"v_mfma_f32_32x32x16_bf16 v[176:191], v[216:219], v[48:51], v[176:191]\n"
"v_mfma_f32_32x32x16_bf16 v[176:191], v[220:223], v[52:55], v[176:191]\n"
"ds_read_b128 v[216:219], v1 offset:6144\n"
"ds_read_b128 v[220:223], v1 offset:7168\n"
"s_waitcnt lgkmcnt(6)\n"
"v_mfma_f32_32x32x16_bf16 v[176:191], v[192:195], v[56:59], v[176:191]\n"
"v_mfma_f32_32x32x16_bf16 v[176:191], v[196:199], v[60:63], v[176:191]\n"
"ds_read_b128 v[192:195], v1 offset:8192\n"
"ds_read_b128 v[196:199], v1 offset:9216\n"
"s_waitcnt lgkmcnt(6)\n"
"v_mfma_f32_32x32x16_bf16 v[176:191], v[200:203], v[64:67], v[176:191]\n"
"v_mfma_f32_32x32x16_bf16 v[176:191], v[204:207], v[68:71], v[176:191]\n"
"ds_read_b128 v[200:203], v1 offset:10240\n"
"ds_read_b128 v[204:207], v1 offset:11264\n"
"s_waitcnt lgkmcnt(6)\n"
"v_mfma_f32_32x32x16_bf16 v[176:191], v[208:211], v[72:75], v[176:191]\n"
"v_mfma_f32_32x32x16_bf16 v[176:191], v[212:215], v[76:79], v[176:191]\n"
"ds_read_b128 v[208:211], v1 offset:12288\n"
"ds_read_b128 v[212:215], v1 offset:13312\n"
"s_waitcnt lgkmcnt(6)\n"
"v_mfma_f32_32x32x16_bf16 v[176:191], v[216:219], v[80:83], v[176:191]\n"
"v_mfma_f32_32x32x16_bf16 v[176:191], v[220:223], v[84:87], v[176:191]\n"
"ds_read_b128 v[216:219], v1 offset:14336\n"
"ds_read_b128 v[220:223], v1 offset:15360\n"
"s_waitcnt lgkmcnt(6)\n"
"v_mfma_f32_32x32x16_bf16 v[176:191], v[192:195], v[88:91], v[176:191]\n"
"v_mfma_f32_32x32x16_bf16 v[176:191], v[196:199], v[92:95], v[176:191]\n"
"ds_read_b128 v[192:195], v1 offset:16384\n"
"ds_read_b128 v[196:199], v1 offset:17408\n"
"s_waitcnt lgkmcnt(6)\n"
"v_mfma_f32_32x32x16_bf16 v[176:191], v[200:203], v[96:99], v[176:191]\n"
"v_mfma_f32_32x32x16_bf16 v[176:191], v[204:207], v[100:103], v[176:191]\n"
"ds_read_b128 v[200:203], v1 offset:18432\n"
"ds_read_b128 v[204:207], v1 offset:19456\n"
"s_waitcnt lgkmcnt(6)\n"
"v_mfma_f32_32x32x16_bf16 v[176:191], v[208:211], v[104:107], v[176:191]\n"
"v_mfma_f32_32x32x16_bf16 v[176:191], v[212:215], v[108:111], v[176:191]\n"
"ds_read_b128 v[208:211], v1 offset:20480\n"
"ds_read_b128 v[212:215], v1 offset:21504\n"
"s_waitcnt lgkmcnt(6)\n"
"v_mfma_f32_32x32x16_bf16 v[176:191], v[216:219], v[112:115], v[176:191]\n"
"v_mfma_f32_32x32x16_bf16 v[176:191], v[220:223], v[116:119], v[176:191]\n"
"ds_read_b128 v[216:219], v1 offset:22528\n"
"s_waitcnt lgkmcnt(5)\n"
"v_mfma_f32_32x32x16_bf16 v[176:191], v[192:195], v[120:123], v[176:191]\n"
"v_mfma_f32_32x32x16_bf16 v[176:191], v[196:199], v[124:127], v[176:191]\n"
"s_waitcnt lgkmcnt(3)\n"
"v_mfma_f32_32x32x16_bf16 v[176:191], v[200:203], v[128:131], v[176:191]\n"
"v_mfma_f32_32x32x16_bf16 v[176:191], v[204:207], v[132:135], v[176:191]\n"
"s_waitcnt lgkmcnt(1)\n"
"v_mfma_f32_32x32x16_bf16 v[176:191], v[208:211], v[136:139], v[176:191]\n"
"v_mfma_f32_32x32x16_bf16 v[176:191], v[212:215], v[140:143], v[176:191]\n"
"s_waitcnt lgkmcnt(0)\n"
"v_mfma_f32_32x32x16_bf16 v[176:191], v[216:219], v[20:23], v[176:191]\n"
"s_barrier\n"
"s_nop 10\n"
"v_pk_fma_f32 v[176:177], v[176:177], v[252:253], v[248:249]\n"
"v_pk_fma_f32 v[178:179], v[178:179], v[252:253], v[248:249]\n"
"v_pk_mul_f32 v[240:241], v[176:177], v[250:251]\n"
"v_pk_mul_f32 v[242:243], v[178:179], v[250:251]\n"
"v_exp_f32_e64 v240, v240 clamp\n"
"v_exp_f32_e64 v241, v241 clamp\n"
"v_exp_f32_e64 v242, v242 clamp\n"
"v_exp_f32_e64 v243, v243 clamp\n"
"v_max_f32 v176, s58, v176\n"
"v_max_f32 v177, s58, v177\n"
"v_max_f32 v178, s58, v178\n"
"v_max_f32 v179, s58, v179\n"
"v_pk_add_f32 v[176:177], v[176:177], v[240:241]\n"
"v_pk_add_f32 v[178:179], v[178:179], v[242:243]\n"
"v_pk_fma_f32 v[180:181], v[180:181], v[252:253], v[248:249]\n"
"v_pk_fma_f32 v[182:183], v[182:183], v[252:253], v[248:249]\n"
"v_pk_mul_f32 v[240:241], v[180:181], v[250:251]\n"
"v_pk_mul_f32 v[242:243], v[182:183], v[250:251]\n"
"v_exp_f32_e64 v240, v240 clamp\n"
"v_exp_f32_e64 v241, v241 clamp\n"
"v_exp_f32_e64 v242, v242 clamp\n"
"v_exp_f32_e64 v243, v243 clamp\n"
"v_max_f32 v180, s58, v180\n"
"v_max_f32 v181, s58, v181\n"
"v_max_f32 v182, s58, v182\n"
"v_max_f32 v183, s58, v183\n"
"v_pk_add_f32 v[180:181], v[180:181], v[240:241]\n"
"v_pk_add_f32 v[182:183], v[182:183], v[242:243]\n"
"v_pk_fma_f32 v[184:185], v[184:185], v[252:253], v[248:249]\n"
"v_pk_fma_f32 v[186:187], v[186:187], v[252:253], v[248:249]\n"
"v_pk_mul_f32 v[240:241], v[184:185], v[250:251]\n"
"v_pk_mul_f32 v[242:243], v[186:187], v[250:251]\n"
"v_exp_f32_e64 v240, v240 clamp\n"
"v_exp_f32_e64 v241, v241 clamp\n"
"v_exp_f32_e64 v242, v242 clamp\n"
"v_exp_f32_e64 v243, v243 clamp\n"
"v_max_f32 v184, s58, v184\n"
"v_max_f32 v185, s58, v185\n"
"v_max_f32 v186, s58, v186\n"
"v_max_f32 v187, s58, v187\n"
"v_pk_add_f32 v[184:185], v[184:185], v[240:241]\n"
"v_pk_add_f32 v[186:187], v[186:187], v[242:243]\n"
"v_pk_fma_f32 v[188:189], v[188:189], v[252:253], v[248:249]\n"
"v_pk_fma_f32 v[190:191], v[190:191], v[252:253], v[248:249]\n"
"v_pk_mul_f32 v[240:241], v[188:189], v[250:251]\n"
"v_pk_mul_f32 v[242:243], v[190:191], v[250:251]\n"
"v_exp_f32_e64 v240, v240 clamp\n"
"v_exp_f32_e64 v241, v241 clamp\n"
"v_exp_f32_e64 v242, v242 clamp\n"
"v_exp_f32_e64 v243, v243 clamp\n"
"v_max_f32 v188, s58, v188\n"
"v_max_f32 v189, s58, v189\n"
"v_max_f32 v190, s58, v190\n"
"v_max_f32 v191, s58, v191\n"
"v_pk_add_f32 v[188:189], v[188:189], v[240:241]\n"
"v_pk_add_f32 v[190:191], v[190:191], v[242:243]\n"
"v_cvt_pk_bf16_f32 v176, v176, v177\n"
"v_cvt_pk_bf16_f32 v177, v178, v179\n"
"v_cvt_pk_bf16_f32 v178, v180, v181\n"
"v_cvt_pk_bf16_f32 v179, v182, v183\n"
"v_cvt_pk_bf16_f32 v180, v184, v185\n"
"v_cvt_pk_bf16_f32 v181, v186, v187\n"
"v_cvt_pk_bf16_f32 v182, v188, v189\n"
"buffer_load_dwordx4 v[32:35], v0, s[44:47], s73 offen sc1\n"
"v_cvt_pk_bf16_f32 v183, v190, v191\n"
"v_cndmask_b32_e64 v182, 0, v182, s[62:63]\n"
"v_cndmask_b32_e64 v183, 0, v183, s[62:63]\n"
"s_add_u32 s94, s73, 4096\n"
"buffer_load_dwordx4 v[36:39], v0, s[44:47], s94 offen sc1\n"
"s_add_u32 s94, s73, 8192\n"
"buffer_load_dwordx4 v[40:43], v0, s[44:47], s94 offen sc1\n"
"s_add_u32 s94, s73, 12288\n"
"buffer_load_dwordx4 v[44:47], v0, s[44:47], s94 offen sc1\n"
"s_add_u32 s94, s73, 16384\n"
"buffer_load_dwordx4 v[48:51], v0, s[44:47], s94 offen sc1\n"
"s_add_u32 s94, s73, 20480\n"
"buffer_load_dwordx4 v[52:55], v0, s[44:47], s94 offen sc1\n"
"s_add_u32 s94, s73, 24576\n"
"buffer_load_dwordx4 v[56:59], v0, s[44:47], s94 offen sc1\n"
"s_add_u32 s94, s73, 28672\n"
"buffer_load_dwordx4 v[60:63], v0, s[44:47], s94 offen sc1\n"
"s_add_u32 s94, s73, 32768\n"
"buffer_load_dwordx4 v[64:67], v0, s[44:47], s94 offen sc1\n"
"s_add_u32 s94, s73, 36864\n"
"buffer_load_dwordx4 v[68:71], v0, s[44:47], s94 offen sc1\n"
"s_add_u32 s94, s73, 40960\n"
"buffer_load_dwordx4 v[72:75], v0, s[44:47], s94 offen sc1\n"
"s_add_u32 s94, s73, 45056\n"
"buffer_load_dwordx4 v[76:79], v0, s[44:47], s94 offen sc1\n"
"s_add_u32 s94, s73, 49152\n"
"buffer_load_dwordx4 v[80:83], v0, s[44:47], s94 offen sc1\n"
"s_add_u32 s94, s73, 53248\n"
"buffer_load_dwordx4 v[84:87], v0, s[44:47], s94 offen sc1\n"
"s_add_u32 s94, s73, 57344\n"
"buffer_load_dwordx4 v[88:91], v0, s[44:47], s94 offen sc1\n"
"s_add_u32 s94, s73, 61440\n"
"buffer_load_dwordx4 v[92:95], v0, s[44:47], s94 offen sc1\n"
"buffer_load_dwordx4 v[96:99], v0, s[40:43], s66 offen\n"
"buffer_load_dwordx4 v[100:103], v0, s[40:43], s66 offen offset:1024\n"
"buffer_load_dwordx4 v[104:107], v0, s[40:43], s66 offen offset:2048\n"
"buffer_load_dwordx4 v[108:111], v0, s[40:43], s66 offen offset:3072\n"
"s_add_u32 s94, s66, 4096\n"
"buffer_load_dwordx4 v[112:115], v0, s[40:43], s94 offen\n"
"buffer_load_dwordx4 v[116:119], v0, s[40:43], s94 offen offset:1024\n"
"buffer_load_dwordx4 v[120:123], v0, s[40:43], s94 offen offset:2048\n"
"buffer_load_dwordx4 v[124:127], v0, s[40:43], s94 offen offset:3072\n"
"L_ks_1:\n"
"s_waitcnt vmcnt(22)\n"
"v_pk_add_f32 v[168:169], v[32:33], v[36:37]\n"
"v_pk_add_f32 v[170:171], v[34:35], v[38:39]\n"
"s_waitcnt vmcnt(21)\n"
"v_pk_add_f32 v[168:169], v[168:169], v[40:41]\n"
"v_pk_add_f32 v[170:171], v[170:171], v[42:43]\n"
"s_waitcnt vmcnt(20)\n"
"v_pk_add_f32 v[168:169], v[168:169], v[44:45]\n"
"v_pk_add_f32 v[170:171], v[170:171], v[46:47]\n"
"s_waitcnt vmcnt(19)\n"
"v_pk_add_f32 v[168:169], v[168:169], v[48:49]\n"
"v_pk_add_f32 v[170:171], v[170:171], v[50:51]\n"
"s_waitcnt vmcnt(18)\n"
"v_pk_add_f32 v[168:169], v[168:169], v[52:53]\n"
"v_pk_add_f32 v[170:171], v[170:171], v[54:55]\n"
"s_waitcnt vmcnt(17)\n"
"v_pk_add_f32 v[168:169], v[168:169], v[56:57]\n"
"v_pk_add_f32 v[170:171], v[170:171], v[58:59]\n"
"s_waitcnt vmcnt(16)\n"
"v_pk_add_f32 v[168:169], v[168:169], v[60:61]\n"
"v_pk_add_f32 v[170:171], v[170:171], v[62:63]\n"
"s_waitcnt vmcnt(15)\n"
"v_pk_add_f32 v[168:169], v[168:169], v[64:65]\n"
"v_pk_add_f32 v[170:171], v[170:171], v[66:67]\n"
"s_waitcnt vmcnt(14)\n"
"v_pk_add_f32 v[168:169], v[168:169], v[68:69]\n"
"v_pk_add_f32 v[170:171], v[170:171], v[70:71]\n"
"s_waitcnt vmcnt(13)\n"
"v_pk_add_f32 v[168:169], v[168:169], v[72:73]\n"
"v_pk_add_f32 v[170:171], v[170:171], v[74:75]\n"
"s_waitcnt vmcnt(12)\n"
"v_pk_add_f32 v[168:169], v[168:169], v[76:77]\n"
"v_pk_add_f32 v[170:171], v[170:171], v[78:79]\n"
"s_waitcnt vmcnt(11)\n"
"v_pk_add_f32 v[168:169], v[168:169], v[80:81]\n"
"v_pk_add_f32 v[170:171], v[170:171], v[82:83]\n"
"s_waitcnt vmcnt(10)\n"
"v_pk_add_f32 v[168:169], v[168:169], v[84:85]\n"
"v_pk_add_f32 v[170:171], v[170:171], v[86:87]\n"
"s_waitcnt vmcnt(9)\n"
"v_pk_add_f32 v[168:169], v[168:169], v[88:89]\n"
"v_pk_add_f32 v[170:171], v[170:171], v[90:91]\n"
"s_waitcnt vmcnt(8)\n"
"v_pk_add_f32 v[168:169], v[168:169], v[92:93]\n"
"v_pk_add_f32 v[170:171], v[170:171], v[94:95]\n"
"v_add_f32 v240, v168, v169\n"
"v_add_f32 v241, v170, v171\n"
"v_cvt_pk_bf16_f32 v242, v168, v169\n"
"v_cvt_pk_bf16_f32 v243, v170, v171\n"
"v_cmp_u_f32 vcc, v240, v241\n"
"v_add_u32 v244, s74, v0\n"
"s_cbranch_vccnz L_kn_s_1\n"
"L_kv_go_1:\n"
"ds_write_b64 v244, v[242:243]\n"
"s_waitcnt lgkmcnt(0)\n"
"s_barrier\n"
"ds_read_b128 v[160:163], v2 offset:17920\n"
"ds_read_b128 v[164:167], v2 offset:18944\n"
"s_add_u32 m0, s87, 0\n"
"s_add_u32 s95, s68, 0\n"
"buffer_load_dwordx4 v0, s[40:43], s95 offen lds\n"
"buffer_load_dwordx4 v0, s[40:43], s95 offen offset:1024 lds\n"
"s_waitcnt lgkmcnt(1)\n"
"v_mfma_f32_32x32x16_bf16 v[144:159], v[160:163], v[176:179], 0\n"
"s_waitcnt lgkmcnt(0)\n"
"v_mfma_f32_32x32x16_bf16 v[144:159], v[164:167], v[180:183], v[144:159]\n"
"buffer_load_dwordx4 v0, s[40:43], s95 offen offset:2048 lds\n"
"buffer_load_dwordx4 v0, s[40:43], s95 offen offset:3072 lds\n"
"s_nop 9\n"
"v_mov_b32 v240, v156\n"
"v_mov_b32 v241, v156\n"
"s_nop 1\n"
"v_permlane32_swap_b32 v240, v241\n"
"v_add_f32 v241, s58, v241\n"
"v_rcp_f32 v240, v241\n"
"v_rcp_f32 v241, v241\n"
"s_nop 0\n"
"v_pk_mul_f32 v[144:145], v[144:145], v[240:241]\n"
"v_pk_mul_f32 v[146:147], v[146:147], v[240:241]\n"
"v_pk_mul_f32 v[148:149], v[148:149], v[240:241]\n"
"v_pk_mul_f32 v[150:151], v[150:151], v[240:241]\n"
"v_pk_mul_f32 v[152:153], v[152:153], v[240:241]\n"
"v_pk_mul_f32 v[154:155], v[154:155], v[240:241]\n"
"v_pk_mul_f32 v[156:157], v[156:157], v[240:241]\n"
"v_pk_mul_f32 v[158:159], v[158:159], v[240:241]\n"
"v_cvt_pk_bf16_f32 v144, v144, v145\n"
"v_cvt_pk_bf16_f32 v145, v146, v147\n"
"v_cvt_pk_bf16_f32 v146, v148, v149\n"
"v_cvt_pk_bf16_f32 v147, v150, v151\n"
"v_min_u32 v144, -2, v144\n"
"v_min_u32 v145, -2, v145\n"
"v_min_u32 v146, -2, v146\n"
"v_min_u32 v147, -2, v147\n"
"buffer_store_dwordx4 v[144:147], v0, s[44:47], s75 offen sc1\n"
"v_cvt_pk_bf16_f32 v148, v152, v153\n"
"v_cvt_pk_bf16_f32 v149, v154, v155\n"
"v_cvt_pk_bf16_f32 v150, v156, v157\n"
"v_cvt_pk_bf16_f32 v151, v158, v159\n"
"v_min_u32 v148, -2, v148\n"
"v_min_u32 v149, -2, v149\n"
"v_min_u32 v150, -2, v150\n"
"v_min_u32 v151, -2, v151\n"
"buffer_store_dwordx4 v[148:151], v0, s[44:47], s75 offen offset:1024 sc1\n"
"s_add_u32 m0, s87, 4096\n"
"s_add_u32 s95, s68, 4096\n"
"buffer_load_dwordx4 v0, s[40:43], s95 offen lds\n"
"buffer_load_dwordx4 v0, s[40:43], s95 offen offset:1024 lds\n"
"buffer_load_dwordx4 v0, s[40:43], s95 offen offset:2048 lds\n"
"buffer_load_dwordx4 v0, s[40:43], s95 offen offset:3072 lds\n"
"s_add_u32 m0, s87, 8192\n"
"s_add_u32 s95, s68, 8192\n"
"buffer_load_dwordx4 v0, s[40:43], s95 offen lds\n"
"buffer_load_dwordx4 v0, s[40:43], s95 offen offset:1024 lds\n"
"buffer_load_dwordx4 v0, s[40:43], s95 offen offset:2048 lds\n"
"buffer_load_dwordx4 v0, s[40:43], s95 offen offset:3072 lds\n"
"s_cmp_eq_u32 %[act], 0\n"
"s_cbranch_scc1 L_wo_skip_1\n"
"v_lshlrev_b32 v160, 16, v8\n"
"v_and_b32 v161, 0xffff0000, v8\n"
"v_lshlrev_b32 v162, 16, v9\n"
"v_and_b32 v163, 0xffff0000, v9\n"
"v_lshlrev_b32 v164, 16, v10\n"
"v_and_b32 v165, 0xffff0000, v10\n"
"v_lshlrev_b32 v166, 16, v11\n"
"v_and_b32 v167, 0xffff0000, v11\n"
"v_lshlrev_b32 v168, 16, v12\n"
"v_and_b32 v169, 0xffff0000, v12\n"
"v_lshlrev_b32 v170, 16, v13\n"
"v_and_b32 v171, 0xffff0000, v13\n"
"v_lshlrev_b32 v172, 16, v14\n"
"v_and_b32 v173, 0xffff0000, v14\n"
"v_lshlrev_b32 v174, 16, v15\n"
"v_and_b32 v175, 0xffff0000, v15\n"
"s_sleep 6\n"
"L_op_1:\n"
"buffer_load_dword v19, v29, s[44:47], 0 offen sc1\n"
"s_waitcnt vmcnt(0)\n"
"v_cmp_eq_u32 vcc, -1, v19\n"
"s_cbranch_vccnz L_op_s_1\n"
"L_op_go_1:\n"
"v_mov_b32 v19, 0\n"
"buffer_load_dwordx4 v[64:67], v0, s[44:47], s77 offen sc1\n"
"buffer_load_dwordx4 v[68:71], v0, s[44:47], s77 offen offset:1024 sc1\n"
"buffer_load_dwordx4 v[72:75], v0, s[44:47], s77 offen offset:2048 sc1\n"
"buffer_load_dwordx4 v[76:79], v0, s[44:47], s77 offen offset:3072 sc1\n"
"s_add_u32 s94, s77, 4096\n"
"buffer_load_dwordx4 v[80:83], v0, s[44:47], s94 offen sc1\n"
"buffer_load_dwordx4 v[84:87], v0, s[44:47], s94 offen offset:1024 sc1\n"
"buffer_load_dwordx4 v[88:91], v0, s[44:47], s94 offen offset:2048 sc1\n"
"buffer_load_dwordx4 v[92:95], v0, s[44:47], s94 offen offset:3072 sc1\n"
"buffer_load_dwordx4 v[32:35], v0, s[44:47], s76 offen sc1\n"
"buffer_load_dwordx4 v[36:39], v0, s[44:47], s76 offen offset:1024 sc1\n"
"buffer_load_dwordx4 v[40:43], v0, s[44:47], s76 offen offset:2048 sc1\n"
"buffer_load_dwordx4 v[44:47], v0, s[44:47], s76 offen offset:3072 sc1\n"
"s_waitcnt vmcnt(11)\n"
"v_mfma_f32_32x32x16_bf16 v[208:223], a[224:227], v[64:67], 0\n"
"v_max3_u32 v19, v19, v64, v65\n"
"s_add_u32 s94, s76, 4096\n"
"buffer_load_dwordx4 v[48:51], v0, s[44:47], s94 offen sc1\n"
"v_mfma_f32_32x32x16_bf16 v[176:191], v[96:99], v[64:67], 0\n"
"v_max3_u32 v19, v19, v66, v67\n"
"s_waitcnt vmcnt(11)\n"
"v_mfma_f32_32x32x16_bf16 v[208:223], a[228:231], v[68:71], v[208:223]\n"
"v_max3_u32 v19, v19, v68, v69\n"
"buffer_load_dwordx4 v[52:55], v0, s[44:47], s94 offen offset:1024 sc1\n"
"v_mfma_f32_32x32x16_bf16 v[176:191], v[100:103], v[68:71], v[176:191]\n"
"v_max3_u32 v19, v19, v70, v71\n"
"s_waitcnt vmcnt(11)\n"
"v_mfma_f32_32x32x16_bf16 v[208:223], a[232:235], v[72:75], v[208:223]\n"
"v_max3_u32 v19, v19, v72, v73\n"
"buffer_load_dwordx4 v[56:59], v0, s[44:47], s94 offen offset:2048 sc1\n"
"v_mfma_f32_32x32x16_bf16 v[176:191], v[104:107], v[72:75], v[176:191]\n"
"v_max3_u32 v19, v19, v74, v75\n"
"buffer_load_dwordx4 v[60:63], v0, s[44:47], s94 offen offset:3072 sc1\n"
"s_waitcnt vmcnt(12)\n"
"v_mfma_f32_32x32x16_bf16 v[208:223], a[236:239], v[76:79], v[208:223]\n"
"v_max3_u32 v19, v19, v76, v77\n"
"v_mfma_f32_32x32x16_bf16 v[176:191], v[108:111], v[76:79], v[176:191]\n"
"v_max3_u32 v19, v19, v78, v79\n"
"s_add_u32 m0, s87, 12288\n"
"s_add_u32 s95, s68, 12288\n"
"buffer_load_dwordx4 v0, s[40:43], s95 offen lds\n"
"s_waitcnt vmcnt(12)\n"
"v_mfma_f32_32x32x16_bf16 v[208:223], a[240:243], v[80:83], v[208:223]\n"
"v_max3_u32 v19, v19, v80, v81\n"
"v_mfma_f32_32x32x16_bf16 v[176:191], v[112:115], v[80:83], v[176:191]\n"
"v_max3_u32 v19, v19, v82, v83\n"
"buffer_load_dwordx4 v0, s[40:43], s95 offen offset:1024 lds\n"
"s_waitcnt vmcnt(12)\n"
"v_mfma_f32_32x32x16_bf16 v[208:223], a[244:247], v[84:87], v[208:223]\n"
"v_max3_u32 v19, v19, v84, v85\n"
"buffer_load_dwordx4 v0, s[40:43], s95 offen offset:2048 lds\n"
"v_mfma_f32_32x32x16_bf16 v[176:191], v[116:119], v[84:87], v[176:191]\n"
"v_max3_u32 v19, v19, v86, v87\n"
"s_waitcnt vmcnt(12)\n"
"v_mfma_f32_32x32x16_bf16 v[208:223], a[248:251], v[88:91], v[208:223]\n"
"v_max3_u32 v19, v19, v88, v89\n"
"buffer_load_dwordx4 v0, s[40:43], s95 offen offset:3072 lds\n"
"v_mfma_f32_32x32x16_bf16 v[176:191], v[120:123], v[88:91], v[176:191]\n"
"v_max3_u32 v19, v19, v90, v91\n"
"s_waitcnt vmcnt(12)\n"
"v_mfma_f32_32x32x16_bf16 v[208:223], a[252:255], v[92:95], v[208:223]\n"
"v_max3_u32 v19, v19, v92, v93\n"
"s_add_u32 m0, s87, 16384\n"
"s_add_u32 s95, s68, 16384\n"
"buffer_load_dwordx4 v0, s[40:43], s95 offen lds\n"
"v_mfma_f32_32x32x16_bf16 v[176:191], v[124:127], v[92:95], v[176:191]\n"
"v_max3_u32 v19, v19, v94, v95\n"
"buffer_load_dwordx4 v0, s[40:43], s95 offen offset:1024 lds\n"
"s_waitcnt vmcnt(13)\n"
"v_mfma_f32_32x32x16_bf16 v[192:207], a[224:227], v[32:35], 0\n"
"v_max3_u32 v19, v19, v32, v33\n"
"v_max3_u32 v19, v19, v34, v35\n"
"s_waitcnt vmcnt(12)\n"
"v_mfma_f32_32x32x16_bf16 v[192:207], a[228:231], v[36:39], v[192:207]\n"
"v_max3_u32 v19, v19, v36, v37\n"
"v_max3_u32 v19, v19, v38, v39\n"
"buffer_load_dwordx4 v0, s[40:43], s95 offen offset:2048 lds\n"
"s_nop 0\n"
"ds_write_b128 v6, v[176:179]\n"
"s_waitcnt vmcnt(12)\n"
"v_mfma_f32_32x32x16_bf16 v[192:207], a[232:235], v[40:43], v[192:207]\n"
"v_max3_u32 v19, v19, v40, v41\n"
"v_max3_u32 v19, v19, v42, v43\n"
"ds_write_b128 v6, v[180:183] offset:1024\n"
"s_waitcnt vmcnt(11)\n"
"v_mfma_f32_32x32x16_bf16 v[192:207], a[236:239], v[44:47], v[192:207]\n"
"v_max3_u32 v19, v19, v44, v45\n"
"v_max3_u32 v19, v19, v46, v47\n"
"buffer_load_dwordx4 v0, s[40:43], s95 offen offset:3072 lds\n"
"ds_write_b128 v6, v[184:187] offset:2048\n"
"s_waitcnt vmcnt(11)\n"
"v_mfma_f32_32x32x16_bf16 v[192:207], a[240:243], v[48:51], v[192:207]\n"
"v_max3_u32 v19, v19, v48, v49\n"
"v_max3_u32 v19, v19, v50, v51\n"
"s_add_u32 m0, s87, 20480\n"
"s_add_u32 s95, s68, 20480\n"
"buffer_load_dwordx4 v0, s[40:43], s95 offen lds\n"
"s_waitcnt vmcnt(11)\n"
"v_mfma_f32_32x32x16_bf16 v[192:207], a[244:247], v[52:55], v[192:207]\n"
"v_max3_u32 v19, v19, v52, v53\n"
"v_max3_u32 v19, v19, v54, v55\n"
"ds_write_b128 v6, v[188:191] offset:3072\n"
"s_waitcnt vmcnt(10)\n"
"v_mfma_f32_32x32x16_bf16 v[192:207], a[248:251], v[56:59], v[192:207]\n"
"v_max3_u32 v19, v19, v56, v57\n"
"v_max3_u32 v19, v19, v58, v59\n"
"buffer_load_dwordx4 v0, s[40:43], s95 offen offset:1024 lds\n"
"ds_write_b128 v24, v[208:211]\n"
"s_waitcnt vmcnt(10)\n"
"v_mfma_f32_32x32x16_bf16 v[192:207], a[252:255], v[60:63], v[192:207]\n"
"v_max3_u32 v19, v19, v60, v61\n"
"v_max3_u32 v19, v19, v62, v63\n"
"ds_write_b128 v24, v[212:215] offset:1024\n"
"ds_write_b128 v24, v[216:219] offset:2048\n"
"ds_write_b128 v24, v[220:223] offset:3072\n"
"v_cmp_eq_u32 vcc, -1, v19\n"
"s_cbranch_vccnz L_ov_s_1\n"
"L_ov_go_1:\n"
"v_mfma_f32_32x32x16_bf16 v[160:175], v[96:99], v[32:35], v[160:175]\n"
"v_mfma_f32_32x32x16_bf16 v[160:175], v[100:103], v[36:39], v[160:175]\n"
"s_nop 2\n"
"ds_write_b128 v7, v[192:195]\n"
"ds_write_b128 v7, v[196:199] offset:1024\n"
"ds_write_b128 v7, v[200:203] offset:2048\n"
"ds_write_b128 v7, v[204:207] offset:3072\n"
"s_waitcnt lgkmcnt(0)\n"
"s_barrier\n"
"ds_read_b128 v[64:67], v5\n"
"ds_read_b128 v[80:83], v5 offset:4096\n"
"ds_read_b128 v[128:131], v5 offset:8192\n"
"ds_read_b128 v[68:71], v5 offset:1024\n"
"ds_read_b128 v[84:87], v5 offset:5120\n"
"ds_read_b128 v[132:135], v5 offset:9216\n"
"ds_read_b128 v[72:75], v5 offset:2048\n"
"ds_read_b128 v[88:91], v5 offset:6144\n"
"ds_read_b128 v[136:139], v5 offset:10240\n"
"ds_read_b128 v[76:79], v5 offset:3072\n"
"ds_read_b128 v[92:95], v5 offset:7168\n"
"ds_read_b128 v[140:143], v5 offset:11264\n"
"v_mfma_f32_32x32x16_bf16 v[160:175], v[104:107], v[40:43], v[160:175]\n"
"v_mfma_f32_32x32x16_bf16 v[160:175], v[108:111], v[44:47], v[160:175]\n"
"v_mfma_f32_32x32x16_bf16 v[160:175], v[112:115], v[48:51], v[160:175]\n"
"s_waitcnt lgkmcnt(10)\n"
"v_add_f32 v64, v64, v80\n"
"v_add_f32 v65, v65, v81\n"
"v_add_f32 v66, v66, v82\n"
"v_add_f32 v67, v67, v83\n"
"s_waitcnt lgkmcnt(9)\n"
"v_add_f32 v64, v64, v128\n"
"v_add_f32 v65, v65, v129\n"
"v_add_f32 v66, v66, v130\n"
"v_add_f32 v67, v67, v131\n"
"s_waitcnt lgkmcnt(7)\n"
"v_add_f32 v68, v68, v84\n"
"v_mfma_f32_32x32x16_bf16 v[160:175], v[116:119], v[52:55], v[160:175]\n"
"v_add_f32 v69, v69, v85\n"
"v_add_f32 v70, v70, v86\n"
"v_add_f32 v71, v71, v87\n"
"s_waitcnt lgkmcnt(6)\n"
"v_add_f32 v68, v68, v132\n"
"v_add_f32 v69, v69, v133\n"
"v_add_f32 v70, v70, v134\n"
"v_add_f32 v71, v71, v135\n"
"s_waitcnt lgkmcnt(4)\n"
"v_add_f32 v72, v72, v88\n"
"v_mfma_f32_32x32x16_bf16 v[160:175], v[120:123], v[56:59], v[160:175]\n"
"v_add_f32 v73, v73, v89\n"
"v_add_f32 v74, v74, v90\n"
"v_add_f32 v75, v75, v91\n"
"s_waitcnt lgkmcnt(3)\n"
"v_add_f32 v72, v72, v136\n"
"v_add_f32 v73, v73, v137\n"
"v_add_f32 v74, v74, v138\n"
"v_add_f32 v75, v75, v139\n"
"s_waitcnt lgkmcnt(1)\n"
"v_add_f32 v76, v76, v92\n"
"v_mfma_f32_32x32x16_bf16 v[160:175], v[124:127], v[60:63], v[160:175]\n"
"v_add_f32 v77, v77, v93\n"
"v_add_f32 v78, v78, v94\n"
"v_add_f32 v79, v79, v95\n"
"s_waitcnt lgkmcnt(0)\n"
"v_add_f32 v76, v76, v140\n"
"v_add_f32 v77, v77, v141\n"
"v_add_f32 v78, v78, v142\n"
"v_add_f32 v79, v79, v143\n"
"s_nop 3\n"
"v_pk_add_f32 v[160:161], v[160:161], v[64:65]\n"
"v_pk_add_f32 v[162:163], v[162:163], v[66:67]\n"
"v_cvt_pk_bf16_f32 v8, v160, v161\n"
"v_cvt_pk_bf16_f32 v9, v162, v163\n"
"v_min_u32 v8, -2, v8\n"
"v_min_u32 v9, -2, v9\n"
"s_add_u32 s60, s78, 2064384\n"
"v_pk_add_f32 v[164:165], v[164:165], v[68:69]\n"
"v_pk_add_f32 v[166:167], v[166:167], v[70:71]\n"
"v_cvt_pk_bf16_f32 v10, v164, v165\n"
"v_cvt_pk_bf16_f32 v11, v166, v167\n"
"v_min_u32 v10, -2, v10\n"
"v_min_u32 v11, -2, v11\n"
"buffer_store_dwordx4 v[8:11], v0, s[44:47], s60 offen sc1\n"
"v_pk_add_f32 v[168:169], v[168:169], v[72:73]\n"
"v_pk_add_f32 v[170:171], v[170:171], v[74:75]\n"
"v_cvt_pk_bf16_f32 v12, v168, v169\n"
"v_cvt_pk_bf16_f32 v13, v170, v171\n"
"v_min_u32 v12, -2, v12\n"
"v_min_u32 v13, -2, v13\n"
"v_pk_add_f32 v[172:173], v[172:173], v[76:77]\n"
"v_pk_add_f32 v[174:175], v[174:175], v[78:79]\n"
"v_cvt_pk_bf16_f32 v14, v172, v173\n"
"v_cvt_pk_bf16_f32 v15, v174, v175\n"
"v_min_u32 v14, -2, v14\n"
"v_min_u32 v15, -2, v15\n"
"buffer_store_dwordx4 v[12:15], v0, s[44:47], s60 offen offset:1024 sc1\n"
"v_mov_b32 v240, 0\n"
"v_mov_b32 v241, 0\n"
"v_dot2c_f32_bf16 v240, 0x3f803f80, v8\n"
"v_dot2c_f32_bf16 v241, v8, v8\n"
"v_dot2c_f32_bf16 v240, 0x3f803f80, v9\n"
"v_dot2c_f32_bf16 v241, v9, v9\n"
"v_dot2c_f32_bf16 v240, 0x3f803f80, v10\n"
"v_dot2c_f32_bf16 v241, v10, v10\n"
"v_dot2c_f32_bf16 v240, 0x3f803f80, v11\n"
"v_dot2c_f32_bf16 v241, v11, v11\n"
"v_dot2c_f32_bf16 v240, 0x3f803f80, v12\n"
"v_dot2c_f32_bf16 v241, v12, v12\n"
"v_dot2c_f32_bf16 v240, 0x3f803f80, v13\n"
"v_dot2c_f32_bf16 v241, v13, v13\n"
"v_dot2c_f32_bf16 v240, 0x3f803f80, v14\n"
"v_dot2c_f32_bf16 v241, v14, v14\n"
"v_dot2c_f32_bf16 v240, 0x3f803f80, v15\n"
"v_dot2c_f32_bf16 v241, v15, v15\n"
"s_nop 1\n"
"v_mov_b32 v242, v240\n"
"v_mov_b32 v243, v241\n"
"s_nop 0\n"
"v_permlane32_swap_b32 v240, v242\n"
"v_permlane32_swap_b32 v241, v243\n"
"v_add_f32 v240, v240, v242\n"
"v_add_f32 v241, v241, v243\n"
"v_min_u32 v240, -2, v240\n"
"v_min_u32 v241, -2, v241\n"
"s_add_u32 s60, s79, 3899392\n"
"v_mov_b32 v242, 0x40000000\n"
"v_cndmask_b32_e64 v242, v242, v4, s[62:63]\n"
"buffer_store_dwordx2 v[240:241], v242, s[44:47], s60 offen sc1\n"
"s_branch L_wo_done_1\n"
"L_wo_skip_1:\n"
"s_add_u32 m0, s87, 12288\n"
"s_add_u32 s95, s68, 12288\n"
"buffer_load_dwordx4 v0, s[40:43], s95 offen lds\n"
"buffer_load_dwordx4 v0, s[40:43], s95 offen offset:1024 lds\n"
"buffer_load_dwordx4 v0, s[40:43], s95 offen offset:2048 lds\n"
"buffer_load_dwordx4 v0, s[40:43], s95 offen offset:3072 lds\n"
"s_add_u32 m0, s87, 16384\n"
"s_add_u32 s95, s68, 16384\n"
"buffer_load_dwordx4 v0, s[40:43], s95 offen lds\n"
"buffer_load_dwordx4 v0, s[40:43], s95 offen offset:1024 lds\n"
"buffer_load_dwordx4 v0, s[40:43], s95 offen offset:2048 lds\n"
"buffer_load_dwordx4 v0, s[40:43], s95 offen offset:3072 lds\n"
"s_add_u32 m0, s87, 20480\n"
"s_add_u32 s95, s68, 20480\n"
"buffer_load_dwordx4 v0, s[40:43], s95 offen lds\n"
"buffer_load_dwordx4 v0, s[40:43], s95 offen offset:1024 lds\n"
"L_ip_1:\n"
"buffer_load_dword v19, v29, s[44:47], 0 offen sc1\n"
"s_waitcnt vmcnt(0)\n"
"v_cmp_eq_u32 vcc, -1, v19\n"
"s_cbranch_vccnz L_ip_s_1\n"
"L_ip_go_1:\n"
"s_barrier\n"
"s_sleep 24\n"
"L_wo_done_1:\n"
"s_mov_b32 s56, 0\n"
"s_sleep 12\n"
"L_x1p_1:\n"
"buffer_load_dword v19, v3, s[44:47], s80 offen sc1\n"
"s_waitcnt vmcnt(0)\n"
"v_cmp_eq_u32 vcc, -1, v19\n"
"s_cbranch_vccnz L_x1p_s_1\n"
"L_x1p_go_1:\n"
"v_mov_b32 v19, 0\n"
"buffer_load_dwordx4 v[32:35], v0, s[44:47], s80 offen sc1\n"
"buffer_load_dwordx4 v[36:39], v0, s[44:47], s80 offen offset:1024 sc1\n"
"buffer_load_dwordx4 v[40:43], v0, s[44:47], s80 offen offset:2048 sc1\n"
"buffer_load_dwordx4 v[44:47], v0, s[44:47], s80 offen offset:3072 sc1\n"
"s_add_u32 s94, s80, 4096\n"
"buffer_load_dwordx4 v[48:51], v0, s[44:47], s94 offen sc1\n"
"buffer_load_dwordx4 v[52:55], v0, s[44:47], s94 offen offset:1024 sc1\n"
"buffer_load_dwordx4 v[56:59], v0, s[44:47], s94 offen offset:2048 sc1\n"
"buffer_load_dwordx4 v[60:63], v0, s[44:47], s94 offen offset:3072 sc1\n"
"s_add_u32 s94, s80, 8192\n"
"buffer_load_dwordx4 v[64:67], v0, s[44:47], s94 offen sc1\n"
"buffer_load_dwordx4 v[68:71], v0, s[44:47], s94 offen offset:1024 sc1\n"
"buffer_load_dwordx4 v[72:75], v0, s[44:47], s94 offen offset:2048 sc1\n"
"buffer_load_dwordx4 v[76:79], v0, s[44:47], s94 offen offset:3072 sc1\n"
"buffer_load_dwordx2 v[192:193], v4, s[44:47], s81 offen sc1\n"
"buffer_load_dwordx2 v[194:195], v4, s[44:47], s81 offen offset:512 sc1\n"
"buffer_load_dwordx2 v[196:197], v4, s[44:47], s81 offen offset:1024 sc1\n"
"buffer_load_dwordx2 v[198:199], v4, s[44:47], s81 offen offset:1536 sc1\n"
"buffer_load_dwordx2 v[200:201], v4, s[44:47], s81 offen offset:2048 sc1\n"
"buffer_load_dwordx2 v[202:203], v4, s[44:47], s81 offen offset:2560 sc1\n"
"buffer_load_dwordx2 v[204:205], v4, s[44:47], s81 offen offset:3072 sc1\n"
"s_cmp_lg_u32 s56, 0\n"
"s_cbranch_scc1 L_gb_1\n"
"s_barrier\n"
"s_mov_b32 s56, 1\n"
"L_gb_1:\n"
"ds_read_b128 v[208:211], v0\n"
"ds_read_b128 v[212:215], v0 offset:29696\n"
"ds_read_b128 v[216:219], v0 offset:1024\n"
"ds_read_b128 v[220:223], v0 offset:30720\n"
"ds_read_b128 v[224:227], v0 offset:2048\n"
"ds_read_b128 v[228:231], v0 offset:31744\n"
"s_waitcnt vmcnt(18) lgkmcnt(4)\n"
"v_mfma_f32_32x32x16_bf16 v[144:159], v[208:211], v[32:35], 0\n"
"v_max3_u32 v19, v19, v32, v33\n"
"s_add_u32 s94, s80, 12288\n"
"buffer_load_dwordx4 v[80:83], v0, s[44:47], s94 offen sc1\n"
"v_mfma_f32_32x32x16_bf16 v[160:175], v[212:215], v[32:35], 0\n"
"ds_read_b128 v[208:211], v0 offset:3072\n"
"ds_read_b128 v[212:215], v0 offset:32768\n"
"v_max3_u32 v19, v19, v34, v35\n"
"s_waitcnt vmcnt(18) lgkmcnt(4)\n"
"v_mfma_f32_32x32x16_bf16 v[144:159], v[216:219], v[36:39], v[144:159]\n"
"v_max3_u32 v19, v19, v36, v37\n"
"buffer_load_dwordx4 v[84:87], v0, s[44:47], s94 offen offset:1024 sc1\n"
"v_mfma_f32_32x32x16_bf16 v[160:175], v[220:223], v[36:39], v[160:175]\n"
"ds_read_b128 v[216:219], v0 offset:4096\n"
"ds_read_b128 v[220:223], v0 offset:33792\n"
"v_max3_u32 v19, v19, v38, v39\n"
"s_waitcnt vmcnt(18) lgkmcnt(4)\n"
"v_mfma_f32_32x32x16_bf16 v[144:159], v[224:227], v[40:43], v[144:159]\n"
"v_max3_u32 v19, v19, v40, v41\n"
"buffer_load_dwordx4 v[88:91], v0, s[44:47], s94 offen offset:2048 sc1\n"
"v_mfma_f32_32x32x16_bf16 v[160:175], v[228:231], v[40:43], v[160:175]\n"
"ds_read_b128 v[224:227], v0 offset:5120\n"
"ds_read_b128 v[228:231], v0 offset:34816\n"
"v_max3_u32 v19, v19, v42, v43\n"
"s_waitcnt vmcnt(18) lgkmcnt(4)\n"
"v_mfma_f32_32x32x16_bf16 v[144:159], v[208:211], v[44:47], v[144:159]\n"
"v_max3_u32 v19, v19, v44, v45\n"
"buffer_load_dwordx4 v[92:95], v0, s[44:47], s94 offen offset:3072 sc1\n"
"v_mfma_f32_32x32x16_bf16 v[160:175], v[212:215], v[44:47], v[160:175]\n"
"ds_read_b128 v[208:211], v0 offset:6144\n"
"ds_read_b128 v[212:215], v0 offset:35840\n"
"v_max3_u32 v19, v19, v46, v47\n"
"s_waitcnt vmcnt(18) lgkmcnt(4)\n"
"v_mfma_f32_32x32x16_bf16 v[144:159], v[216:219], v[48:51], v[144:159]\n"
"v_max3_u32 v19, v19, v48, v49\n"
"s_add_u32 s94, s80, 16384\n"
"buffer_load_dwordx4 v[96:99], v0, s[44:47], s94 offen sc1\n"
"v_mfma_f32_32x32x16_bf16 v[160:175], v[220:223], v[48:51], v[160:175]\n"
"ds_read_b128 v[216:219], v0 offset:7168\n"
"ds_read_b128 v[220:223], v0 offset:36864\n"
"v_max3_u32 v19, v19, v50, v51\n"
"s_waitcnt vmcnt(18) lgkmcnt(4)\n"
"v_mfma_f32_32x32x16_bf16 v[144:159], v[224:227], v[52:55], v[144:159]\n"
"v_max3_u32 v19, v19, v52, v53\n"
"buffer_load_dwordx4 v[100:103], v0, s[44:47], s94 offen offset:1024 sc1\n"
"v_mfma_f32_32x32x16_bf16 v[160:175], v[228:231], v[52:55], v[160:175]\n"
"ds_read_b128 v[224:227], v0 offset:8192\n"
"ds_read_b128 v[228:231], v0 offset:37888\n"
"v_max3_u32 v19, v19, v54, v55\n"
"s_waitcnt vmcnt(18) lgkmcnt(4)\n"
"v_mfma_f32_32x32x16_bf16 v[144:159], v[208:211], v[56:59], v[144:159]\n"
"v_max3_u32 v19, v19, v56, v57\n"
"buffer_load_dwordx4 v[104:107], v0, s[44:47], s94 offen offset:2048 sc1\n"
"v_mfma_f32_32x32x16_bf16 v[160:175], v[212:215], v[56:59], v[160:175]\n"
"ds_read_b128 v[208:211], v0 offset:9216\n"
"ds_read_b128 v[212:215], v0 offset:38912\n"
"v_max3_u32 v19, v19, v58, v59\n"
"s_waitcnt vmcnt(18) lgkmcnt(4)\n"
"v_mfma_f32_32x32x16_bf16 v[144:159], v[216:219], v[60:63], v[144:159]\n"
"v_max3_u32 v19, v19, v60, v61\n"
"buffer_load_dwordx4 v[108:111], v0, s[44:47], s94 offen offset:3072 sc1\n"
"v_mfma_f32_32x32x16_bf16 v[160:175], v[220:223], v[60:63], v[160:175]\n"
"ds_read_b128 v[216:219], v0 offset:10240\n"
"ds_read_b128 v[220:223], v0 offset:39936\n"
"v_max3_u32 v19, v19, v62, v63\n"
"s_waitcnt vmcnt(18) lgkmcnt(4)\n"
"v_mfma_f32_32x32x16_bf16 v[144:159], v[224:227], v[64:67], v[144:159]\n"
"v_max3_u32 v19, v19, v64, v65\n"
"s_add_u32 s94, s80, 20480\n"
"buffer_load_dwordx4 v[112:115], v0, s[44:47], s94 offen sc1\n"
"v_mfma_f32_32x32x16_bf16 v[160:175], v[228:231], v[64:67], v[160:175]\n"
"ds_read_b128 v[224:227], v0 offset:11264\n"
"ds_read_b128 v[228:231], v0 offset:40960\n"
"v_max3_u32 v19, v19, v66, v67\n"
"s_waitcnt vmcnt(18) lgkmcnt(4)\n"
"v_mfma_f32_32x32x16_bf16 v[144:159], v[208:211], v[68:71], v[144:159]\n"
"v_max3_u32 v19, v19, v68, v69\n"
"buffer_load_dwordx4 v[116:119], v0, s[44:47], s94 offen offset:1024 sc1\n"
"v_mfma_f32_32x32x16_bf16 v[160:175], v[212:215], v[68:71], v[160:175]\n"
"ds_read_b128 v[208:211], v0 offset:12288\n"
"ds_read_b128 v[212:215], v0 offset:41984\n"
"v_max3_u32 v19, v19, v70, v71\n"
"s_waitcnt vmcnt(18) lgkmcnt(4)\n"
"v_mfma_f32_32x32x16_bf16 v[144:159], v[216:219], v[72:75], v[144:159]\n"
"v_max3_u32 v19, v19, v72, v73\n"
"buffer_load_dwordx4 v[120:123], v0, s[44:47], s94 offen offset:2048 sc1\n"
"v_mfma_f32_32x32x16_bf16 v[160:175], v[220:223], v[72:75], v[160:175]\n"
"ds_read_b128 v[216:219], v0 offset:13312\n"
"ds_read_b128 v[220:223], v0 offset:43008\n"
"v_max3_u32 v19, v19, v74, v75\n"
"s_waitcnt vmcnt(18) lgkmcnt(4)\n"
"v_mfma_f32_32x32x16_bf16 v[144:159], v[224:227], v[76:79], v[144:159]\n"
"v_max3_u32 v19, v19, v76, v77\n"
"buffer_load_dwordx4 v[124:127], v0, s[44:47], s94 offen offset:3072 sc1\n"
"v_mfma_f32_32x32x16_bf16 v[160:175], v[228:231], v[76:79], v[160:175]\n"
"ds_read_b128 v[224:227], v0 offset:14336\n"
"ds_read_b128 v[228:231], v0 offset:44032\n"
"v_max3_u32 v19, v19, v78, v79\n"
"s_waitcnt vmcnt(11) lgkmcnt(4)\n"
"v_mfma_f32_32x32x16_bf16 v[144:159], v[208:211], v[80:83], v[144:159]\n"
"v_max3_u32 v19, v19, v80, v81\n"
"s_add_u32 s94, s80, 24576\n"
"buffer_load_dwordx4 v[128:131], v0, s[44:47], s94 offen sc1\n"
"v_mfma_f32_32x32x16_bf16 v[160:175], v[212:215], v[80:83], v[160:175]\n"
"ds_read_b128 v[208:211], v0 offset:15360\n"
"ds_read_b128 v[212:215], v0 offset:45056\n"
"v_max3_u32 v19, v19, v82, v83\n"
"s_waitcnt vmcnt(11) lgkmcnt(4)\n"
"v_mfma_f32_32x32x16_bf16 v[144:159], v[216:219], v[84:87], v[144:159]\n"
"v_max3_u32 v19, v19, v84, v85\n"
"buffer_load_dwordx4 v[132:135], v0, s[44:47], s94 offen offset:1024 sc1\n"
"v_max3_u32 v19, v19, v192, v193\n"
"v_max3_u32 v19, v19, v194, v195\n"
"v_mfma_f32_32x32x16_bf16 v[160:175], v[220:223], v[84:87], v[160:175]\n"
"ds_read_b128 v[216:219], v0 offset:16384\n"
"ds_read_b128 v[220:223], v0 offset:46080\n"
"v_max3_u32 v19, v19, v86, v87\n"
"v_max3_u32 v19, v19, v196, v197\n"
"v_max3_u32 v19, v19, v198, v199\n"
"v_max3_u32 v19, v19, v200, v201\n"
"s_waitcnt vmcnt(11) lgkmcnt(4)\n"
"v_mfma_f32_32x32x16_bf16 v[144:159], v[224:227], v[88:91], v[144:159]\n"
"v_max3_u32 v19, v19, v88, v89\n"
"buffer_load_dwordx4 v[136:139], v0, s[44:47], s94 offen offset:2048 sc1\n"
"v_max3_u32 v19, v19, v202, v203\n"
"v_mfma_f32_32x32x16_bf16 v[160:175], v[228:231], v[88:91], v[160:175]\n"
"ds_read_b128 v[224:227], v0 offset:17408\n"
"ds_read_b128 v[228:231], v0 offset:47104\n"
"v_max3_u32 v19, v19, v90, v91\n"
"v_max3_u32 v19, v19, v204, v205\n"
"v_add_f32 v192, v192, v194\n"
"v_add_f32 v193, v193, v195\n"
"v_add_f32 v196, v196, v198\n"
"s_waitcnt vmcnt(11) lgkmcnt(4)\n"
"v_mfma_f32_32x32x16_bf16 v[144:159], v[208:211], v[92:95], v[144:159]\n"
"v_max3_u32 v19, v19, v92, v93\n"
"buffer_load_dwordx4 v[140:143], v0, s[44:47], s94 offen offset:3072 sc1\n"
"v_add_f32 v197, v197, v199\n"
"v_mfma_f32_32x32x16_bf16 v[160:175], v[212:215], v[92:95], v[160:175]\n"
"ds_read_b128 v[208:211], v0 offset:18432\n"
"ds_read_b128 v[212:215], v0 offset:48128\n"
"v_max3_u32 v19, v19, v94, v95\n"
"v_add_f32 v200, v200, v202\n"
"v_add_f32 v201, v201, v203\n"
"v_add_f32 v192, v192, v196\n"
"s_waitcnt vmcnt(11) lgkmcnt(4)\n"
"v_mfma_f32_32x32x16_bf16 v[144:159], v[216:219], v[96:99], v[144:159]\n"
"v_max3_u32 v19, v19, v96, v97\n"
"s_add_u32 m0, s89, 89088\n"
"s_add_u32 s95, s88, 89088\n"
"buffer_load_dwordx4 v0, s[40:43], s95 offen lds\n"
"v_add_f32 v193, v193, v197\n"
"v_mfma_f32_32x32x16_bf16 v[160:175], v[220:223], v[96:99], v[160:175]\n"
"ds_read_b128 v[216:219], v0 offset:19456\n"
"ds_read_b128 v[220:223], v0 offset:49152\n"
"v_max3_u32 v19, v19, v98, v99\n"
"v_add_f32 v200, v200, v204\n"
"v_add_f32 v201, v201, v205\n"
"v_add_f32 v192, v192, v200\n"
"v_add_f32 v193, v193, v201\n"
"s_waitcnt vmcnt(11) lgkmcnt(4)\n"
"v_mfma_f32_32x32x16_bf16 v[144:159], v[224:227], v[100:103], v[144:159]\n"
"v_max3_u32 v19, v19, v100, v101\n"
"buffer_load_dwordx4 v0, s[40:43], s95 offen offset:1024 lds\n"
"v_mov_b32 v232, v192\n"
"v_mfma_f32_32x32x16_bf16 v[160:175], v[228:231], v[100:103], v[160:175]\n"
"ds_read_b128 v[224:227], v0 offset:20480\n"
"ds_read_b128 v[228:231], v0 offset:50176\n"
"v_max3_u32 v19, v19, v102, v103\n"
"v_mov_b32 v233, v193\n"
"v_permlane32_swap_b32 v192, v232\n"
"s_nop 0\n"
"v_permlane32_swap_b32 v193, v233\n"
"s_waitcnt vmcnt(11) lgkmcnt(4)\n"
"v_mfma_f32_32x32x16_bf16 v[144:159], v[208:211], v[104:107], v[144:159]\n"
"v_max3_u32 v19, v19, v104, v105\n"
"buffer_load_dwordx4 v0, s[40:43], s95 offen offset:2048 lds\n"
"v_add_f32 v192, v192, v232\n"
"v_mfma_f32_32x32x16_bf16 v[160:175], v[212:215], v[104:107], v[160:175]\n"
"ds_read_b128 v[208:211], v0 offset:21504\n"
"ds_read_b128 v[212:215], v0 offset:51200\n"
"v_max3_u32 v19, v19, v106, v107\n"
"v_add_f32 v193, v193, v233\n"
"v_mul_f32 v192, 0x3b124925, v192\n"
"v_mul_f32 v193, 0x3b124925, v193\n"
"v_fma_f32 v193, -v192, v192, v193\n"
"s_waitcnt vmcnt(11) lgkmcnt(4)\n"
"v_mfma_f32_32x32x16_bf16 v[144:159], v[216:219], v[108:111], v[144:159]\n"
"v_max3_u32 v19, v19, v108, v109\n"
"buffer_load_dwordx4 v0, s[40:43], s95 offen offset:3072 lds\n"
"v_max_f32 v193, 0, v193\n"
"v_mfma_f32_32x32x16_bf16 v[160:175], v[220:223], v[108:111], v[160:175]\n"
"ds_read_b128 v[216:219], v0 offset:22528\n"
"ds_read_b128 v[220:223], v0 offset:52224\n"
"v_max3_u32 v19, v19, v110, v111\n"
"v_cvt_pk_bf16_f32 v193, v193, v193\n"
"v_and_b32 v193, 0xffff0000, v193\n"
"v_add_f32 v193, 0x3727c5ac, v193\n"
"s_waitcnt vmcnt(11) lgkmcnt(4)\n"
"v_mfma_f32_32x32x16_bf16 v[144:159], v[224:227], v[112:115], v[144:159]\n"
"v_max3_u32 v19, v19, v112, v113\n"
"s_add_u32 m0, s89, 93184\n"
"s_add_u32 s95, s88, 93184\n"
"buffer_load_dwordx4 v0, s[40:43], s95 offen lds\n"
"v_cvt_pk_bf16_f32 v193, v193, v193\n"
"v_mfma_f32_32x32x16_bf16 v[160:175], v[228:231], v[112:115], v[160:175]\n"
"ds_read_b128 v[224:227], v0 offset:23552\n"
"ds_read_b128 v[228:231], v0 offset:53248\n"
"v_max3_u32 v19, v19, v114, v115\n"
"v_and_b32 v193, 0xffff0000, v193\n"
"v_sqrt_f32 v193, v193\n"
"s_nop 0\n"
"v_cvt_pk_bf16_f32 v193, v193, v193\n"
"s_waitcnt vmcnt(11) lgkmcnt(4)\n"
"v_mfma_f32_32x32x16_bf16 v[144:159], v[208:211], v[116:119], v[144:159]\n"
"v_max3_u32 v19, v19, v116, v117\n"
"buffer_load_dwordx4 v0, s[40:43], s95 offen offset:1024 lds\n"
"v_and_b32 v193, 0xffff0000, v193\n"
"v_mfma_f32_32x32x16_bf16 v[160:175], v[212:215], v[116:119], v[160:175]\n"
"ds_read_b128 v[208:211], v0 offset:24576\n"
"ds_read_b128 v[212:215], v0 offset:54272\n"
"v_max3_u32 v19, v19, v118, v119\n"
"v_rcp_f32 v16, v193\n"
"v_cvt_pk_bf16_f32 v20, -v192, -v192\n"
"s_waitcnt vmcnt(11) lgkmcnt(4)\n"
"v_mfma_f32_32x32x16_bf16 v[144:159], v[216:219], v[120:123], v[144:159]\n"
"v_max3_u32 v19, v19, v120, v121\n"
"buffer_load_dwordx4 v0, s[40:43], s95 offen offset:2048 lds\n"
"v_lshrrev_b32 v233, 16, v193\n"
"v_mfma_f32_32x32x16_bf16 v[160:175], v[220:223], v[120:123], v[160:175]\n"
"ds_read_b128 v[216:219], v0 offset:25600\n"
"ds_read_b128 v[220:223], v0 offset:55296\n"
"v_max3_u32 v19, v19, v122, v123\n"
"v_mul_f32 v18, v193, v193\n"
"v_mov_b32 v21, v20\n"
"v_mov_b32 v22, v20\n"
"v_cndmask_b32_e64 v23, v233, v20, s[62:63]\n"
"s_waitcnt vmcnt(11) lgkmcnt(4)\n"
"v_mfma_f32_32x32x16_bf16 v[144:159], v[224:227], v[124:127], v[144:159]\n"
"v_max3_u32 v19, v19, v124, v125\n"
"buffer_load_dwordx4 v0, s[40:43], s95 offen offset:3072 lds\n"
"v_mul_f32 v17, 0xbfb8aa3b, v16\n"
"v_mfma_f32_32x32x16_bf16 v[160:175], v[228:231], v[124:127], v[160:175]\n"
"ds_read_b128 v[224:227], v0 offset:26624\n"
"ds_read_b128 v[228:231], v0 offset:56320\n"
"v_max3_u32 v19, v19, v126, v127\n"
"s_waitcnt vmcnt(11) lgkmcnt(4)\n"
"v_mfma_f32_32x32x16_bf16 v[144:159], v[208:211], v[128:131], v[144:159]\n"
"v_max3_u32 v19, v19, v128, v129\n"
"v_mfma_f32_32x32x16_bf16 v[160:175], v[212:215], v[128:131], v[160:175]\n"
"ds_read_b128 v[208:211], v0 offset:27648\n"
"ds_read_b128 v[212:215], v0 offset:57344\n"
"v_max3_u32 v19, v19, v130, v131\n"
"s_waitcnt vmcnt(10) lgkmcnt(4)\n"
"v_mfma_f32_32x32x16_bf16 v[144:159], v[216:219], v[132:135], v[144:159]\n"
"v_max3_u32 v19, v19, v132, v133\n"
"v_mfma_f32_32x32x16_bf16 v[160:175], v[220:223], v[132:135], v[160:175]\n"
"ds_read_b128 v[216:219], v0 offset:28672\n"
"ds_read_b128 v[220:223], v0 offset:58368\n"
"v_max3_u32 v19, v19, v134, v135\n"
"s_waitcnt vmcnt(9) lgkmcnt(4)\n"
"v_mfma_f32_32x32x16_bf16 v[144:159], v[224:227], v[136:139], v[144:159]\n"
"v_max3_u32 v19, v19, v136, v137\n"
"v_mfma_f32_32x32x16_bf16 v[160:175], v[228:231], v[136:139], v[160:175]\n"
"ds_read_b128 v[224:227], v0 offset:59392\n"
"ds_read_b128 v[228:231], v0 offset:60416\n"
"v_max3_u32 v19, v19, v138, v139\n"
"s_waitcnt vmcnt(8) lgkmcnt(4)\n"
"v_mfma_f32_32x32x16_bf16 v[144:159], v[208:211], v[140:143], v[144:159]\n"
"v_max3_u32 v19, v19, v140, v141\n"
"v_mfma_f32_32x32x16_bf16 v[160:175], v[212:215], v[140:143], v[160:175]\n"
"ds_read_b128 v[208:211], v0 offset:61440\n"
"ds_read_b128 v[212:215], v0 offset:62464\n"
"v_max3_u32 v19, v19, v142, v143\n"
"s_waitcnt lgkmcnt(4)\n"
"v_mfma_f32_32x32x16_bf16 v[144:159], v[216:219], v[20:23], v[144:159]\n"
"v_mfma_f32_32x32x16_bf16 v[160:175], v[220:223], v[20:23], v[160:175]\n"
"ds_read_b128 v[216:219], v0 offset:63488\n"
"ds_read_b128 v[220:223], v0 offset:64512\n"
"v_cmp_eq_u32 vcc, -1, v19\n"
"s_cbranch_vccnz L_x1v_s_1\n"
"L_x1v_go_1:\n"
"s_barrier\n"
"s_waitcnt lgkmcnt(4)\n"
"v_mfma_f32_32x32x16_bf16 v[176:191], v[224:227], v[32:35], 0\n"
"s_add_u32 m0, s89, 118784\n"
"s_add_u32 s95, s88, 118784\n"
"buffer_load_dwordx4 v0, s[40:43], s95 offen lds\n"
"v_mfma_f32_32x32x16_bf16 v[176:191], v[228:231], v[36:39], v[176:191]\n"
"ds_read_b128 v[224:227], v1\n"
"ds_read_b128 v[228:231], v1 offset:1024\n"
"v_mul_f32 v232, v17, v144\n"
"v_mul_f32 v233, v17, v145\n"
"v_mul_f32 v234, v17, v146\n"
"v_mul_f32 v235, v17, v147\n"
"s_waitcnt lgkmcnt(4)\n"
"v_mfma_f32_32x32x16_bf16 v[176:191], v[208:211], v[40:43], v[176:191]\n"
"buffer_load_dwordx4 v0, s[40:43], s95 offen offset:1024 lds\n"
"v_exp_f32 v232, v232\n"
"v_mfma_f32_32x32x16_bf16 v[176:191], v[212:215], v[44:47], v[176:191]\n"
"ds_read_b128 v[208:211], v1 offset:2048\n"
"ds_read_b128 v[212:215], v1 offset:3072\n"
"v_exp_f32 v233, v233\n"
"v_exp_f32 v234, v234\n"
"s_waitcnt lgkmcnt(4)\n"
"v_mfma_f32_32x32x16_bf16 v[176:191], v[216:219], v[48:51], v[176:191]\n"
"buffer_load_dwordx4 v0, s[40:43], s95 offen offset:2048 lds\n"
"v_exp_f32 v235, v235\n"
"v_mul_f32 v144, v144, v152\n"
"v_mfma_f32_32x32x16_bf16 v[176:191], v[220:223], v[52:55], v[176:191]\n"
"ds_read_b128 v[216:219], v1 offset:4096\n"
"ds_read_b128 v[220:223], v1 offset:5120\n"
"v_mul_f32 v145, v145, v153\n"
"v_mul_f32 v146, v146, v154\n"
"v_mul_f32 v147, v147, v155\n"
"v_fma_f32 v232, v232, v18, v18\n"
"s_waitcnt lgkmcnt(4)\n"
"v_mfma_f32_32x32x16_bf16 v[176:191], v[224:227], v[56:59], v[176:191]\n"
"buffer_load_dwordx4 v0, s[40:43], s95 offen offset:3072 lds\n"
"v_fma_f32 v233, v233, v18, v18\n"
"v_fma_f32 v234, v234, v18, v18\n"
"v_mfma_f32_32x32x16_bf16 v[176:191], v[228:231], v[60:63], v[176:191]\n"
"ds_read_b128 v[224:227], v1 offset:6144\n"
"ds_read_b128 v[228:231], v1 offset:7168\n"
"v_fma_f32 v235, v235, v18, v18\n"
"v_rcp_f32 v232, v232\n"
"v_rcp_f32 v233, v233\n"
"s_waitcnt lgkmcnt(4)\n"
"v_mfma_f32_32x32x16_bf16 v[176:191], v[208:211], v[64:67], v[176:191]\n"
"s_add_u32 m0, s89, 122880\n"
"s_add_u32 s95, s88, 122880\n"
"buffer_load_dwordx4 v0, s[40:43], s95 offen lds\n"
"v_rcp_f32 v234, v234\n"
"v_mfma_f32_32x32x16_bf16 v[176:191], v[212:215], v[68:71], v[176:191]\n"
"ds_read_b128 v[208:211], v1 offset:8192\n"
"ds_read_b128 v[212:215], v1 offset:9216\n"
"v_rcp_f32 v235, v235\n"
"v_mul_f32 v144, v144, v232\n"
"v_mul_f32 v145, v145, v233\n"
"s_waitcnt lgkmcnt(4)\n"
"v_mfma_f32_32x32x16_bf16 v[176:191], v[216:219], v[72:75], v[176:191]\n"
"buffer_load_dwordx4 v0, s[40:43], s95 offen offset:1024 lds\n"
"v_mul_f32 v146, v146, v234\n"
"v_mul_f32 v147, v147, v235\n"
"v_mfma_f32_32x32x16_bf16 v[176:191], v[220:223], v[76:79], v[176:191]\n"
"ds_read_b128 v[216:219], v1 offset:10240\n"
"ds_read_b128 v[220:223], v1 offset:11264\n"
"v_cvt_pk_bf16_f32 v240, v144, v145\n"
"v_cvt_pk_bf16_f32 v241, v146, v147\n"
"v_min_u32 v240, -2, v240\n"
"v_min_u32 v241, -2, v241\n"
"v_mul_f32 v236, v17, v148\n"
"s_waitcnt lgkmcnt(4)\n"
"v_mfma_f32_32x32x16_bf16 v[176:191], v[224:227], v[80:83], v[176:191]\n"
"buffer_load_dwordx4 v0, s[40:43], s95 offen offset:2048 lds\n"
"v_mul_f32 v237, v17, v149\n"
"v_mul_f32 v238, v17, v150\n"
"v_mfma_f32_32x32x16_bf16 v[176:191], v[228:231], v[84:87], v[176:191]\n"
"ds_read_b128 v[224:227], v1 offset:12288\n"
"ds_read_b128 v[228:231], v1 offset:13312\n"
"v_mul_f32 v239, v17, v151\n"
"v_exp_f32 v236, v236\n"
"s_waitcnt lgkmcnt(4)\n"
"v_mfma_f32_32x32x16_bf16 v[176:191], v[208:211], v[88:91], v[176:191]\n"
"buffer_load_dwordx4 v0, s[40:43], s95 offen offset:3072 lds\n"
"v_exp_f32 v237, v237\n"
"v_mfma_f32_32x32x16_bf16 v[176:191], v[212:215], v[92:95], v[176:191]\n"
"ds_read_b128 v[208:211], v1 offset:14336\n"
"ds_read_b128 v[212:215], v1 offset:15360\n"
"v_exp_f32 v238, v238\n"
"v_exp_f32 v239, v239\n"
"v_mul_f32 v148, v148, v156\n"
"s_waitcnt lgkmcnt(4)\n"
"v_mfma_f32_32x32x16_bf16 v[176:191], v[216:219], v[96:99], v[176:191]\n"
"s_add_u32 m0, s89, 0\n"
"s_add_u32 s95, s88, 148480\n"
"buffer_load_dwordx4 v0, s[40:43], s95 offen lds\n"
"v_mul_f32 v149, v149, v157\n"
"v_mul_f32 v150, v150, v158\n"
"v_mfma_f32_32x32x16_bf16 v[176:191], v[220:223], v[100:103], v[176:191]\n"
"ds_read_b128 v[216:219], v1 offset:16384\n"
"ds_read_b128 v[220:223], v1 offset:17408\n"
"v_mul_f32 v151, v151, v159\n"
"v_fma_f32 v236, v236, v18, v18\n"
"v_fma_f32 v237, v237, v18, v18\n"
"v_fma_f32 v238, v238, v18, v18\n"
"v_fma_f32 v239, v239, v18, v18\n"
"s_waitcnt lgkmcnt(4)\n"
"v_mfma_f32_32x32x16_bf16 v[176:191], v[224:227], v[104:107], v[176:191]\n"
"buffer_load_dwordx4 v0, s[40:43], s95 offen offset:1024 lds\n"
"v_rcp_f32 v236, v236\n"
"v_mfma_f32_32x32x16_bf16 v[176:191], v[228:231], v[108:111], v[176:191]\n"
"ds_read_b128 v[224:227], v1 offset:18432\n"
"ds_read_b128 v[228:231], v1 offset:19456\n"
"buffer_load_dwordx4 v0, s[40:43], s95 offen offset:2048 lds\n"
"v_rcp_f32 v237, v237\n"
"s_waitcnt lgkmcnt(4)\n"
"v_mfma_f32_32x32x16_bf16 v[176:191], v[208:211], v[112:115], v[176:191]\n"
"s_waitcnt vmcnt(11)\n"
"s_barrier\n"
"v_rcp_f32 v238, v238\n"
"v_rcp_f32 v239, v239\n"
"v_mfma_f32_32x32x16_bf16 v[176:191], v[212:215], v[116:119], v[176:191]\n"
"ds_read_b128 v[208:211], v1 offset:20480\n"
"ds_read_b128 v[212:215], v1 offset:21504\n"
"buffer_load_dwordx4 v0, s[40:43], s95 offen offset:3072 lds\n"
"v_mul_f32 v148, v148, v236\n"
"v_mul_f32 v149, v149, v237\n"
"s_waitcnt lgkmcnt(4)\n"
"v_mfma_f32_32x32x16_bf16 v[176:191], v[216:219], v[120:123], v[176:191]\n"
"v_mul_f32 v150, v150, v238\n"
"v_mul_f32 v151, v151, v239\n"
"v_cvt_pk_bf16_f32 v242, v148, v149\n"
"v_cvt_pk_bf16_f32 v243, v150, v151\n"
"v_min_u32 v242, -2, v242\n"
"v_mfma_f32_32x32x16_bf16 v[176:191], v[220:223], v[124:127], v[176:191]\n"
"ds_read_b128 v[216:219], v1 offset:22528\n"
"ds_read_b128 v[220:223], v1 offset:23552\n"
"s_add_u32 m0, s89, 4096\n"
"s_add_u32 s95, s88, 152576\n"
"buffer_load_dwordx4 v0, s[40:43], s95 offen lds\n"
"v_min_u32 v243, -2, v243\n"
"s_waitcnt lgkmcnt(4)\n"
"v_mfma_f32_32x32x16_bf16 v[176:191], v[224:227], v[128:131], v[176:191]\n"
"buffer_store_dwordx4 v[240:243], v0, s[44:47], s82 offen sc1\n"
"v_mul_f32 v232, v17, v160\n"
"v_mul_f32 v233, v17, v161\n"
"v_mul_f32 v234, v17, v162\n"
"v_mfma_f32_32x32x16_bf16 v[176:191], v[228:231], v[132:135], v[176:191]\n"
"ds_read_b128 v[224:227], v1 offset:24576\n"
"ds_read_b128 v[228:231], v1 offset:25600\n"
"buffer_load_dwordx4 v0, s[40:43], s95 offen offset:1024 lds\n"
"v_mul_f32 v235, v17, v163\n"
"s_waitcnt lgkmcnt(4)\n"
"v_mfma_f32_32x32x16_bf16 v[176:191], v[208:211], v[136:139], v[176:191]\n"
"v_exp_f32 v232, v232\n"
"v_exp_f32 v233, v233\n"
"v_mfma_f32_32x32x16_bf16 v[176:191], v[212:215], v[140:143], v[176:191]\n"
"ds_read_b128 v[208:211], v1 offset:26624\n"
"ds_read_b128 v[212:215], v1 offset:27648\n"
"buffer_load_dwordx4 v0, s[40:43], s95 offen offset:2048 lds\n"
"v_exp_f32 v234, v234\n"
"s_waitcnt lgkmcnt(4)\n"
"v_mfma_f32_32x32x16_bf16 v[176:191], v[216:219], v[20:23], v[176:191]\n"
"v_exp_f32 v235, v235\n"
"v_mul_f32 v160, v160, v168\n"
"v_mul_f32 v161, v161, v169\n"
"v_mul_f32 v162, v162, v170\n"
"v_mfma_f32_32x32x16_bf16 v[144:159], v[220:223], v[32:35], 0\n"
"ds_read_b128 v[216:219], v1 offset:28672\n"
"ds_read_b128 v[220:223], v1 offset:29696\n"
"buffer_load_dwordx4 v0, s[40:43], s95 offen offset:3072 lds\n"
"v_mul_f32 v163, v163, v171\n"
"v_fma_f32 v232, v232, v18, v18\n"
"s_waitcnt lgkmcnt(4)\n"
"v_mfma_f32_32x32x16_bf16 v[144:159], v[224:227], v[36:39], v[144:159]\n"
"v_fma_f32 v233, v233, v18, v18\n"
"v_fma_f32 v234, v234, v18, v18\n"
"v_fma_f32 v235, v235, v18, v18\n"
"v_rcp_f32 v232, v232\n"
"v_mfma_f32_32x32x16_bf16 v[144:159], v[228:231], v[40:43], v[144:159]\n"
"ds_read_b128 v[224:227], v1 offset:30720\n"
"ds_read_b128 v[228:231], v1 offset:31744\n"
"s_add_u32 m0, s89, 29696\n"
"s_add_u32 s95, s88, 178176\n"
"buffer_load_dwordx4 v0, s[40:43], s95 offen lds\n"
"v_rcp_f32 v233, v233\n"
"s_waitcnt lgkmcnt(4)\n"
"v_mfma_f32_32x32x16_bf16 v[144:159], v[208:211], v[44:47], v[144:159]\n"
"v_rcp_f32 v234, v234\n"
"v_rcp_f32 v235, v235\n"
"v_mfma_f32_32x32x16_bf16 v[144:159], v[212:215], v[48:51], v[144:159]\n"
"ds_read_b128 v[208:211], v1 offset:32768\n"
"ds_read_b128 v[212:215], v1 offset:33792\n"
"buffer_load_dwordx4 v0, s[40:43], s95 offen offset:1024 lds\n"
"v_mul_f32 v160, v160, v232\n"
"v_mul_f32 v161, v161, v233\n"
"s_waitcnt lgkmcnt(4)\n"
"v_mfma_f32_32x32x16_bf16 v[144:159], v[216:219], v[52:55], v[144:159]\n"
"v_mul_f32 v162, v162, v234\n"
"v_mul_f32 v163, v163, v235\n"
"v_cvt_pk_bf16_f32 v244, v160, v161\n"
"v_cvt_pk_bf16_f32 v245, v162, v163\n"
"v_min_u32 v244, -2, v244\n"
"v_mfma_f32_32x32x16_bf16 v[144:159], v[220:223], v[56:59], v[144:159]\n"
"ds_read_b128 v[216:219], v1 offset:34816\n"
"ds_read_b128 v[220:223], v1 offset:35840\n"
"buffer_load_dwordx4 v0, s[40:43], s95 offen offset:2048 lds\n"
"v_min_u32 v245, -2, v245\n"
"v_mul_f32 v236, v17, v164\n"
"s_waitcnt lgkmcnt(4)\n"
"v_mfma_f32_32x32x16_bf16 v[144:159], v[224:227], v[60:63], v[144:159]\n"
"v_mul_f32 v237, v17, v165\n"
"v_mul_f32 v238, v17, v166\n"
"v_mul_f32 v239, v17, v167\n"
"v_mfma_f32_32x32x16_bf16 v[144:159], v[228:231], v[64:67], v[144:159]\n"
"ds_read_b128 v[224:227], v1 offset:36864\n"
"ds_read_b128 v[228:231], v1 offset:37888\n"
"buffer_load_dwordx4 v0, s[40:43], s95 offen offset:3072 lds\n"
"v_exp_f32 v236, v236\n"
"s_waitcnt lgkmcnt(4)\n"
"v_mfma_f32_32x32x16_bf16 v[144:159], v[208:211], v[68:71], v[144:159]\n"
"s_add_u32 m0, s89, 33792\n"
"s_add_u32 s95, s88, 182272\n"
"buffer_load_dwordx4 v0, s[40:43], s95 offen lds\n"
"v_exp_f32 v237, v237\n"
"v_mfma_f32_32x32x16_bf16 v[144:159], v[212:215], v[72:75], v[144:159]\n"
"ds_read_b128 v[208:211], v1 offset:38912\n"
"ds_read_b128 v[212:215], v1 offset:39936\n"
"v_exp_f32 v238, v238\n"
"v_exp_f32 v239, v239\n"
"v_mul_f32 v164, v164, v172\n"
"s_waitcnt lgkmcnt(4)\n"
"v_mfma_f32_32x32x16_bf16 v[144:159], v[216:219], v[76:79], v[144:159]\n"
"buffer_load_dwordx4 v0, s[40:43], s95 offen offset:1024 lds\n"
"v_mul_f32 v165, v165, v173\n"
"v_mul_f32 v166, v166, v174\n"
"v_mfma_f32_32x32x16_bf16 v[144:159], v[220:223], v[80:83], v[144:159]\n"
"ds_read_b128 v[216:219], v1 offset:40960\n"
"ds_read_b128 v[220:223], v1 offset:41984\n"
"v_mul_f32 v167, v167, v175\n"
"v_fma_f32 v236, v236, v18, v18\n"
"v_fma_f32 v237, v237, v18, v18\n"
"v_fma_f32 v238, v238, v18, v18\n"
"v_fma_f32 v239, v239, v18, v18\n"
"s_waitcnt lgkmcnt(4)\n"
"v_mfma_f32_32x32x16_bf16 v[144:159], v[224:227], v[84:87], v[144:159]\n"
"buffer_load_dwordx4 v0, s[40:43], s95 offen offset:2048 lds\n"
"v_rcp_f32 v236, v236\n"
"v_mfma_f32_32x32x16_bf16 v[144:159], v[228:231], v[88:91], v[144:159]\n"
"ds_read_b128 v[224:227], v1 offset:43008\n"
"ds_read_b128 v[228:231], v1 offset:44032\n"
"v_rcp_f32 v237, v237\n"
"v_rcp_f32 v238, v238\n"
"s_waitcnt lgkmcnt(4)\n"
"v_mfma_f32_32x32x16_bf16 v[144:159], v[208:211], v[92:95], v[144:159]\n"
"buffer_load_dwordx4 v0, s[40:43], s95 offen offset:3072 lds\n"
"v_rcp_f32 v239, v239\n"
"v_mfma_f32_32x32x16_bf16 v[144:159], v[212:215], v[96:99], v[144:159]\n"
"ds_read_b128 v[208:211], v1 offset:45056\n"
"ds_read_b128 v[212:215], v1 offset:46080\n"
"v_mul_f32 v164, v164, v236\n"
"v_mul_f32 v165, v165, v237\n"
"v_mul_f32 v166, v166, v238\n"
"v_mul_f32 v167, v167, v239\n"
"v_cvt_pk_bf16_f32 v246, v164, v165\n"
"s_waitcnt lgkmcnt(4)\n"
"v_mfma_f32_32x32x16_bf16 v[144:159], v[216:219], v[100:103], v[144:159]\n"
"v_cvt_pk_bf16_f32 v247, v166, v167\n"
"v_min_u32 v246, -2, v246\n"
"v_min_u32 v247, -2, v247\n"
"v_mfma_f32_32x32x16_bf16 v[144:159], v[220:223], v[104:107], v[144:159]\n"
"ds_read_b128 v[216:219], v1 offset:47104\n"
"ds_read_b128 v[220:223], v1 offset:48128\n"
"buffer_store_dwordx4 v[244:247], v0, s[44:47], s82 offen offset:1024 sc1\n"
"v_mul_f32 v232, v17, v176\n"
"v_mul_f32 v233, v17, v177\n"
"v_mul_f32 v234, v17, v178\n"
"s_waitcnt lgkmcnt(4)\n"
"v_mfma_f32_32x32x16_bf16 v[144:159], v[224:227], v[108:111], v[144:159]\n"
"v_mul_f32 v235, v17, v179\n"
"v_exp_f32 v232, v232\n"
"v_mfma_f32_32x32x16_bf16 v[144:159], v[228:231], v[112:115], v[144:159]\n"
"ds_read_b128 v[224:227], v1 offset:49152\n"
"ds_read_b128 v[228:231], v1 offset:50176\n"
"v_exp_f32 v233, v233\n"
"v_exp_f32 v234, v234\n"
"s_waitcnt lgkmcnt(4)\n"
"v_mfma_f32_32x32x16_bf16 v[144:159], v[208:211], v[116:119], v[144:159]\n"
"s_waitcnt vmcnt(18)\n"
"s_barrier\n"
"v_exp_f32 v235, v235\n"
"v_mul_f32 v176, v176, v184\n"
"v_mul_f32 v177, v177, v185\n"
"v_mul_f32 v178, v178, v186\n"
"v_mfma_f32_32x32x16_bf16 v[144:159], v[212:215], v[120:123], v[144:159]\n"
"ds_read_b128 v[208:211], v1 offset:51200\n"
"ds_read_b128 v[212:215], v1 offset:52224\n"
"v_mul_f32 v179, v179, v187\n"
"v_fma_f32 v232, v232, v18, v18\n"
"v_fma_f32 v233, v233, v18, v18\n"
"v_fma_f32 v234, v234, v18, v18\n"
"v_fma_f32 v235, v235, v18, v18\n"
"s_waitcnt lgkmcnt(4)\n"
"v_mfma_f32_32x32x16_bf16 v[144:159], v[216:219], v[124:127], v[144:159]\n"
"v_rcp_f32 v232, v232\n"
"v_rcp_f32 v233, v233\n"
"v_mfma_f32_32x32x16_bf16 v[144:159], v[220:223], v[128:131], v[144:159]\n"
"ds_read_b128 v[216:219], v1 offset:53248\n"
"ds_read_b128 v[220:223], v1 offset:54272\n"
"v_rcp_f32 v234, v234\n"
"v_rcp_f32 v235, v235\n"
"v_mul_f32 v176, v176, v232\n"
"s_waitcnt lgkmcnt(4)\n"
"v_mfma_f32_32x32x16_bf16 v[144:159], v[224:227], v[132:135], v[144:159]\n"
"v_mul_f32 v177, v177, v233\n"
"v_mul_f32 v178, v178, v234\n"
"v_mul_f32 v179, v179, v235\n"
"v_cvt_pk_bf16_f32 v240, v176, v177\n"
"v_mfma_f32_32x32x16_bf16 v[144:159], v[228:231], v[136:139], v[144:159]\n"
"ds_read_b128 v[224:227], v1 offset:55296\n"
"ds_read_b128 v[228:231], v1 offset:56320\n"
"v_cvt_pk_bf16_f32 v241, v178, v179\n"
"v_min_u32 v240, -2, v240\n"
"v_min_u32 v241, -2, v241\n"
"v_mul_f32 v236, v17, v180\n"
"v_mul_f32 v237, v17, v181\n"
"s_waitcnt lgkmcnt(4)\n"
"v_mfma_f32_32x32x16_bf16 v[144:159], v[208:211], v[140:143], v[144:159]\n"
"v_mul_f32 v238, v17, v182\n"
"v_mul_f32 v239, v17, v183\n"
"v_exp_f32 v236, v236\n"
"v_mfma_f32_32x32x16_bf16 v[144:159], v[212:215], v[20:23], v[144:159]\n"
"ds_read_b128 v[208:211], v1 offset:57344\n"
"ds_read_b128 v[212:215], v1 offset:58368\n"
"v_exp_f32 v237, v237\n"
"v_exp_f32 v238, v238\n"
"s_waitcnt lgkmcnt(4)\n"
"v_mfma_f32_32x32x16_bf16 v[160:175], v[216:219], v[32:35], 0\n"
"v_exp_f32 v239, v239\n"
"v_mul_f32 v180, v180, v188\n"
"v_mul_f32 v181, v181, v189\n"
"v_mul_f32 v182, v182, v190\n"
"v_mfma_f32_32x32x16_bf16 v[160:175], v[220:223], v[36:39], v[160:175]\n"
"ds_read_b128 v[216:219], v1 offset:59392\n"
"ds_read_b128 v[220:223], v1 offset:60416\n"
"v_mul_f32 v183, v183, v191\n"
"v_fma_f32 v236, v236, v18, v18\n"
"v_fma_f32 v237, v237, v18, v18\n"
"v_fma_f32 v238, v238, v18, v18\n"
"v_fma_f32 v239, v239, v18, v18\n"
"s_waitcnt lgkmcnt(4)\n"
"v_mfma_f32_32x32x16_bf16 v[160:175], v[224:227], v[40:43], v[160:175]\n"
"v_rcp_f32 v236, v236\n"
"v_rcp_f32 v237, v237\n"
"v_mfma_f32_32x32x16_bf16 v[160:175], v[228:231], v[44:47], v[160:175]\n"
"ds_read_b128 v[224:227], v1 offset:61440\n"
"ds_read_b128 v[228:231], v1 offset:62464\n"
"v_rcp_f32 v238, v238\n"
"v_rcp_f32 v239, v239\n"
"v_mul_f32 v180, v180, v236\n"
"s_waitcnt lgkmcnt(4)\n"
"v_mfma_f32_32x32x16_bf16 v[160:175], v[208:211], v[48:51], v[160:175]\n"
"v_mul_f32 v181, v181, v237\n"
"v_mul_f32 v182, v182, v238\n"
"v_mul_f32 v183, v183, v239\n"
"v_cvt_pk_bf16_f32 v242, v180, v181\n"
"v_mfma_f32_32x32x16_bf16 v[160:175], v[212:215], v[52:55], v[160:175]\n"
"ds_read_b128 v[208:211], v1 offset:63488\n"
"ds_read_b128 v[212:215], v1 offset:64512\n"
"v_cvt_pk_bf16_f32 v243, v182, v183\n"
"v_min_u32 v242, -2, v242\n"
"v_min_u32 v243, -2, v243\n"
"s_waitcnt lgkmcnt(4)\n"
"v_mfma_f32_32x32x16_bf16 v[160:175], v[216:219], v[56:59], v[160:175]\n"
"buffer_store_dwordx4 v[240:243], v0, s[44:47], s82 offen offset:2048 sc1\n"
"v_mul_f32 v232, v17, v144\n"
"v_mul_f32 v233, v17, v145\n"
"v_mul_f32 v234, v17, v146\n"
"v_mfma_f32_32x32x16_bf16 v[160:175], v[220:223], v[60:63], v[160:175]\n"
"ds_read_b128 v[216:219], v2\n"
"ds_read_b128 v[220:223], v2 offset:1024\n"
"v_mul_f32 v235, v17, v147\n"
"v_exp_f32 v232, v232\n"
"s_waitcnt lgkmcnt(4)\n"
"v_mfma_f32_32x32x16_bf16 v[160:175], v[224:227], v[64:67], v[160:175]\n"
"v_exp_f32 v233, v233\n"
"v_exp_f32 v234, v234\n"
"v_mfma_f32_32x32x16_bf16 v[160:175], v[228:231], v[68:71], v[160:175]\n"
"ds_read_b128 v[224:227], v2 offset:2048\n"
"ds_read_b128 v[228:231], v2 offset:3072\n"
"v_exp_f32 v235, v235\n"
"v_mul_f32 v144, v144, v152\n"
"v_mul_f32 v145, v145, v153\n"
"v_mul_f32 v146, v146, v154\n"
"s_waitcnt lgkmcnt(4)\n"
"v_mfma_f32_32x32x16_bf16 v[160:175], v[208:211], v[72:75], v[160:175]\n"
"v_mul_f32 v147, v147, v155\n"
"v_fma_f32 v232, v232, v18, v18\n"
"v_fma_f32 v233, v233, v18, v18\n"
"v_fma_f32 v234, v234, v18, v18\n"
"v_fma_f32 v235, v235, v18, v18\n"
"v_mfma_f32_32x32x16_bf16 v[160:175], v[212:215], v[76:79], v[160:175]\n"
"ds_read_b128 v[208:211], v2 offset:4096\n"
"ds_read_b128 v[212:215], v2 offset:5120\n"
"v_rcp_f32 v232, v232\n"
"v_rcp_f32 v233, v233\n"
"s_waitcnt lgkmcnt(4)\n"
"v_mfma_f32_32x32x16_bf16 v[160:175], v[216:219], v[80:83], v[160:175]\n"
"v_rcp_f32 v234, v234\n"
"v_rcp_f32 v235, v235\n"
"v_mul_f32 v144, v144, v232\n"
"v_mfma_f32_32x32x16_bf16 v[160:175], v[220:223], v[84:87], v[160:175]\n"
"ds_read_b128 v[216:219], v2 offset:6144\n"
"ds_read_b128 v[220:223], v2 offset:7168\n"
"v_mul_f32 v145, v145, v233\n"
"v_mul_f32 v146, v146, v234\n"
"v_mul_f32 v147, v147, v235\n"
"v_cvt_pk_bf16_f32 v244, v144, v145\n"
"s_waitcnt lgkmcnt(4)\n"
"v_mfma_f32_32x32x16_bf16 v[160:175], v[224:227], v[88:91], v[160:175]\n"
"v_cvt_pk_bf16_f32 v245, v146, v147\n"
"v_min_u32 v244, -2, v244\n"
"v_min_u32 v245, -2, v245\n"
"v_mul_f32 v236, v17, v148\n"
"v_mul_f32 v237, v17, v149\n"
"v_mfma_f32_32x32x16_bf16 v[160:175], v[228:231], v[92:95], v[160:175]\n"
"ds_read_b128 v[224:227], v2 offset:8192\n"
"ds_read_b128 v[228:231], v2 offset:9216\n"
"v_mul_f32 v238, v17, v150\n"
"v_mul_f32 v239, v17, v151\n"
"v_exp_f32 v236, v236\n"
"s_waitcnt lgkmcnt(4)\n"
"v_mfma_f32_32x32x16_bf16 v[160:175], v[208:211], v[96:99], v[160:175]\n"
"v_exp_f32 v237, v237\n"
"v_exp_f32 v238, v238\n"
"v_mfma_f32_32x32x16_bf16 v[160:175], v[212:215], v[100:103], v[160:175]\n"
"ds_read_b128 v[208:211], v2 offset:10240\n"
"ds_read_b128 v[212:215], v2 offset:11264\n"
"v_exp_f32 v239, v239\n"
"v_mul_f32 v148, v148, v156\n"
"v_mul_f32 v149, v149, v157\n"
"v_mul_f32 v150, v150, v158\n"
"s_waitcnt lgkmcnt(4)\n"
"v_mfma_f32_32x32x16_bf16 v[160:175], v[216:219], v[104:107], v[160:175]\n"
"v_mul_f32 v151, v151, v159\n"
"v_fma_f32 v236, v236, v18, v18\n"
"v_fma_f32 v237, v237, v18, v18\n"
"v_fma_f32 v238, v238, v18, v18\n"
"v_fma_f32 v239, v239, v18, v18\n"
"v_mfma_f32_32x32x16_bf16 v[160:175], v[220:223], v[108:111], v[160:175]\n"
"ds_read_b128 v[216:219], v2 offset:12288\n"
"ds_read_b128 v[220:223], v2 offset:13312\n"
"v_rcp_f32 v236, v236\n"
"v_rcp_f32 v237, v237\n"
"s_waitcnt lgkmcnt(4)\n"
"v_mfma_f32_32x32x16_bf16 v[160:175], v[224:227], v[112:115], v[160:175]\n"
"s_waitcnt vmcnt(10)\n"
"s_waitcnt vmcnt(2)\n"
"s_barrier\n"
"v_rcp_f32 v238, v238\n"
"v_rcp_f32 v239, v239\n"
"v_mul_f32 v148, v148, v236\n"
"v_mfma_f32_32x32x16_bf16 v[160:175], v[228:231], v[116:119], v[160:175]\n"
"ds_read_b128 v[224:227], v2 offset:14336\n"
"ds_read_b128 v[228:231], v2 offset:15360\n"
"v_mul_f32 v149, v149, v237\n"
"v_mul_f32 v150, v150, v238\n"
"v_mul_f32 v151, v151, v239\n"
"v_cvt_pk_bf16_f32 v246, v148, v149\n"
"s_waitcnt lgkmcnt(4)\n"
"v_mfma_f32_32x32x16_bf16 v[160:175], v[208:211], v[120:123], v[160:175]\n"
"v_cvt_pk_bf16_f32 v247, v150, v151\n"
"v_min_u32 v246, -2, v246\n"
"v_min_u32 v247, -2, v247\n"
"v_mfma_f32_32x32x16_bf16 v[160:175], v[212:215], v[124:127], v[160:175]\n"
"ds_read_b128 v[208:211], v2 offset:16384\n"
"ds_read_b128 v[212:215], v0\n"
"buffer_store_dwordx4 v[244:247], v0, s[44:47], s82 offen offset:3072 sc1\n"
"s_waitcnt lgkmcnt(4)\n"
"v_mfma_f32_32x32x16_bf16 v[160:175], v[216:219], v[128:131], v[160:175]\n"
"v_mfma_f32_32x32x16_bf16 v[160:175], v[220:223], v[132:135], v[160:175]\n"
"ds_read_b128 v[216:219], v0 offset:1024\n"
"ds_read_b128 v[220:223], v0 offset:2048\n"
"s_waitcnt lgkmcnt(4)\n"
"v_mfma_f32_32x32x16_bf16 v[160:175], v[224:227], v[136:139], v[160:175]\n"
"v_mfma_f32_32x32x16_bf16 v[160:175], v[228:231], v[140:143], v[160:175]\n"
"ds_read_b128 v[224:227], v0 offset:3072\n"
"ds_read_b128 v[228:231], v0 offset:4096\n"
"s_waitcnt lgkmcnt(4)\n"
"v_mfma_f32_32x32x16_bf16 v[160:175], v[208:211], v[20:23], v[160:175]\n"
"v_mfma_f32_32x32x16_bf16 v[176:191], v[212:215], v[32:35], 0\n"
"ds_read_b128 v[208:211], v0 offset:5120\n"
"ds_read_b128 v[212:215], v0 offset:6144\n"
"s_waitcnt lgkmcnt(4)\n"
"v_mfma_f32_32x32x16_bf16 v[176:191], v[216:219], v[36:39], v[176:191]\n"
"s_nop 6\n"
"v_mul_f32 v232, v17, v160\n"
"v_mul_f32 v233, v17, v161\n"
"v_mul_f32 v234, v17, v162\n"
"v_mul_f32 v235, v17, v163\n"
"v_mfma_f32_32x32x16_bf16 v[176:191], v[220:223], v[40:43], v[176:191]\n"
"ds_read_b128 v[216:219], v0 offset:7168\n"
"ds_read_b128 v[220:223], v0 offset:8192\n"
"v_exp_f32 v232, v232\n"
"v_exp_f32 v233, v233\n"
"s_waitcnt lgkmcnt(4)\n"
"v_mfma_f32_32x32x16_bf16 v[176:191], v[224:227], v[44:47], v[176:191]\n"
"v_exp_f32 v234, v234\n"
"v_exp_f32 v235, v235\n"
"v_mul_f32 v160, v160, v168\n"
"v_mfma_f32_32x32x16_bf16 v[176:191], v[228:231], v[48:51], v[176:191]\n"
"ds_read_b128 v[224:227], v0 offset:9216\n"
"ds_read_b128 v[228:231], v0 offset:10240\n"
"v_mul_f32 v161, v161, v169\n"
"v_mul_f32 v162, v162, v170\n"
"v_mul_f32 v163, v163, v171\n"
"v_fma_f32 v232, v232, v18, v18\n"
"v_fma_f32 v233, v233, v18, v18\n"
"s_waitcnt lgkmcnt(4)\n"
"v_mfma_f32_32x32x16_bf16 v[176:191], v[208:211], v[52:55], v[176:191]\n"
"v_fma_f32 v234, v234, v18, v18\n"
"v_fma_f32 v235, v235, v18, v18\n"
"v_rcp_f32 v232, v232\n"
"v_mfma_f32_32x32x16_bf16 v[176:191], v[212:215], v[56:59], v[176:191]\n"
"ds_read_b128 v[208:211], v0 offset:11264\n"
"ds_read_b128 v[212:215], v0 offset:12288\n"
"v_rcp_f32 v233, v233\n"
"v_rcp_f32 v234, v234\n"
"s_waitcnt lgkmcnt(4)\n"
"v_mfma_f32_32x32x16_bf16 v[176:191], v[216:219], v[60:63], v[176:191]\n"
"v_rcp_f32 v235, v235\n"
"v_mul_f32 v160, v160, v232\n"
"v_mul_f32 v161, v161, v233\n"
"v_mul_f32 v162, v162, v234\n"
"v_mfma_f32_32x32x16_bf16 v[176:191], v[220:223], v[64:67], v[176:191]\n"
"ds_read_b128 v[216:219], v0 offset:13312\n"
"ds_read_b128 v[220:223], v0 offset:14336\n"
"v_mul_f32 v163, v163, v235\n"
"v_cvt_pk_bf16_f32 v240, v160, v161\n"
"v_cvt_pk_bf16_f32 v241, v162, v163\n"
"v_min_u32 v240, -2, v240\n"
"v_min_u32 v241, -2, v241\n"
"s_waitcnt lgkmcnt(4)\n"
"v_mfma_f32_32x32x16_bf16 v[176:191], v[224:227], v[68:71], v[176:191]\n"
"v_mul_f32 v236, v17, v164\n"
"v_mul_f32 v237, v17, v165\n"
"v_mul_f32 v238, v17, v166\n"
"v_mul_f32 v239, v17, v167\n"
"v_mfma_f32_32x32x16_bf16 v[176:191], v[228:231], v[72:75], v[176:191]\n"
"ds_read_b128 v[224:227], v0 offset:15360\n"
"ds_read_b128 v[228:231], v0 offset:16384\n"
"v_exp_f32 v236, v236\n"
"v_exp_f32 v237, v237\n"
"s_waitcnt lgkmcnt(4)\n"
"v_mfma_f32_32x32x16_bf16 v[176:191], v[208:211], v[76:79], v[176:191]\n"
"v_exp_f32 v238, v238\n"
"v_exp_f32 v239, v239\n"
"v_mul_f32 v164, v164, v172\n"
"v_mfma_f32_32x32x16_bf16 v[176:191], v[212:215], v[80:83], v[176:191]\n"
"ds_read_b128 v[208:211], v0 offset:17408\n"
"ds_read_b128 v[212:215], v0 offset:18432\n"
"v_mul_f32 v165, v165, v173\n"
"v_mul_f32 v166, v166, v174\n"
"v_mul_f32 v167, v167, v175\n"
"v_fma_f32 v236, v236, v18, v18\n"
"v_fma_f32 v237, v237, v18, v18\n"
"s_waitcnt lgkmcnt(4)\n"
"v_mfma_f32_32x32x16_bf16 v[176:191], v[216:219], v[84:87], v[176:191]\n"
"buffer_load_dwordx4 v[192:195], v0, s[44:47], s83 offen sc1\n"
"v_fma_f32 v238, v238, v18, v18\n"
"v_fma_f32 v239, v239, v18, v18\n"
"v_mfma_f32_32x32x16_bf16 v[176:191], v[220:223], v[88:91], v[176:191]\n"
"ds_read_b128 v[216:219], v0 offset:19456\n"
"ds_read_b128 v[220:223], v0 offset:20480\n"
"buffer_load_dwordx4 v[196:199], v0, s[44:47], s84 offen sc1\n"
"v_rcp_f32 v236, v236\n"
"s_waitcnt lgkmcnt(4)\n"
"v_mfma_f32_32x32x16_bf16 v[176:191], v[224:227], v[92:95], v[176:191]\n"
"v_rcp_f32 v237, v237\n"
"v_rcp_f32 v238, v238\n"
"v_mfma_f32_32x32x16_bf16 v[176:191], v[228:231], v[96:99], v[176:191]\n"
"ds_read_b128 v[224:227], v0 offset:21504\n"
"ds_read_b128 v[228:231], v0 offset:22528\n"
"s_add_u32 s94, s83, 4096\n"
"buffer_load_dwordx4 v[200:203], v0, s[44:47], s94 offen offset:3072 sc1\n"
"v_rcp_f32 v239, v239\n"
"s_waitcnt lgkmcnt(4)\n"
"v_mfma_f32_32x32x16_bf16 v[176:191], v[208:211], v[100:103], v[176:191]\n"
"v_mul_f32 v164, v164, v236\n"
"v_mul_f32 v165, v165, v237\n"
"v_mul_f32 v166, v166, v238\n"
"v_mul_f32 v167, v167, v239\n"
"v_cvt_pk_bf16_f32 v242, v164, v165\n"
"v_mfma_f32_32x32x16_bf16 v[176:191], v[212:215], v[104:107], v[176:191]\n"
"ds_read_b128 v[208:211], v0 offset:23552\n"
"ds_read_b128 v[212:215], v0 offset:24576\n"
"s_add_u32 s94, s84, 4096\n"
"buffer_load_dwordx4 v[204:207], v0, s[44:47], s94 offen offset:3072 sc1\n"
"v_cvt_pk_bf16_f32 v243, v166, v167\n"
"v_min_u32 v242, -2, v242\n"
"s_waitcnt lgkmcnt(4)\n"
"v_mfma_f32_32x32x16_bf16 v[176:191], v[216:219], v[108:111], v[176:191]\n"
"v_min_u32 v243, -2, v243\n"
"s_add_u32 s59, s82, 4096\n"
"buffer_store_dwordx4 v[240:243], v0, s[44:47], s59 offen sc1\n"
"v_mfma_f32_32x32x16_bf16 v[176:191], v[220:223], v[112:115], v[176:191]\n"
"ds_read_b128 v[216:219], v0 offset:25600\n"
"ds_read_b128 v[220:223], v0 offset:26624\n"
"s_waitcnt lgkmcnt(4)\n"
"v_mfma_f32_32x32x16_bf16 v[176:191], v[224:227], v[116:119], v[176:191]\n"
"v_mfma_f32_32x32x16_bf16 v[176:191], v[228:231], v[120:123], v[176:191]\n"
"ds_read_b128 v[224:227], v0 offset:27648\n"
"ds_read_b128 v[228:231], v0 offset:28672\n"
"s_waitcnt lgkmcnt(4)\n"
"v_mfma_f32_32x32x16_bf16 v[176:191], v[208:211], v[124:127], v[176:191]\n"
"v_mfma_f32_32x32x16_bf16 v[176:191], v[212:215], v[128:131], v[176:191]\n"
"ds_read_b128 v[208:211], v0 offset:29696\n"
"ds_read_b128 v[212:215], v0 offset:30720\n"
"s_waitcnt lgkmcnt(4)\n"
"v_mfma_f32_32x32x16_bf16 v[176:191], v[216:219], v[132:135], v[176:191]\n"
"v_mfma_f32_32x32x16_bf16 v[176:191], v[220:223], v[136:139], v[176:191]\n"
"ds_read_b128 v[216:219], v0 offset:31744\n"
"ds_read_b128 v[220:223], v0 offset:32768\n"
"s_waitcnt lgkmcnt(4)\n"
"v_mfma_f32_32x32x16_bf16 v[176:191], v[224:227], v[140:143], v[176:191]\n"
"v_mfma_f32_32x32x16_bf16 v[176:191], v[228:231], v[20:23], v[176:191]\n"
"ds_read_b128 v[224:227], v0 offset:33792\n"
"ds_read_b128 v[228:231], v0 offset:34816\n"
"s_waitcnt lgkmcnt(4)\n"
"v_mfma_f32_32x32x16_bf16 v[144:159], v[208:211], v[32:35], 0\n"
"v_mfma_f32_32x32x16_bf16 v[144:159], v[212:215], v[36:39], v[144:159]\n"
"ds_read_b128 v[208:211], v0 offset:35840\n"
"ds_read_b128 v[212:215], v0 offset:36864\n"
"s_add_u32 s94, s83, 12288\n"
"buffer_load_dwordx4 v[32:35], v0, s[44:47], s94 offen offset:2048 sc1\n"
"s_nop 2\n"
"v_mul_f32 v232, v17, v176\n"
"v_mul_f32 v233, v17, v177\n"
"v_mul_f32 v234, v17, v178\n"
"s_waitcnt lgkmcnt(4)\n"
"v_mfma_f32_32x32x16_bf16 v[144:159], v[216:219], v[40:43], v[144:159]\n"
"s_add_u32 s94, s84, 12288\n"
"buffer_load_dwordx4 v[36:39], v0, s[44:47], s94 offen offset:2048 sc1\n"
"v_mul_f32 v235, v17, v179\n"
"v_mfma_f32_32x32x16_bf16 v[144:159], v[220:223], v[44:47], v[144:159]\n"
"ds_read_b128 v[216:219], v0 offset:37888\n"
"ds_read_b128 v[220:223], v0 offset:38912\n"
"v_exp_f32 v232, v232\n"
"v_exp_f32 v233, v233\n"
"s_waitcnt lgkmcnt(4)\n"
"v_mfma_f32_32x32x16_bf16 v[144:159], v[224:227], v[48:51], v[144:159]\n"
"s_add_u32 s94, s83, 20480\n"
"buffer_load_dwordx4 v[40:43], v0, s[44:47], s94 offen offset:1024 sc1\n"
"v_exp_f32 v234, v234\n"
"v_mfma_f32_32x32x16_bf16 v[144:159], v[228:231], v[52:55], v[144:159]\n"
"ds_read_b128 v[224:227], v0 offset:39936\n"
"ds_read_b128 v[228:231], v0 offset:40960\n"
"v_exp_f32 v235, v235\n"
"v_mul_f32 v176, v176, v184\n"
"v_mul_f32 v177, v177, v185\n"
"v_mul_f32 v178, v178, v186\n"
"s_waitcnt lgkmcnt(4)\n"
"v_mfma_f32_32x32x16_bf16 v[144:159], v[208:211], v[56:59], v[144:159]\n"
"s_add_u32 s94, s84, 20480\n"
"buffer_load_dwordx4 v[44:47], v0, s[44:47], s94 offen offset:1024 sc1\n"
"v_mul_f32 v179, v179, v187\n"
"v_fma_f32 v232, v232, v18, v18\n"
"v_mfma_f32_32x32x16_bf16 v[144:159], v[212:215], v[60:63], v[144:159]\n"
"ds_read_b128 v[208:211], v0 offset:41984\n"
"ds_read_b128 v[212:215], v0 offset:43008\n"
"v_fma_f32 v233, v233, v18, v18\n"
"v_fma_f32 v234, v234, v18, v18\n"
"v_fma_f32 v235, v235, v18, v18\n"
"v_rcp_f32 v232, v232\n"
"s_waitcnt lgkmcnt(4)\n"
"v_mfma_f32_32x32x16_bf16 v[144:159], v[216:219], v[64:67], v[144:159]\n"
"buffer_load_dwordx4 v[48:51], v0, s[44:47], s83 offen offset:1024 sc1\n"
"v_rcp_f32 v233, v233\n"
"v_mfma_f32_32x32x16_bf16 v[144:159], v[220:223], v[68:71], v[144:159]\n"
"ds_read_b128 v[216:219], v0 offset:44032\n"
"ds_read_b128 v[220:223], v0 offset:45056\n"
"v_rcp_f32 v234, v234\n"
"v_rcp_f32 v235, v235\n"
"s_waitcnt lgkmcnt(4)\n"
"v_mfma_f32_32x32x16_bf16 v[144:159], v[224:227], v[72:75], v[144:159]\n"
"buffer_load_dwordx4 v[52:55], v0, s[44:47], s84 offen offset:1024 sc1\n"
"v_mul_f32 v176, v176, v232\n"
"v_mul_f32 v177, v177, v233\n"
"v_mfma_f32_32x32x16_bf16 v[144:159], v[228:231], v[76:79], v[144:159]\n"
"ds_read_b128 v[224:227], v0 offset:46080\n"
"ds_read_b128 v[228:231], v0 offset:47104\n"
"v_mul_f32 v178, v178, v234\n"
"v_mul_f32 v179, v179, v235\n"
"v_cvt_pk_bf16_f32 v244, v176, v177\n"
"v_cvt_pk_bf16_f32 v245, v178, v179\n"
"v_min_u32 v244, -2, v244\n"
"s_waitcnt lgkmcnt(4)\n"
"v_mfma_f32_32x32x16_bf16 v[144:159], v[208:211], v[80:83], v[144:159]\n"
"s_add_u32 s94, s83, 8192\n"
"buffer_load_dwordx4 v[56:59], v0, s[44:47], s94 offen sc1\n"
"v_min_u32 v245, -2, v245\n"
"v_mul_f32 v236, v17, v180\n"
"v_mfma_f32_32x32x16_bf16 v[144:159], v[212:215], v[84:87], v[144:159]\n"
"ds_read_b128 v[208:211], v0 offset:48128\n"
"ds_read_b128 v[212:215], v0 offset:49152\n"
"v_mul_f32 v237, v17, v181\n"
"v_mul_f32 v238, v17, v182\n"
"v_mul_f32 v239, v17, v183\n"
"s_waitcnt lgkmcnt(4)\n"
"v_mfma_f32_32x32x16_bf16 v[144:159], v[216:219], v[88:91], v[144:159]\n"
"s_add_u32 s94, s84, 8192\n"
"buffer_load_dwordx4 v[60:63], v0, s[44:47], s94 offen sc1\n"
"v_exp_f32 v236, v236\n"
"v_mfma_f32_32x32x16_bf16 v[144:159], v[220:223], v[92:95], v[144:159]\n"
"ds_read_b128 v[216:219], v0 offset:50176\n"
"ds_read_b128 v[220:223], v0 offset:51200\n"
"v_exp_f32 v237, v237\n"
"v_exp_f32 v238, v238\n"
"s_waitcnt lgkmcnt(4)\n"
"v_mfma_f32_32x32x16_bf16 v[144:159], v[224:227], v[96:99], v[144:159]\n"
"s_add_u32 s94, s83, 12288\n"
"buffer_load_dwordx4 v[64:67], v0, s[44:47], s94 offen offset:3072 sc1\n"
"v_exp_f32 v239, v239\n"
"v_mul_f32 v180, v180, v188\n"
"v_mfma_f32_32x32x16_bf16 v[144:159], v[228:231], v[100:103], v[144:159]\n"
"ds_read_b128 v[224:227], v0 offset:52224\n"
"ds_read_b128 v[228:231], v0 offset:53248\n"
"v_mul_f32 v181, v181, v189\n"
"v_mul_f32 v182, v182, v190\n"
"v_mul_f32 v183, v183, v191\n"
"v_fma_f32 v236, v236, v18, v18\n"
"s_waitcnt lgkmcnt(4)\n"
"v_mfma_f32_32x32x16_bf16 v[144:159], v[208:211], v[104:107], v[144:159]\n"
"s_add_u32 s94, s84, 12288\n"
"buffer_load_dwordx4 v[68:71], v0, s[44:47], s94 offen offset:3072 sc1\n"
"v_fma_f32 v237, v237, v18, v18\n"
"v_fma_f32 v238, v238, v18, v18\n"
"v_mfma_f32_32x32x16_bf16 v[144:159], v[212:215], v[108:111], v[144:159]\n"
"ds_read_b128 v[208:211], v0 offset:54272\n"
"ds_read_b128 v[212:215], v0 offset:55296\n"
"v_fma_f32 v239, v239, v18, v18\n"
"v_rcp_f32 v236, v236\n"
"v_rcp_f32 v237, v237\n"
"s_waitcnt lgkmcnt(4)\n"
"v_mfma_f32_32x32x16_bf16 v[144:159], v[216:219], v[112:115], v[144:159]\n"
"s_add_u32 s94, s83, 20480\n"
"buffer_load_dwordx4 v[72:75], v0, s[44:47], s94 offen offset:2048 sc1\n"
"v_rcp_f32 v238, v238\n"
"v_mfma_f32_32x32x16_bf16 v[144:159], v[220:223], v[116:119], v[144:159]\n"
"ds_read_b128 v[216:219], v0 offset:56320\n"
"ds_read_b128 v[220:223], v0 offset:57344\n"
"v_rcp_f32 v239, v239\n"
"v_mul_f32 v180, v180, v236\n"
"v_mul_f32 v181, v181, v237\n"
"s_waitcnt lgkmcnt(4)\n"
"v_mfma_f32_32x32x16_bf16 v[144:159], v[224:227], v[120:123], v[144:159]\n"
"s_add_u32 s94, s84, 20480\n"
"buffer_load_dwordx4 v[76:79], v0, s[44:47], s94 offen offset:2048 sc1\n"
"v_mul_f32 v182, v182, v238\n"
"v_mul_f32 v183, v183, v239\n"
"v_mfma_f32_32x32x16_bf16 v[144:159], v[228:231], v[124:127], v[144:159]\n"
"ds_read_b128 v[224:227], v0 offset:58368\n"
"buffer_load_dwordx4 v[80:83], v0, s[44:47], s83 offen offset:2048 sc1\n"
"v_cvt_pk_bf16_f32 v246, v180, v181\n"
"v_cvt_pk_bf16_f32 v247, v182, v183\n"
"s_waitcnt lgkmcnt(3)\n"
"v_mfma_f32_32x32x16_bf16 v[144:159], v[208:211], v[128:131], v[144:159]\n"
"v_min_u32 v246, -2, v246\n"
"v_min_u32 v247, -2, v247\n"
"s_add_u32 s59, s82, 4096\n"
"v_mfma_f32_32x32x16_bf16 v[144:159], v[212:215], v[132:135], v[144:159]\n"
"buffer_load_dwordx4 v[84:87], v0, s[44:47], s84 offen offset:2048 sc1\n"
"buffer_store_dwordx4 v[244:247], v0, s[44:47], s59 offen offset:1024 sc1\n"
"s_waitcnt lgkmcnt(1)\n"
"v_mfma_f32_32x32x16_bf16 v[144:159], v[216:219], v[136:139], v[144:159]\n"
"v_lshlrev_b32 v160, 16, v8\n"
"v_and_b32 v161, 0xffff0000, v8\n"
"v_lshlrev_b32 v162, 16, v9\n"
"v_and_b32 v163, 0xffff0000, v9\n"
"v_lshlrev_b32 v164, 16, v10\n"
"v_mfma_f32_32x32x16_bf16 v[144:159], v[220:223], v[140:143], v[144:159]\n"
"s_add_u32 s94, s83, 8192\n"
"buffer_load_dwordx4 v[88:91], v0, s[44:47], s94 offen offset:1024 sc1\n"
"v_and_b32 v165, 0xffff0000, v10\n"
"v_lshlrev_b32 v166, 16, v11\n"
"s_waitcnt lgkmcnt(0)\n"
"v_mfma_f32_32x32x16_bf16 v[144:159], v[224:227], v[20:23], v[144:159]\n"
"v_and_b32 v167, 0xffff0000, v11\n"
"v_lshlrev_b32 v168, 16, v12\n"
"v_and_b32 v169, 0xffff0000, v12\n"
"v_lshlrev_b32 v170, 16, v13\n"
"v_and_b32 v171, 0xffff0000, v13\n"
"v_lshlrev_b32 v172, 16, v14\n"
"v_and_b32 v173, 0xffff0000, v14\n"
"v_lshlrev_b32 v174, 16, v15\n"
"v_and_b32 v175, 0xffff0000, v15\n"
"s_cmp_eq_u32 %[act], 0\n"
"s_cbranch_scc1 L_dn_skip_1\n"
"s_waitcnt vmcnt(20)\n"
"v_max3_u32 v19, v192, v193, v194\n"
"v_max_u32 v19, v19, v195\n"
"s_waitcnt vmcnt(19)\n"
"v_max3_u32 v19, v19, v196, v197\n"
"v_max3_u32 v19, v19, v198, v199\n"
"s_waitcnt vmcnt(18)\n"
"v_max3_u32 v19, v19, v200, v201\n"
"v_max3_u32 v19, v19, v202, v203\n"
"s_waitcnt vmcnt(17)\n"
"v_max3_u32 v19, v19, v204, v205\n"
"v_max3_u32 v19, v19, v206, v207\n"
"s_waitcnt vmcnt(15)\n"
"v_max3_u32 v19, v19, v32, v33\n"
"v_max3_u32 v19, v19, v34, v35\n"
"s_waitcnt vmcnt(14)\n"
"v_max3_u32 v19, v19, v36, v37\n"
"v_max3_u32 v19, v19, v38, v39\n"
"s_waitcnt vmcnt(13)\n"
"v_max3_u32 v19, v19, v40, v41\n"
"v_max3_u32 v19, v19, v42, v43\n"
"s_waitcnt vmcnt(12)\n"
"v_max3_u32 v19, v19, v44, v45\n"
"v_max3_u32 v19, v19, v46, v47\n"
"L_hc0_1:\n"
"v_cmp_eq_u32 vcc, -1, v19\n"
"s_cbranch_vccnz L_h0_s_1\n"
"L_h0_go_1:\n"
"v_mfma_f32_32x32x16_bf16 v[160:175], a[0:3], v[192:195], v[160:175]\n"
"s_add_u32 s94, s84, 8192\n"
"buffer_load_dwordx4 v[92:95], v0, s[44:47], s94 offen offset:1024 sc1\n"
"v_mul_f32 v232, v17, v144\n"
"v_mul_f32 v233, v17, v145\n"
"v_mul_f32 v234, v17, v146\n"
"v_mfma_f32_32x32x16_bf16 v[176:191], a[0:3], v[196:199], 0\n"
"v_mul_f32 v235, v17, v147\n"
"v_exp_f32 v232, v232\n"
"v_exp_f32 v233, v233\n"
"v_mfma_f32_32x32x16_bf16 a[224:239], a[4:7], v[192:195], 0\n"
"s_add_u32 s94, s83, 16384\n"
"buffer_load_dwordx4 v[96:99], v0, s[44:47], s94 offen sc1\n"
"v_exp_f32 v234, v234\n"
"v_exp_f32 v235, v235\n"
"v_mfma_f32_32x32x16_bf16 a[240:255], a[4:7], v[196:199], 0\n"
"v_mul_f32 v144, v144, v152\n"
"v_mul_f32 v145, v145, v153\n"
"v_mul_f32 v146, v146, v154\n"
"v_mul_f32 v147, v147, v155\n"
"v_fma_f32 v232, v232, v18, v18\n"
"v_fma_f32 v233, v233, v18, v18\n"
"v_mfma_f32_32x32x16_bf16 v[160:175], a[8:11], v[200:203], v[160:175]\n"
"s_add_u32 s94, s84, 16384\n"
"buffer_load_dwordx4 v[100:103], v0, s[44:47], s94 offen sc1\n"
"v_fma_f32 v234, v234, v18, v18\n"
"v_fma_f32 v235, v235, v18, v18\n"
"v_rcp_f32 v232, v232\n"
"v_mfma_f32_32x32x16_bf16 v[176:191], a[8:11], v[204:207], v[176:191]\n"
"v_rcp_f32 v233, v233\n"
"v_rcp_f32 v234, v234\n"
"v_rcp_f32 v235, v235\n"
"v_mfma_f32_32x32x16_bf16 a[224:239], a[12:15], v[200:203], a[224:239]\n"
"s_add_u32 s94, s83, 20480\n"
"buffer_load_dwordx4 v[104:107], v0, s[44:47], s94 offen offset:3072 sc1\n"
"v_mul_f32 v144, v144, v232\n"
"v_mul_f32 v145, v145, v233\n"
"v_mul_f32 v146, v146, v234\n"
"v_mfma_f32_32x32x16_bf16 a[240:255], a[12:15], v[204:207], a[240:255]\n"
"v_mul_f32 v147, v147, v235\n"
"v_cvt_pk_bf16_f32 v240, v144, v145\n"
"v_cvt_pk_bf16_f32 v241, v146, v147\n"
"v_min_u32 v240, -2, v240\n"
"v_min_u32 v241, -2, v241\n"
"v_mul_f32 v236, v17, v148\n"
"v_mfma_f32_32x32x16_bf16 v[160:175], a[16:19], v[32:35], v[160:175]\n"
"s_add_u32 s94, s84, 20480\n"
"buffer_load_dwordx4 v[108:111], v0, s[44:47], s94 offen offset:3072 sc1\n"
"v_mul_f32 v237, v17, v149\n"
"v_mul_f32 v238, v17, v150\n"
"v_mul_f32 v239, v17, v151\n"
"v_mfma_f32_32x32x16_bf16 v[176:191], a[16:19], v[36:39], v[176:191]\n"
"buffer_load_dwordx4 v[112:115], v0, s[44:47], s83 offen offset:3072 sc1\n"
"v_exp_f32 v236, v236\n"
"v_exp_f32 v237, v237\n"
"v_mfma_f32_32x32x16_bf16 a[224:239], a[20:23], v[32:35], a[224:239]\n"
"v_exp_f32 v238, v238\n"
"v_exp_f32 v239, v239\n"
"v_mul_f32 v148, v148, v156\n"
"v_mul_f32 v149, v149, v157\n"
"v_mfma_f32_32x32x16_bf16 a[240:255], a[20:23], v[36:39], a[240:255]\n"
"buffer_load_dwordx4 v[116:119], v0, s[44:47], s84 offen offset:3072 sc1\n"
"v_mul_f32 v150, v150, v158\n"
"v_mul_f32 v151, v151, v159\n"
"v_fma_f32 v236, v236, v18, v18\n"
"v_fma_f32 v237, v237, v18, v18\n"
"v_mfma_f32_32x32x16_bf16 v[160:175], a[24:27], v[40:43], v[160:175]\n"
"s_waitcnt vmcnt(18)\n"
"v_max3_u32 v19, v48, v49, v50\n"
"v_max_u32 v19, v19, v51\n"
"s_waitcnt vmcnt(17)\n"
"v_max3_u32 v19, v19, v52, v53\n"
"v_max3_u32 v19, v19, v54, v55\n"
"v_fma_f32 v238, v238, v18, v18\n"
"v_fma_f32 v239, v239, v18, v18\n"
"v_rcp_f32 v236, v236\n"
"v_rcp_f32 v237, v237\n"
"v_mfma_f32_32x32x16_bf16 v[176:191], a[24:27], v[44:47], v[176:191]\n"
"s_waitcnt vmcnt(16)\n"
"v_max3_u32 v19, v19, v56, v57\n"
"v_max3_u32 v19, v19, v58, v59\n"
"s_waitcnt vmcnt(15)\n"
"v_max3_u32 v19, v19, v60, v61\n"
"v_max3_u32 v19, v19, v62, v63\n"
"s_add_u32 s94, s83, 8192\n"
"buffer_load_dwordx4 v[120:123], v0, s[44:47], s94 offen offset:2048 sc1\n"
"v_rcp_f32 v238, v238\n"
"v_mfma_f32_32x32x16_bf16 a[224:239], a[28:31], v[40:43], a[224:239]\n"
"s_waitcnt vmcnt(15)\n"
"v_max3_u32 v19, v19, v64, v65\n"
"v_max3_u32 v19, v19, v66, v67\n"
"s_waitcnt vmcnt(14)\n"
"v_max3_u32 v19, v19, v68, v69\n"
"v_max3_u32 v19, v19, v70, v71\n"
"v_rcp_f32 v239, v239\n"
"v_mul_f32 v148, v148, v236\n"
"v_mul_f32 v149, v149, v237\n"
"v_mul_f32 v150, v150, v238\n"
"v_mul_f32 v151, v151, v239\n"
"v_cvt_pk_bf16_f32 v242, v148, v149\n"
"v_mfma_f32_32x32x16_bf16 a[240:255], a[28:31], v[44:47], a[240:255]\n"
"s_waitcnt vmcnt(13)\n"
"v_max3_u32 v19, v19, v72, v73\n"
"v_max3_u32 v19, v19, v74, v75\n"
"s_waitcnt vmcnt(12)\n"
"v_max3_u32 v19, v19, v76, v77\n"
"v_max3_u32 v19, v19, v78, v79\n"
"L_hc1_1:\n"
"v_cmp_eq_u32 vcc, -1, v19\n"
"s_cbranch_vccnz L_h1_s_1\n"
"L_h1_go_1:\n"
"s_add_u32 s94, s84, 8192\n"
"buffer_load_dwordx4 v[124:127], v0, s[44:47], s94 offen offset:2048 sc1\n"
"v_cvt_pk_bf16_f32 v243, v150, v151\n"
"v_min_u32 v242, -2, v242\n"
"v_min_u32 v243, -2, v243\n"
"v_mfma_f32_32x32x16_bf16 v[160:175], a[32:35], v[48:51], v[160:175]\n"
"s_add_u32 s59, s82, 4096\n"
"buffer_store_dwordx4 v[240:243], v0, s[44:47], s59 offen offset:2048 sc1\n"
"v_mfma_f32_32x32x16_bf16 v[176:191], a[32:35], v[52:55], v[176:191]\n"
"s_add_u32 s94, s83, 16384\n"
"buffer_load_dwordx4 v[128:131], v0, s[44:47], s94 offen offset:1024 sc1\n"
"v_mfma_f32_32x32x16_bf16 a[224:239], a[36:39], v[48:51], a[224:239]\n"
"v_mfma_f32_32x32x16_bf16 a[240:255], a[36:39], v[52:55], a[240:255]\n"
"s_add_u32 s94, s84, 16384\n"
"buffer_load_dwordx4 v[132:135], v0, s[44:47], s94 offen offset:1024 sc1\n"
"v_mfma_f32_32x32x16_bf16 v[160:175], a[40:43], v[56:59], v[160:175]\n"
"s_barrier\n"
"s_add_u32 s94, s83, 24576\n"
"buffer_load_dwordx4 v[136:139], v0, s[44:47], s94 offen sc1\n"
"v_mfma_f32_32x32x16_bf16 v[176:191], a[40:43], v[60:63], v[176:191]\n"
"v_mfma_f32_32x32x16_bf16 a[224:239], a[44:47], v[56:59], a[224:239]\n"
"s_add_u32 s94, s84, 24576\n"
"buffer_load_dwordx4 v[140:143], v0, s[44:47], s94 offen sc1\n"
"v_mfma_f32_32x32x16_bf16 a[240:255], a[44:47], v[60:63], a[240:255]\n"
"v_mfma_f32_32x32x16_bf16 v[160:175], a[48:51], v[64:67], v[160:175]\n"
"s_add_u32 s94, s83, 4096\n"
"buffer_load_dwordx4 v[208:211], v0, s[44:47], s94 offen sc1\n"
"v_mfma_f32_32x32x16_bf16 v[176:191], a[48:51], v[68:71], v[176:191]\n"
"v_mfma_f32_32x32x16_bf16 a[224:239], a[52:55], v[64:67], a[224:239]\n"
"s_add_u32 s94, s84, 4096\n"
"buffer_load_dwordx4 v[212:215], v0, s[44:47], s94 offen sc1\n"
"v_mfma_f32_32x32x16_bf16 a[240:255], a[52:55], v[68:71], a[240:255]\n"
"v_mfma_f32_32x32x16_bf16 v[160:175], a[56:59], v[72:75], v[160:175]\n"
"s_waitcnt vmcnt(19)\n"
"v_max3_u32 v19, v80, v81, v82\n"
"v_max_u32 v19, v19, v83\n"
"s_waitcnt vmcnt(18)\n"
"v_max3_u32 v19, v19, v84, v85\n"
"v_max3_u32 v19, v19, v86, v87\n"
"s_add_u32 s94, s83, 8192\n"
"buffer_load_dwordx4 v[216:219], v0, s[44:47], s94 offen offset:3072 sc1\n"
"v_mfma_f32_32x32x16_bf16 v[176:191], a[56:59], v[76:79], v[176:191]\n"
"s_waitcnt vmcnt(17)\n"
"v_max3_u32 v19, v19, v88, v89\n"
"v_max3_u32 v19, v19, v90, v91\n"
"s_waitcnt vmcnt(16)\n"
"v_max3_u32 v19, v19, v92, v93\n"
"v_max3_u32 v19, v19, v94, v95\n"
"v_mfma_f32_32x32x16_bf16 a[224:239], a[60:63], v[72:75], a[224:239]\n"
"s_waitcnt vmcnt(15)\n"
"v_max3_u32 v19, v19, v96, v97\n"
"v_max3_u32 v19, v19, v98, v99\n"
"s_waitcnt vmcnt(14)\n"
"v_max3_u32 v19, v19, v100, v101\n"
"v_max3_u32 v19, v19, v102, v103\n"
"s_add_u32 s94, s84, 8192\n"
"buffer_load_dwordx4 v[220:223], v0, s[44:47], s94 offen offset:3072 sc1\n"
"v_mfma_f32_32x32x16_bf16 a[240:255], a[60:63], v[76:79], a[240:255]\n"
"s_waitcnt vmcnt(14)\n"
"v_max3_u32 v19, v19, v104, v105\n"
"v_max3_u32 v19, v19, v106, v107\n"
"s_waitcnt vmcnt(13)\n"
"v_max3_u32 v19, v19, v108, v109\n"
"v_max3_u32 v19, v19, v110, v111\n"
"L_hc2_1:\n"
"v_cmp_eq_u32 vcc, -1, v19\n"
"s_cbranch_vccnz L_h2_s_1\n"
"L_h2_go_1:\n"
"s_add_u32 s94, s83, 16384\n"
"buffer_load_dwordx4 v[224:227], v0, s[44:47], s94 offen offset:2048 sc1\n"
"v_mfma_f32_32x32x16_bf16 v[160:175], a[64:67], v[80:83], v[160:175]\n"
"v_mfma_f32_32x32x16_bf16 v[176:191], a[64:67], v[84:87], v[176:191]\n"
"s_add_u32 s94, s84, 16384\n"
"buffer_load_dwordx4 v[228:231], v0, s[44:47], s94 offen offset:2048 sc1\n"
"v_mfma_f32_32x32x16_bf16 a[224:239], a[68:71], v[80:83], a[224:239]\n"
"v_mfma_f32_32x32x16_bf16 a[240:255], a[68:71], v[84:87], a[240:255]\n"
"s_add_u32 s94, s83, 24576\n"
"buffer_load_dwordx4 v[232:235], v0, s[44:47], s94 offen offset:1024 sc1\n"
"v_mfma_f32_32x32x16_bf16 v[160:175], a[72:75], v[88:91], v[160:175]\n"
"v_mfma_f32_32x32x16_bf16 v[176:191], a[72:75], v[92:95], v[176:191]\n"
"s_add_u32 s94, s84, 24576\n"
"buffer_load_dwordx4 v[236:239], v0, s[44:47], s94 offen offset:1024 sc1\n"
"v_mfma_f32_32x32x16_bf16 a[224:239], a[76:79], v[88:91], a[224:239]\n"
"v_mfma_f32_32x32x16_bf16 a[240:255], a[76:79], v[92:95], a[240:255]\n"
"s_add_u32 s94, s83, 4096\n"
"buffer_load_dwordx4 v[240:243], v0, s[44:47], s94 offen offset:1024 sc1\n"
"v_mfma_f32_32x32x16_bf16 v[160:175], a[80:83], v[96:99], v[160:175]\n"
"s_add_u32 s94, s84, 4096\n"
"buffer_load_dwordx4 v[244:247], v0, s[44:47], s94 offen offset:1024 sc1\n"
"v_mfma_f32_32x32x16_bf16 v[176:191], a[80:83], v[100:103], v[176:191]\n"
"v_mfma_f32_32x32x16_bf16 a[224:239], a[84:87], v[96:99], a[224:239]\n"
"s_add_u32 s94, s83, 12288\n"
"buffer_load_dwordx4 v[248:251], v0, s[44:47], s94 offen sc1\n"
"v_mfma_f32_32x32x16_bf16 a[240:255], a[84:87], v[100:103], a[240:255]\n"
"v_mfma_f32_32x32x16_bf16 v[160:175], a[88:91], v[104:107], v[160:175]\n"
"s_waitcnt vmcnt(19)\n"
"v_max3_u32 v19, v112, v113, v114\n"
"v_max_u32 v19, v19, v115\n"
"s_waitcnt vmcnt(18)\n"
"v_max3_u32 v19, v19, v116, v117\n"
"v_max3_u32 v19, v19, v118, v119\n"
"s_add_u32 s94, s84, 12288\n"
"buffer_load_dwordx4 v[252:255], v0, s[44:47], s94 offen sc1\n"
"v_mfma_f32_32x32x16_bf16 v[176:191], a[88:91], v[108:111], v[176:191]\n"
"s_waitcnt vmcnt(18)\n"
"v_max3_u32 v19, v19, v120, v121\n"
"v_max3_u32 v19, v19, v122, v123\n"
"s_waitcnt vmcnt(17)\n"
"v_max3_u32 v19, v19, v124, v125\n"
"v_max3_u32 v19, v19, v126, v127\n"
"v_mfma_f32_32x32x16_bf16 a[224:239], a[92:95], v[104:107], a[224:239]\n"
"s_waitcnt vmcnt(15)\n"
"v_max3_u32 v19, v19, v128, v129\n"
"v_max3_u32 v19, v19, v130, v131\n"
"s_waitcnt vmcnt(14)\n"
"v_max3_u32 v19, v19, v132, v133\n"
"v_max3_u32 v19, v19, v134, v135\n"
"s_add_u32 s94, s83, 16384\n"
"buffer_load_dwordx4 v[144:147], v0, s[44:47], s94 offen offset:3072 sc1\n"
"v_mfma_f32_32x32x16_bf16 a[240:255], a[92:95], v[108:111], a[240:255]\n"
"s_waitcnt vmcnt(14)\n"
"v_max3_u32 v19, v19, v136, v137\n"
"v_max3_u32 v19, v19, v138, v139\n"
"s_waitcnt vmcnt(13)\n"
"v_max3_u32 v19, v19, v140, v141\n"
"v_max3_u32 v19, v19, v142, v143\n"
"L_hc3_1:\n"
"v_cmp_eq_u32 vcc, -1, v19\n"
"s_cbranch_vccnz L_h3_s_1\n"
"L_h3_go_1:\n"
"v_mfma_f32_32x32x16_bf16 v[160:175], a[96:99], v[112:115], v[160:175]\n"
"s_add_u32 s94, s84, 16384\n"
"buffer_load_dwordx4 v[148:151], v0, s[44:47], s94 offen offset:3072 sc1\n"
"v_mfma_f32_32x32x16_bf16 v[176:191], a[96:99], v[116:119], v[176:191]\n"
"s_add_u32 s94, s83, 24576\n"
"buffer_load_dwordx4 v[152:155], v0, s[44:47], s94 offen offset:2048 sc1\n"
"v_mfma_f32_32x32x16_bf16 a[224:239], a[100:103], v[112:115], a[224:239]\n"
"v_mfma_f32_32x32x16_bf16 a[240:255], a[100:103], v[116:119], a[240:255]\n"
"s_add_u32 s94, s84, 24576\n"
"buffer_load_dwordx4 v[156:159], v0, s[44:47], s94 offen offset:2048 sc1\n"
"v_mfma_f32_32x32x16_bf16 v[160:175], a[104:107], v[120:123], v[160:175]\n"
"v_mfma_f32_32x32x16_bf16 v[176:191], a[104:107], v[124:127], v[176:191]\n"
"s_add_u32 s94, s83, 4096\n"
"buffer_load_dwordx4 v[192:195], v0, s[44:47], s94 offen offset:2048 sc1\n"
"v_mfma_f32_32x32x16_bf16 a[224:239], a[108:111], v[120:123], a[224:239]\n"
"v_mfma_f32_32x32x16_bf16 a[240:255], a[108:111], v[124:127], a[240:255]\n"
"s_add_u32 s94, s84, 4096\n"
"buffer_load_dwordx4 v[196:199], v0, s[44:47], s94 offen offset:2048 sc1\n"
"v_mfma_f32_32x32x16_bf16 v[160:175], a[112:115], v[128:131], v[160:175]\n"
"v_mfma_f32_32x32x16_bf16 v[176:191], a[112:115], v[132:135], v[176:191]\n"
"s_add_u32 s94, s83, 12288\n"
"buffer_load_dwordx4 v[200:203], v0, s[44:47], s94 offen offset:1024 sc1\n"
"v_mfma_f32_32x32x16_bf16 a[224:239], a[116:119], v[128:131], a[224:239]\n"
"s_add_u32 s94, s84, 12288\n"
"buffer_load_dwordx4 v[204:207], v0, s[44:47], s94 offen offset:1024 sc1\n"
"v_mfma_f32_32x32x16_bf16 a[240:255], a[116:119], v[132:135], a[240:255]\n"
"v_mfma_f32_32x32x16_bf16 v[160:175], a[120:123], v[136:139], v[160:175]\n"
"s_waitcnt vmcnt(19)\n"
"v_max3_u32 v19, v208, v209, v210\n"
"v_max_u32 v19, v19, v211\n"
"s_waitcnt vmcnt(18)\n"
"v_max3_u32 v19, v19, v212, v213\n"
"v_max3_u32 v19, v19, v214, v215\n"
"s_add_u32 s94, s83, 20480\n"
"buffer_load_dwordx4 v[32:35], v0, s[44:47], s94 offen sc1\n"
"v_mfma_f32_32x32x16_bf16 v[176:191], a[120:123], v[140:143], v[176:191]\n"
"s_waitcnt vmcnt(18)\n"
"v_max3_u32 v19, v19, v216, v217\n"
"v_max3_u32 v19, v19, v218, v219\n"
"s_waitcnt vmcnt(17)\n"
"v_max3_u32 v19, v19, v220, v221\n"
"v_max3_u32 v19, v19, v222, v223\n"
"v_mfma_f32_32x32x16_bf16 a[224:239], a[124:127], v[136:139], a[224:239]\n"
"s_waitcnt vmcnt(16)\n"
"v_max3_u32 v19, v19, v224, v225\n"
"v_max3_u32 v19, v19, v226, v227\n"
"s_waitcnt vmcnt(15)\n"
"v_max3_u32 v19, v19, v228, v229\n"
"v_max3_u32 v19, v19, v230, v231\n"
"s_add_u32 s94, s84, 20480\n"
"buffer_load_dwordx4 v[36:39], v0, s[44:47], s94 offen sc1\n"
"v_mfma_f32_32x32x16_bf16 a[240:255], a[124:127], v[140:143], a[240:255]\n"
"s_waitcnt vmcnt(15)\n"
"v_max3_u32 v19, v19, v232, v233\n"
"v_max3_u32 v19, v19, v234, v235\n"
"s_waitcnt vmcnt(14)\n"
"v_max3_u32 v19, v19, v236, v237\n"
"v_max3_u32 v19, v19, v238, v239\n"
"L_hc4_1:\n"
"v_cmp_eq_u32 vcc, -1, v19\n"
"s_cbranch_vccnz L_h4_s_1\n"
"L_h4_go_1:\n"
"v_mfma_f32_32x32x16_bf16 v[176:191], a[128:131], v[212:215], v[176:191]\n"
"s_add_u32 s94, s83, 24576\n"
"buffer_load_dwordx4 v[40:43], v0, s[44:47], s94 offen offset:3072 sc1\n"
"v_mfma_f32_32x32x16_bf16 a[224:239], a[132:135], v[208:211], a[224:239]\n"
"v_mfma_f32_32x32x16_bf16 a[240:255], a[132:135], v[212:215], a[240:255]\n"
"s_add_u32 s94, s84, 24576\n"
"buffer_load_dwordx4 v[44:47], v0, s[44:47], s94 offen offset:3072 sc1\n"
"v_mfma_f32_32x32x16_bf16 v[176:191], a[136:139], v[220:223], v[176:191]\n"
"v_mfma_f32_32x32x16_bf16 a[224:239], a[140:143], v[216:219], a[224:239]\n"
"v_mfma_f32_32x32x16_bf16 a[240:255], a[140:143], v[220:223], a[240:255]\n"
"v_mfma_f32_32x32x16_bf16 v[176:191], a[144:147], v[228:231], v[176:191]\n"
"v_mfma_f32_32x32x16_bf16 a[224:239], a[148:151], v[224:227], a[224:239]\n"
"v_mfma_f32_32x32x16_bf16 a[240:255], a[148:151], v[228:231], a[240:255]\n"
"s_waitcnt vmcnt(15)\n"
"v_max3_u32 v19, v240, v241, v242\n"
"v_max_u32 v19, v19, v243\n"
"s_waitcnt vmcnt(14)\n"
"v_max3_u32 v19, v19, v244, v245\n"
"v_max3_u32 v19, v19, v246, v247\n"
"v_mfma_f32_32x32x16_bf16 v[176:191], a[152:155], v[236:239], v[176:191]\n"
"s_waitcnt vmcnt(13)\n"
"v_max3_u32 v19, v19, v248, v249\n"
"v_max3_u32 v19, v19, v250, v251\n"
"s_waitcnt vmcnt(12)\n"
"v_max3_u32 v19, v19, v252, v253\n"
"v_max3_u32 v19, v19, v254, v255\n"
"v_mfma_f32_32x32x16_bf16 a[224:239], a[156:159], v[232:235], a[224:239]\n"
"s_waitcnt vmcnt(11)\n"
"v_max3_u32 v19, v19, v144, v145\n"
"v_max3_u32 v19, v19, v146, v147\n"
"s_waitcnt vmcnt(10)\n"
"v_max3_u32 v19, v19, v148, v149\n"
"v_max3_u32 v19, v19, v150, v151\n"
"v_mfma_f32_32x32x16_bf16 a[240:255], a[156:159], v[236:239], a[240:255]\n"
"s_waitcnt vmcnt(9)\n"
"v_max3_u32 v19, v19, v152, v153\n"
"v_max3_u32 v19, v19, v154, v155\n"
"s_waitcnt vmcnt(8)\n"
"v_max3_u32 v19, v19, v156, v157\n"
"v_max3_u32 v19, v19, v158, v159\n"
"L_hc5_1:\n"
"v_cmp_eq_u32 vcc, -1, v19\n"
"s_cbranch_vccnz L_h5_s_1\n"
"L_h5_go_1:\n"
"v_mfma_f32_32x32x16_bf16 v[176:191], a[160:163], v[244:247], v[176:191]\n"
"v_mfma_f32_32x32x16_bf16 a[224:239], a[164:167], v[240:243], a[224:239]\n"
"v_mfma_f32_32x32x16_bf16 a[240:255], a[164:167], v[244:247], a[240:255]\n"
"v_mfma_f32_32x32x16_bf16 v[176:191], a[168:171], v[252:255], v[176:191]\n"
"v_mfma_f32_32x32x16_bf16 a[224:239], a[172:175], v[248:251], a[224:239]\n"
"v_mfma_f32_32x32x16_bf16 a[240:255], a[172:175], v[252:255], a[240:255]\n"
"v_mfma_f32_32x32x16_bf16 v[176:191], a[176:179], v[148:151], v[176:191]\n"
"v_mfma_f32_32x32x16_bf16 a[224:239], a[180:183], v[144:147], a[224:239]\n"
"v_mfma_f32_32x32x16_bf16 a[240:255], a[180:183], v[148:151], a[240:255]\n"
"s_waitcnt vmcnt(7)\n"
"v_max3_u32 v19, v192, v193, v194\n"
"v_max_u32 v19, v19, v195\n"
"s_waitcnt vmcnt(6)\n"
"v_max3_u32 v19, v19, v196, v197\n"
"v_max3_u32 v19, v19, v198, v199\n"
"v_mfma_f32_32x32x16_bf16 v[176:191], a[184:187], v[156:159], v[176:191]\n"
"s_waitcnt vmcnt(5)\n"
"v_max3_u32 v19, v19, v200, v201\n"
"v_max3_u32 v19, v19, v202, v203\n"
"s_waitcnt vmcnt(4)\n"
"v_max3_u32 v19, v19, v204, v205\n"
"v_max3_u32 v19, v19, v206, v207\n"
"v_mfma_f32_32x32x16_bf16 a[224:239], a[188:191], v[152:155], a[224:239]\n"
"s_waitcnt vmcnt(3)\n"
"v_max3_u32 v19, v19, v32, v33\n"
"v_max3_u32 v19, v19, v34, v35\n"
"s_waitcnt vmcnt(2)\n"
"v_max3_u32 v19, v19, v36, v37\n"
"v_max3_u32 v19, v19, v38, v39\n"
"v_mfma_f32_32x32x16_bf16 a[240:255], a[188:191], v[156:159], a[240:255]\n"
"s_waitcnt vmcnt(1)\n"
"v_max3_u32 v19, v19, v40, v41\n"
"v_max3_u32 v19, v19, v42, v43\n"
"s_waitcnt vmcnt(0)\n"
"v_max3_u32 v19, v19, v44, v45\n"
"v_max3_u32 v19, v19, v46, v47\n"
"L_hc6_1:\n"
"v_cmp_eq_u32 vcc, -1, v19\n"
"s_cbranch_vccnz L_h6_s_1\n"
"L_h6_go_1:\n"
"v_mfma_f32_32x32x16_bf16 a[240:255], a[196:199], v[196:199], a[240:255]\n"
"v_mfma_f32_32x32x16_bf16 a[240:255], a[204:207], v[204:207], a[240:255]\n"
"v_mfma_f32_32x32x16_bf16 a[240:255], a[212:215], v[36:39], a[240:255]\n"
"v_mfma_f32_32x32x16_bf16 a[240:255], a[220:223], v[44:47], a[240:255]\n"
"v_mfma_f32_32x32x16_bf16 a[224:239], a[196:199], v[192:195], a[224:239]\n"
"v_mfma_f32_32x32x16_bf16 a[224:239], a[204:207], v[200:203], a[224:239]\n"
"v_mfma_f32_32x32x16_bf16 a[224:239], a[212:215], v[32:35], a[224:239]\n"
"v_mfma_f32_32x32x16_bf16 a[224:239], a[220:223], v[40:43], a[224:239]\n"
"s_nop 7\n"
"ds_write_b128 v24, a[240:243]\n"
"v_mfma_f32_32x32x16_bf16 v[176:191], a[192:195], v[196:199], v[176:191]\n"
"ds_write_b128 v24, a[244:247] offset:1024\n"
"v_mfma_f32_32x32x16_bf16 v[176:191], a[200:203], v[204:207], v[176:191]\n"
"ds_write_b128 v24, a[248:251] offset:2048\n"
"v_mfma_f32_32x32x16_bf16 v[176:191], a[208:211], v[36:39], v[176:191]\n"
"ds_write_b128 v24, a[252:255] offset:3072\n"
"v_mfma_f32_32x32x16_bf16 v[176:191], a[216:219], v[44:47], v[176:191]\n"
"ds_write_b128 v7, a[224:227]\n"
"v_mfma_f32_32x32x16_bf16 v[160:175], a[128:131], v[208:211], v[160:175]\n"
"ds_write_b128 v7, a[228:231] offset:1024\n"
"v_mfma_f32_32x32x16_bf16 v[160:175], a[136:139], v[216:219], v[160:175]\n"
"ds_write_b128 v7, a[232:235] offset:2048\n"
"v_mfma_f32_32x32x16_bf16 v[160:175], a[144:147], v[224:227], v[160:175]\n"
"ds_write_b128 v7, a[236:239] offset:3072\n"
"v_mfma_f32_32x32x16_bf16 v[160:175], a[152:155], v[232:235], v[160:175]\n"
"s_nop 3\n"
"ds_write_b128 v6, v[176:179]\n"
"ds_write_b128 v6, v[180:183] offset:1024\n"
"ds_write_b128 v6, v[184:187] offset:2048\n"
"ds_write_b128 v6, v[188:191] offset:3072\n"
"s_waitcnt lgkmcnt(0)\n"
"s_barrier\n"
"ds_read_b128 v[48:51], v5\n"
"ds_read_b128 v[64:67], v5 offset:4096\n"
"ds_read_b128 v[80:83], v5 offset:8192\n"
"ds_read_b128 v[52:55], v5 offset:1024\n"
"ds_read_b128 v[68:71], v5 offset:5120\n"
"ds_read_b128 v[84:87], v5 offset:9216\n"
"ds_read_b128 v[56:59], v5 offset:2048\n"
"ds_read_b128 v[72:75], v5 offset:6144\n"
"ds_read_b128 v[88:91], v5 offset:10240\n"
"ds_read_b128 v[60:63], v5 offset:3072\n"
"ds_read_b128 v[76:79], v5 offset:7168\n"
"ds_read_b128 v[92:95], v5 offset:11264\n"
"v_mfma_f32_32x32x16_bf16 v[160:175], a[160:163], v[240:243], v[160:175]\n"
"v_mfma_f32_32x32x16_bf16 v[160:175], a[168:171], v[248:251], v[160:175]\n"
"v_mfma_f32_32x32x16_bf16 v[160:175], a[176:179], v[144:147], v[160:175]\n"
"s_waitcnt lgkmcnt(10)\n"
"v_add_f32 v48, v48, v64\n"
"v_add_f32 v49, v49, v65\n"
"v_add_f32 v50, v50, v66\n"
"v_add_f32 v51, v51, v67\n"
"s_waitcnt lgkmcnt(9)\n"
"v_add_f32 v48, v48, v80\n"
"v_add_f32 v49, v49, v81\n"
"v_add_f32 v50, v50, v82\n"
"v_add_f32 v51, v51, v83\n"
"s_waitcnt lgkmcnt(7)\n"
"v_add_f32 v52, v52, v68\n"
"v_mfma_f32_32x32x16_bf16 v[160:175], a[184:187], v[152:155], v[160:175]\n"
"v_add_f32 v53, v53, v69\n"
"v_add_f32 v54, v54, v70\n"
"v_add_f32 v55, v55, v71\n"
"s_waitcnt lgkmcnt(6)\n"
"v_add_f32 v52, v52, v84\n"
"v_add_f32 v53, v53, v85\n"
"v_add_f32 v54, v54, v86\n"
"v_add_f32 v55, v55, v87\n"
"s_waitcnt lgkmcnt(4)\n"
"v_add_f32 v56, v56, v72\n"
"v_mfma_f32_32x32x16_bf16 v[160:175], a[192:195], v[192:195], v[160:175]\n"
"v_add_f32 v57, v57, v73\n"
"v_add_f32 v58, v58, v74\n"
"v_add_f32 v59, v59, v75\n"
"s_waitcnt lgkmcnt(3)\n"
"v_add_f32 v56, v56, v88\n"
"v_add_f32 v57, v57, v89\n"
"v_add_f32 v58, v58, v90\n"
"v_add_f32 v59, v59, v91\n"
"s_waitcnt lgkmcnt(1)\n"
"v_add_f32 v60, v60, v76\n"
"v_mfma_f32_32x32x16_bf16 v[160:175], a[200:203], v[200:203], v[160:175]\n"
"v_add_f32 v61, v61, v77\n"
"v_add_f32 v62, v62, v78\n"
"v_add_f32 v63, v63, v79\n"
"s_waitcnt lgkmcnt(0)\n"
"v_add_f32 v60, v60, v92\n"
"v_add_f32 v61, v61, v93\n"
"v_add_f32 v62, v62, v94\n"
"v_add_f32 v63, v63, v95\n"
"v_mfma_f32_32x32x16_bf16 v[160:175], a[208:211], v[32:35], v[160:175]\n"
"v_mfma_f32_32x32x16_bf16 v[160:175], a[216:219], v[40:43], v[160:175]\n"
"s_nop 11\n"
"v_pk_add_f32 v[160:161], v[160:161], v[48:49]\n"
"v_pk_add_f32 v[162:163], v[162:163], v[50:51]\n"
"v_cvt_pk_bf16_f32 v8, v160, v161\n"
"v_cvt_pk_bf16_f32 v9, v162, v163\n"
"v_pk_add_f32 v[164:165], v[164:165], v[52:53]\n"
"v_pk_add_f32 v[166:167], v[166:167], v[54:55]\n"
"v_cvt_pk_bf16_f32 v10, v164, v165\n"
"v_cvt_pk_bf16_f32 v11, v166, v167\n"
"v_pk_add_f32 v[168:169], v[168:169], v[56:57]\n"
"v_pk_add_f32 v[170:171], v[170:171], v[58:59]\n"
"v_cvt_pk_bf16_f32 v12, v168, v169\n"
"v_cvt_pk_bf16_f32 v13, v170, v171\n"
"v_pk_add_f32 v[172:173], v[172:173], v[60:61]\n"
"v_pk_add_f32 v[174:175], v[174:175], v[62:63]\n"
"v_cvt_pk_bf16_f32 v14, v172, v173\n"
"v_cvt_pk_bf16_f32 v15, v174, v175\n"
"s_mov_b32 s60, 14336\n"
"v_permlane16_swap_b32 v8, v12\n"
"v_permlane16_swap_b32 v9, v13\n"
"v_permlane16_swap_b32 v10, v14\n"
"v_permlane16_swap_b32 v11, v15\n"
"buffer_store_dwordx4 v[8:11], v30, s[52:55], 0 offen sc1\n"
"buffer_store_dwordx4 v[12:15], v30, s[52:55], s60 offen sc1\n"
"s_branch L_dn_done_1\n"
"L_dn_skip_1:\n"
"s_nop 0\n"
"v_mul_f32 v232, v17, v144\n"
"v_mul_f32 v233, v17, v145\n"
"v_mul_f32 v234, v17, v146\n"
"v_mul_f32 v235, v17, v147\n"
"v_exp_f32 v232, v232\n"
"v_exp_f32 v233, v233\n"
"v_exp_f32 v234, v234\n"
"v_exp_f32 v235, v235\n"
"v_mul_f32 v144, v144, v152\n"
"v_mul_f32 v145, v145, v153\n"
"v_mul_f32 v146, v146, v154\n"
"v_mul_f32 v147, v147, v155\n"
"v_fma_f32 v232, v232, v18, v18\n"
"v_fma_f32 v233, v233, v18, v18\n"
"v_fma_f32 v234, v234, v18, v18\n"
"v_fma_f32 v235, v235, v18, v18\n"
"v_rcp_f32 v232, v232\n"
"v_rcp_f32 v233, v233\n"
"v_rcp_f32 v234, v234\n"
"v_rcp_f32 v235, v235\n"
"v_mul_f32 v144, v144, v232\n"
"v_mul_f32 v145, v145, v233\n"
"v_mul_f32 v146, v146, v234\n"
"v_mul_f32 v147, v147, v235\n"
"v_cvt_pk_bf16_f32 v240, v144, v145\n"
"v_cvt_pk_bf16_f32 v241, v146, v147\n"
"v_min_u32 v240, -2, v240\n"
"v_min_u32 v241, -2, v241\n"
"v_mul_f32 v236, v17, v148\n"
"v_mul_f32 v237, v17, v149\n"
"v_mul_f32 v238, v17, v150\n"
"v_mul_f32 v239, v17, v151\n"
"v_exp_f32 v236, v236\n"
"v_exp_f32 v237, v237\n"
"v_exp_f32 v238, v238\n"
"v_exp_f32 v239, v239\n"
"v_mul_f32 v148, v148, v156\n"
"v_mul_f32 v149, v149, v157\n"
"v_mul_f32 v150, v150, v158\n"
"v_mul_f32 v151, v151, v159\n"
"v_fma_f32 v236, v236, v18, v18\n"
"v_fma_f32 v237, v237, v18, v18\n"
"v_fma_f32 v238, v238, v18, v18\n"
"v_fma_f32 v239, v239, v18, v18\n"
"v_rcp_f32 v236, v236\n"
"v_rcp_f32 v237, v237\n"
"v_rcp_f32 v238, v238\n"
"v_rcp_f32 v239, v239\n"
"v_mul_f32 v148, v148, v236\n"
"v_mul_f32 v149, v149, v237\n"
"v_mul_f32 v150, v150, v238\n"
"v_mul_f32 v151, v151, v239\n"
"v_cvt_pk_bf16_f32 v242, v148, v149\n"
"v_cvt_pk_bf16_f32 v243, v150, v151\n"
"v_min_u32 v242, -2, v242\n"
"v_min_u32 v243, -2, v243\n"
"s_add_u32 s59, s82, 4096\n"
"buffer_store_dwordx4 v[240:243], v0, s[44:47], s59 offen offset:2048 sc1\n"
"L_dn_done_1:\n"
"s_endpgm\n"
"K2_end:\n"
"L_kp_s_0:\n"
"s_cmp_eq_u32 s57, 0\n"
"s_cbranch_scc1 L_kp_go_0\n"
"s_sub_u32 s57, s57, 1\n"
"s_waitcnt vmcnt(0) lgkmcnt(0)\n"
"s_sleep 0\n"
"s_nop 15\n"
"s_branch L_kp_0\n"
"L_kr_0:\n"
"L_kp_0:\n"
"buffer_load_dword v19, v28, s[44:47], s73 offen sc1\n"
"s_waitcnt vmcnt(0)\n"
"v_cmp_eq_u32 vcc, -1, v19\n"
"s_cbranch_vccnz L_kp_s_0\n"
"L_kp_go_0:\n"
"buffer_load_dwordx4 v[32:35], v0, s[44:47], s73 offen sc1\n"
"s_add_u32 s94, s73, 4096\n"
"buffer_load_dwordx4 v[36:39], v0, s[44:47], s94 offen sc1\n"
"s_add_u32 s94, s73, 8192\n"
"buffer_load_dwordx4 v[40:43], v0, s[44:47], s94 offen sc1\n"
"s_add_u32 s94, s73, 12288\n"
"buffer_load_dwordx4 v[44:47], v0, s[44:47], s94 offen sc1\n"
"s_add_u32 s94, s73, 16384\n"
"buffer_load_dwordx4 v[48:51], v0, s[44:47], s94 offen sc1\n"
"s_add_u32 s94, s73, 20480\n"
"buffer_load_dwordx4 v[52:55], v0, s[44:47], s94 offen sc1\n"
"s_add_u32 s94, s73, 24576\n"
"buffer_load_dwordx4 v[56:59], v0, s[44:47], s94 offen sc1\n"
"s_add_u32 s94, s73, 28672\n"
"buffer_load_dwordx4 v[60:63], v0, s[44:47], s94 offen sc1\n"
"s_add_u32 s94, s73, 32768\n"
"buffer_load_dwordx4 v[64:67], v0, s[44:47], s94 offen sc1\n"
"s_add_u32 s94, s73, 36864\n"
"buffer_load_dwordx4 v[68:71], v0, s[44:47], s94 offen sc1\n"
"s_add_u32 s94, s73, 40960\n"
"buffer_load_dwordx4 v[72:75], v0, s[44:47], s94 offen sc1\n"
"s_add_u32 s94, s73, 45056\n"
"buffer_load_dwordx4 v[76:79], v0, s[44:47], s94 offen sc1\n"
"s_add_u32 s94, s73, 49152\n"
"buffer_load_dwordx4 v[80:83], v0, s[44:47], s94 offen sc1\n"
"s_add_u32 s94, s73, 53248\n"
"buffer_load_dwordx4 v[84:87], v0, s[44:47], s94 offen sc1\n"
"s_add_u32 s94, s73, 57344\n"
"buffer_load_dwordx4 v[88:91], v0, s[44:47], s94 offen sc1\n"
"s_add_u32 s94, s73, 61440\n"
"buffer_load_dwordx4 v[92:95], v0, s[44:47], s94 offen sc1\n"
"s_waitcnt vmcnt(0)\n"
"s_branch L_ks_0\n"
"L_kv_s_0:\n"
"s_cmp_eq_u32 s57, 0\n"
"s_cbranch_scc1 L_kv_go_0\n"
"s_sub_u32 s57, s57, 1\n"
"s_waitcnt vmcnt(0) lgkmcnt(0)\n"
"s_sleep 0\n"
"s_nop 15\n"
"s_branch L_kr_0\n"
"L_kn_s_0:\n"
"v_max3_u32 v19, v32, v33, v34\n"
"v_max_u32 v19, v19, v35\n"
"v_max3_u32 v19, v19, v36, v37\n"
"v_max3_u32 v19, v19, v38, v39\n"
"v_max3_u32 v19, v19, v40, v41\n"
"v_max3_u32 v19, v19, v42, v43\n"
"v_max3_u32 v19, v19, v44, v45\n"
"v_max3_u32 v19, v19, v46, v47\n"
"v_max3_u32 v19, v19, v48, v49\n"
"v_max3_u32 v19, v19, v50, v51\n"
"v_max3_u32 v19, v19, v52, v53\n"
"v_max3_u32 v19, v19, v54, v55\n"
"v_max3_u32 v19, v19, v56, v57\n"
"v_max3_u32 v19, v19, v58, v59\n"
"v_max3_u32 v19, v19, v60, v61\n"
"v_max3_u32 v19, v19, v62, v63\n"
"v_max3_u32 v19, v19, v64, v65\n"
"v_max3_u32 v19, v19, v66, v67\n"
"v_max3_u32 v19, v19, v68, v69\n"
"v_max3_u32 v19, v19, v70, v71\n"
"v_max3_u32 v19, v19, v72, v73\n"
"v_max3_u32 v19, v19, v74, v75\n"
"v_max3_u32 v19, v19, v76, v77\n"
"v_max3_u32 v19, v19, v78, v79\n"
"v_max3_u32 v19, v19, v80, v81\n"
"v_max3_u32 v19, v19, v82, v83\n"
"v_max3_u32 v19, v19, v84, v85\n"
"v_max3_u32 v19, v19, v86, v87\n"
"v_max3_u32 v19, v19, v88, v89\n"
"v_max3_u32 v19, v19, v90, v91\n"
"v_max3_u32 v19, v19, v92, v93\n"
"v_max3_u32 v19, v19, v94, v95\n"
"v_cmp_eq_u32 vcc, -1, v19\n"
"s_cbranch_vccnz L_kv_s_0\n"
"s_branch L_kv_go_0\n"
"L_op_s_0:\n"
"s_cmp_eq_u32 s57, 0\n"
"s_cbranch_scc1 L_op_go_0\n"
"s_sub_u32 s57, s57, 1\n"
"s_waitcnt vmcnt(0) lgkmcnt(0)\n"
"s_sleep 0\n"
"s_nop 15\n"
"s_branch L_op_0\n"
"L_ov_s_0:\n"
"s_cmp_eq_u32 s57, 0\n"
"s_cbranch_scc1 L_ov_go_0\n"
"s_sub_u32 s57, s57, 1\n"
"s_waitcnt vmcnt(0) lgkmcnt(0)\n"
"s_sleep 0\n"
"s_nop 15\n"
"s_branch L_op_0\n"
"L_ip_s_0:\n"
"s_cmp_eq_u32 s57, 0\n"
"s_cbranch_scc1 L_ip_go_0\n"
"s_sub_u32 s57, s57, 1\n"
"s_waitcnt vmcnt(0) lgkmcnt(0)\n"
"s_sleep 0\n"
"s_nop 15\n"
"s_branch L_ip_0\n"
"L_x1p_s_0:\n"
"s_cmp_eq_u32 s57, 0\n"
"s_cbranch_scc1 L_x1p_go_0\n"
"s_sub_u32 s57, s57, 1\n"
"s_waitcnt vmcnt(0) lgkmcnt(0)\n"
"s_sleep 0\n"
"s_nop 15\n"
"s_branch L_x1p_0\n"
"L_x1v_s_0:\n"
"s_cmp_eq_u32 s57, 0\n"
"s_cbranch_scc1 L_x1v_go_0\n"
"s_sub_u32 s57, s57, 1\n"
"s_waitcnt vmcnt(0) lgkmcnt(0)\n"
"s_sleep 0\n"
"s_nop 15\n"
"s_branch L_x1p_0\n"
"L_hr1_0:\n"
"buffer_load_dwordx4 v[48:51], v0, s[44:47], s83 offen offset:1024 sc1\n"
"buffer_load_dwordx4 v[52:55], v0, s[44:47], s84 offen offset:1024 sc1\n"
"s_add_u32 s94, s83, 8192\n"
"buffer_load_dwordx4 v[56:59], v0, s[44:47], s94 offen sc1\n"
"s_add_u32 s94, s84, 8192\n"
"buffer_load_dwordx4 v[60:63], v0, s[44:47], s94 offen sc1\n"
"s_add_u32 s94, s83, 12288\n"
"buffer_load_dwordx4 v[64:67], v0, s[44:47], s94 offen offset:3072 sc1\n"
"s_add_u32 s94, s84, 12288\n"
"buffer_load_dwordx4 v[68:71], v0, s[44:47], s94 offen offset:3072 sc1\n"
"s_add_u32 s94, s83, 20480\n"
"buffer_load_dwordx4 v[72:75], v0, s[44:47], s94 offen offset:2048 sc1\n"
"s_add_u32 s94, s84, 20480\n"
"buffer_load_dwordx4 v[76:79], v0, s[44:47], s94 offen offset:2048 sc1\n"
"s_waitcnt vmcnt(0)\n"
"v_max3_u32 v19, v48, v49, v50\n"
"v_max_u32 v19, v19, v51\n"
"v_max3_u32 v19, v19, v52, v53\n"
"v_max3_u32 v19, v19, v54, v55\n"
"v_max3_u32 v19, v19, v56, v57\n"
"v_max3_u32 v19, v19, v58, v59\n"
"v_max3_u32 v19, v19, v60, v61\n"
"v_max3_u32 v19, v19, v62, v63\n"
"v_max3_u32 v19, v19, v64, v65\n"
"v_max3_u32 v19, v19, v66, v67\n"
"v_max3_u32 v19, v19, v68, v69\n"
"v_max3_u32 v19, v19, v70, v71\n"
"v_max3_u32 v19, v19, v72, v73\n"
"v_max3_u32 v19, v19, v74, v75\n"
"v_max3_u32 v19, v19, v76, v77\n"
"v_max3_u32 v19, v19, v78, v79\n"
"s_branch L_hc1_0\n"
"L_h1_s_0:\n"
"s_cmp_eq_u32 s57, 0\n"
"s_cbranch_scc1 L_h1_go_0\n"
"s_sub_u32 s57, s57, 1\n"
"s_waitcnt vmcnt(0) lgkmcnt(0)\n"
"s_sleep 0\n"
"s_nop 15\n"
"s_branch L_hr1_0\n"
"L_hr2_0:\n"
"buffer_load_dwordx4 v[80:83], v0, s[44:47], s83 offen offset:2048 sc1\n"
"buffer_load_dwordx4 v[84:87], v0, s[44:47], s84 offen offset:2048 sc1\n"
"s_add_u32 s94, s83, 8192\n"
"buffer_load_dwordx4 v[88:91], v0, s[44:47], s94 offen offset:1024 sc1\n"
"s_add_u32 s94, s84, 8192\n"
"buffer_load_dwordx4 v[92:95], v0, s[44:47], s94 offen offset:1024 sc1\n"
"s_add_u32 s94, s83, 16384\n"
"buffer_load_dwordx4 v[96:99], v0, s[44:47], s94 offen sc1\n"
"s_add_u32 s94, s84, 16384\n"
"buffer_load_dwordx4 v[100:103], v0, s[44:47], s94 offen sc1\n"
"s_add_u32 s94, s83, 20480\n"
"buffer_load_dwordx4 v[104:107], v0, s[44:47], s94 offen offset:3072 sc1\n"
"s_add_u32 s94, s84, 20480\n"
"buffer_load_dwordx4 v[108:111], v0, s[44:47], s94 offen offset:3072 sc1\n"
"s_waitcnt vmcnt(0)\n"
"v_max3_u32 v19, v80, v81, v82\n"
"v_max_u32 v19, v19, v83\n"
"v_max3_u32 v19, v19, v84, v85\n"
"v_max3_u32 v19, v19, v86, v87\n"
"v_max3_u32 v19, v19, v88, v89\n"
"v_max3_u32 v19, v19, v90, v91\n"
"v_max3_u32 v19, v19, v92, v93\n"
"v_max3_u32 v19, v19, v94, v95\n"
"v_max3_u32 v19, v19, v96, v97\n"
"v_max3_u32 v19, v19, v98, v99\n"
"v_max3_u32 v19, v19, v100, v101\n"
"v_max3_u32 v19, v19, v102, v103\n"
"v_max3_u32 v19, v19, v104, v105\n"
"v_max3_u32 v19, v19, v106, v107\n"
"v_max3_u32 v19, v19, v108, v109\n"
"v_max3_u32 v19, v19, v110, v111\n"
"s_branch L_hc2_0\n"
"L_h2_s_0:\n"
"s_cmp_eq_u32 s57, 0\n"
"s_cbranch_scc1 L_h2_go_0\n"
"s_sub_u32 s57, s57, 1\n"
"s_waitcnt vmcnt(0) lgkmcnt(0)\n"
"s_sleep 0\n"
"s_nop 15\n"
"s_branch L_hr2_0\n"
"L_hr3_0:\n"
"buffer_load_dwordx4 v[112:115], v0, s[44:47], s83 offen offset:3072 sc1\n"
"buffer_load_dwordx4 v[116:119], v0, s[44:47], s84 offen offset:3072 sc1\n"
"s_add_u32 s94, s83, 8192\n"
"buffer_load_dwordx4 v[120:123], v0, s[44:47], s94 offen offset:2048 sc1\n"
"s_add_u32 s94, s84, 8192\n"
"buffer_load_dwordx4 v[124:127], v0, s[44:47], s94 offen offset:2048 sc1\n"
"s_add_u32 s94, s83, 16384\n"
"buffer_load_dwordx4 v[128:131], v0, s[44:47], s94 offen offset:1024 sc1\n"
"s_add_u32 s94, s84, 16384\n"
"buffer_load_dwordx4 v[132:135], v0, s[44:47], s94 offen offset:1024 sc1\n"
"s_add_u32 s94, s83, 24576\n"
"buffer_load_dwordx4 v[136:139], v0, s[44:47], s94 offen sc1\n"
"s_add_u32 s94, s84, 24576\n"
"buffer_load_dwordx4 v[140:143], v0, s[44:47], s94 offen sc1\n"
"s_waitcnt vmcnt(0)\n"
"v_max3_u32 v19, v112, v113, v114\n"
"v_max_u32 v19, v19, v115\n"
"v_max3_u32 v19, v19, v116, v117\n"
"v_max3_u32 v19, v19, v118, v119\n"
"v_max3_u32 v19, v19, v120, v121\n"
"v_max3_u32 v19, v19, v122, v123\n"
"v_max3_u32 v19, v19, v124, v125\n"
"v_max3_u32 v19, v19, v126, v127\n"
"v_max3_u32 v19, v19, v128, v129\n"
"v_max3_u32 v19, v19, v130, v131\n"
"v_max3_u32 v19, v19, v132, v133\n"
"v_max3_u32 v19, v19, v134, v135\n"
"v_max3_u32 v19, v19, v136, v137\n"
"v_max3_u32 v19, v19, v138, v139\n"
"v_max3_u32 v19, v19, v140, v141\n"
"v_max3_u32 v19, v19, v142, v143\n"
"s_branch L_hc3_0\n"
"L_h3_s_0:\n"
"s_cmp_eq_u32 s57, 0\n"
"s_cbranch_scc1 L_h3_go_0\n"
"s_sub_u32 s57, s57, 1\n"
"s_waitcnt vmcnt(0) lgkmcnt(0)\n"
"s_sleep 0\n"
"s_nop 15\n"
"s_branch L_hr3_0\n"
"L_hr4_0:\n"
"s_add_u32 s94, s83, 4096\n"
"buffer_load_dwordx4 v[208:211], v0, s[44:47], s94 offen sc1\n"
"s_add_u32 s94, s84, 4096\n"
"buffer_load_dwordx4 v[212:215], v0, s[44:47], s94 offen sc1\n"
"s_add_u32 s94, s83, 8192\n"
"buffer_load_dwordx4 v[216:219], v0, s[44:47], s94 offen offset:3072 sc1\n"
"s_add_u32 s94, s84, 8192\n"
"buffer_load_dwordx4 v[220:223], v0, s[44:47], s94 offen offset:3072 sc1\n"
"s_add_u32 s94, s83, 16384\n"
"buffer_load_dwordx4 v[224:227], v0, s[44:47], s94 offen offset:2048 sc1\n"
"s_add_u32 s94, s84, 16384\n"
"buffer_load_dwordx4 v[228:231], v0, s[44:47], s94 offen offset:2048 sc1\n"
"s_add_u32 s94, s83, 24576\n"
"buffer_load_dwordx4 v[232:235], v0, s[44:47], s94 offen offset:1024 sc1\n"
"s_add_u32 s94, s84, 24576\n"
"buffer_load_dwordx4 v[236:239], v0, s[44:47], s94 offen offset:1024 sc1\n"
"s_waitcnt vmcnt(0)\n"
"v_max3_u32 v19, v208, v209, v210\n"
"v_max_u32 v19, v19, v211\n"
"v_max3_u32 v19, v19, v212, v213\n"
"v_max3_u32 v19, v19, v214, v215\n"
"v_max3_u32 v19, v19, v216, v217\n"
"v_max3_u32 v19, v19, v218, v219\n"
"v_max3_u32 v19, v19, v220, v221\n"
"v_max3_u32 v19, v19, v222, v223\n"
"v_max3_u32 v19, v19, v224, v225\n"
"v_max3_u32 v19, v19, v226, v227\n"
"v_max3_u32 v19, v19, v228, v229\n"
"v_max3_u32 v19, v19, v230, v231\n"
"v_max3_u32 v19, v19, v232, v233\n"
"v_max3_u32 v19, v19, v234, v235\n"
"v_max3_u32 v19, v19, v236, v237\n"
"v_max3_u32 v19, v19, v238, v239\n"
"s_branch L_hc4_0\n"
"L_h4_s_0:\n"
"s_cmp_eq_u32 s57, 0\n"
"s_cbranch_scc1 L_h4_go_0\n"
"s_sub_u32 s57, s57, 1\n"
"s_waitcnt vmcnt(0) lgkmcnt(0)\n"
"s_sleep 0\n"
"s_nop 15\n"
"s_branch L_hr4_0\n"
"L_hr5_0:\n"
"s_add_u32 s94, s83, 4096\n"
"buffer_load_dwordx4 v[240:243], v0, s[44:47], s94 offen offset:1024 sc1\n"
"s_add_u32 s94, s84, 4096\n"
"buffer_load_dwordx4 v[244:247], v0, s[44:47], s94 offen offset:1024 sc1\n"
"s_add_u32 s94, s83, 12288\n"
"buffer_load_dwordx4 v[248:251], v0, s[44:47], s94 offen sc1\n"
"s_add_u32 s94, s84, 12288\n"
"buffer_load_dwordx4 v[252:255], v0, s[44:47], s94 offen sc1\n"
"s_add_u32 s94, s83, 16384\n"
"buffer_load_dwordx4 v[144:147], v0, s[44:47], s94 offen offset:3072 sc1\n"
"s_add_u32 s94, s84, 16384\n"
"buffer_load_dwordx4 v[148:151], v0, s[44:47], s94 offen offset:3072 sc1\n"
"s_add_u32 s94, s83, 24576\n"
"buffer_load_dwordx4 v[152:155], v0, s[44:47], s94 offen offset:2048 sc1\n"
"s_add_u32 s94, s84, 24576\n"
"buffer_load_dwordx4 v[156:159], v0, s[44:47], s94 offen offset:2048 sc1\n"
"s_waitcnt vmcnt(0)\n"
"v_max3_u32 v19, v240, v241, v242\n"
"v_max_u32 v19, v19, v243\n"
"v_max3_u32 v19, v19, v244, v245\n"
"v_max3_u32 v19, v19, v246, v247\n"
"v_max3_u32 v19, v19, v248, v249\n"
"v_max3_u32 v19, v19, v250, v251\n"
"v_max3_u32 v19, v19, v252, v253\n"
"v_max3_u32 v19, v19, v254, v255\n"
"v_max3_u32 v19, v19, v144, v145\n"
"v_max3_u32 v19, v19, v146, v147\n"
"v_max3_u32 v19, v19, v148, v149\n"
"v_max3_u32 v19, v19, v150, v151\n"
"v_max3_u32 v19, v19, v152, v153\n"
"v_max3_u32 v19, v19, v154, v155\n"
"v_max3_u32 v19, v19, v156, v157\n"
"v_max3_u32 v19, v19, v158, v159\n"
"s_branch L_hc5_0\n"
"L_h5_s_0:\n"
"s_cmp_eq_u32 s57, 0\n"
"s_cbranch_scc1 L_h5_go_0\n"
"s_sub_u32 s57, s57, 1\n"
"s_waitcnt vmcnt(0) lgkmcnt(0)\n"
"s_sleep 0\n"
"s_nop 15\n"
"s_branch L_hr5_0\n"
"L_hr6_0:\n"
"s_add_u32 s94, s83, 4096\n"
"buffer_load_dwordx4 v[192:195], v0, s[44:47], s94 offen offset:2048 sc1\n"
"s_add_u32 s94, s84, 4096\n"
"buffer_load_dwordx4 v[196:199], v0, s[44:47], s94 offen offset:2048 sc1\n"
"s_add_u32 s94, s83, 12288\n"
"buffer_load_dwordx4 v[200:203], v0, s[44:47], s94 offen offset:1024 sc1\n"
"s_add_u32 s94, s84, 12288\n"
"buffer_load_dwordx4 v[204:207], v0, s[44:47], s94 offen offset:1024 sc1\n"
"s_add_u32 s94, s83, 20480\n"
"buffer_load_dwordx4 v[32:35], v0, s[44:47], s94 offen sc1\n"
"s_add_u32 s94, s84, 20480\n"
"buffer_load_dwordx4 v[36:39], v0, s[44:47], s94 offen sc1\n"
"s_add_u32 s94, s83, 24576\n"
"buffer_load_dwordx4 v[40:43], v0, s[44:47], s94 offen offset:3072 sc1\n"
"s_add_u32 s94, s84, 24576\n"
"buffer_load_dwordx4 v[44:47], v0, s[44:47], s94 offen offset:3072 sc1\n"
"s_waitcnt vmcnt(0)\n"
"v_max3_u32 v19, v192, v193, v194\n"
"v_max_u32 v19, v19, v195\n"
"v_max3_u32 v19, v19, v196, v197\n"
"v_max3_u32 v19, v19, v198, v199\n"
"v_max3_u32 v19, v19, v200, v201\n"
"v_max3_u32 v19, v19, v202, v203\n"
"v_max3_u32 v19, v19, v204, v205\n"
"v_max3_u32 v19, v19, v206, v207\n"
"v_max3_u32 v19, v19, v32, v33\n"
"v_max3_u32 v19, v19, v34, v35\n"
"v_max3_u32 v19, v19, v36, v37\n"
"v_max3_u32 v19, v19, v38, v39\n"
"v_max3_u32 v19, v19, v40, v41\n"
"v_max3_u32 v19, v19, v42, v43\n"
"v_max3_u32 v19, v19, v44, v45\n"
"v_max3_u32 v19, v19, v46, v47\n"
"s_branch L_hc6_0\n"
"L_h6_s_0:\n"
"s_cmp_eq_u32 s57, 0\n"
"s_cbranch_scc1 L_h6_go_0\n"
"s_sub_u32 s57, s57, 1\n"
"s_waitcnt vmcnt(0) lgkmcnt(0)\n"
"s_sleep 0\n"
"s_nop 15\n"
"s_branch L_hr6_0\n"
"L_hr0_0:\n"
"buffer_load_dwordx4 v[192:195], v0, s[44:47], s83 offen sc1\n"
"buffer_load_dwordx4 v[196:199], v0, s[44:47], s84 offen sc1\n"
"s_add_u32 s94, s83, 4096\n"
"buffer_load_dwordx4 v[200:203], v0, s[44:47], s94 offen offset:3072 sc1\n"
"s_add_u32 s94, s84, 4096\n"
"buffer_load_dwordx4 v[204:207], v0, s[44:47], s94 offen offset:3072 sc1\n"
"s_add_u32 s94, s83, 12288\n"
"buffer_load_dwordx4 v[32:35], v0, s[44:47], s94 offen offset:2048 sc1\n"
"s_add_u32 s94, s84, 12288\n"
"buffer_load_dwordx4 v[36:39], v0, s[44:47], s94 offen offset:2048 sc1\n"
"s_add_u32 s94, s83, 20480\n"
"buffer_load_dwordx4 v[40:43], v0, s[44:47], s94 offen offset:1024 sc1\n"
"s_add_u32 s94, s84, 20480\n"
"buffer_load_dwordx4 v[44:47], v0, s[44:47], s94 offen offset:1024 sc1\n"
"s_waitcnt vmcnt(0)\n"
"v_max3_u32 v19, v192, v193, v194\n"
"v_max_u32 v19, v19, v195\n"
"v_max3_u32 v19, v19, v196, v197\n"
"v_max3_u32 v19, v19, v198, v199\n"
"v_max3_u32 v19, v19, v200, v201\n"
"v_max3_u32 v19, v19, v202, v203\n"
"v_max3_u32 v19, v19, v204, v205\n"
"v_max3_u32 v19, v19, v206, v207\n"
"v_max3_u32 v19, v19, v32, v33\n"
"v_max3_u32 v19, v19, v34, v35\n"
"v_max3_u32 v19, v19, v36, v37\n"
"v_max3_u32 v19, v19, v38, v39\n"
"v_max3_u32 v19, v19, v40, v41\n"
"v_max3_u32 v19, v19, v42, v43\n"
"v_max3_u32 v19, v19, v44, v45\n"
"v_max3_u32 v19, v19, v46, v47\n"
"s_branch L_hc0_0\n"
"L_h0_s_0:\n"
"s_cmp_eq_u32 s57, 0\n"
"s_cbranch_scc1 L_h0_go_0\n"
"s_sub_u32 s57, s57, 1\n"
"s_waitcnt vmcnt(0) lgkmcnt(0)\n"
"s_sleep 0\n"
"s_nop 15\n"
"s_branch L_hr0_0\n"
"L_xp_s_1:\n"
"s_cmp_eq_u32 s57, 0\n"
"s_cbranch_scc1 L_xp_go_1\n"
"s_sub_u32 s57, s57, 1\n"
"s_waitcnt vmcnt(0) lgkmcnt(0)\n"
"s_sleep 0\n"
"s_nop 15\n"
"s_branch L_xp_1\n"
"L_xv_s_1:\n"
"s_cmp_eq_u32 s57, 0\n"
"s_cbranch_scc1 L_xv_go_1\n"
"s_sub_u32 s57, s57, 1\n"
"s_waitcnt vmcnt(0) lgkmcnt(0)\n"
"s_sleep 0\n"
"s_nop 15\n"
"s_branch L_xp_1\n"
"L_kp_s_1:\n"
"s_cmp_eq_u32 s57, 0\n"
"s_cbranch_scc1 L_kp_go_1\n"
"s_sub_u32 s57, s57, 1\n"
"s_waitcnt vmcnt(0) lgkmcnt(0)\n"
"s_sleep 0\n"
"s_nop 15\n"
"s_branch L_kp_1\n"
"L_kr_1:\n"
"L_kp_1:\n"
"buffer_load_dword v19, v28, s[44:47], s73 offen sc1\n"
"s_waitcnt vmcnt(0)\n"
"v_cmp_eq_u32 vcc, -1, v19\n"
"s_cbranch_vccnz L_kp_s_1\n"
"L_kp_go_1:\n"
"buffer_load_dwordx4 v[32:35], v0, s[44:47], s73 offen sc1\n"
"s_add_u32 s94, s73, 4096\n"
"buffer_load_dwordx4 v[36:39], v0, s[44:47], s94 offen sc1\n"
"s_add_u32 s94, s73, 8192\n"
"buffer_load_dwordx4 v[40:43], v0, s[44:47], s94 offen sc1\n"
"s_add_u32 s94, s73, 12288\n"
"buffer_load_dwordx4 v[44:47], v0, s[44:47], s94 offen sc1\n"
"s_add_u32 s94, s73, 16384\n"
"buffer_load_dwordx4 v[48:51], v0, s[44:47], s94 offen sc1\n"
"s_add_u32 s94, s73, 20480\n"
"buffer_load_dwordx4 v[52:55], v0, s[44:47], s94 offen sc1\n"
"s_add_u32 s94, s73, 24576\n"
"buffer_load_dwordx4 v[56:59], v0, s[44:47], s94 offen sc1\n"
"s_add_u32 s94, s73, 28672\n"
"buffer_load_dwordx4 v[60:63], v0, s[44:47], s94 offen sc1\n"
"s_add_u32 s94, s73, 32768\n"
"buffer_load_dwordx4 v[64:67], v0, s[44:47], s94 offen sc1\n"
"s_add_u32 s94, s73, 36864\n"
"buffer_load_dwordx4 v[68:71], v0, s[44:47], s94 offen sc1\n"
"s_add_u32 s94, s73, 40960\n"
"buffer_load_dwordx4 v[72:75], v0, s[44:47], s94 offen sc1\n"
"s_add_u32 s94, s73, 45056\n"
"buffer_load_dwordx4 v[76:79], v0, s[44:47], s94 offen sc1\n"
"s_add_u32 s94, s73, 49152\n"
"buffer_load_dwordx4 v[80:83], v0, s[44:47], s94 offen sc1\n"
"s_add_u32 s94, s73, 53248\n"
"buffer_load_dwordx4 v[84:87], v0, s[44:47], s94 offen sc1\n"
"s_add_u32 s94, s73, 57344\n"
"buffer_load_dwordx4 v[88:91], v0, s[44:47], s94 offen sc1\n"
"s_add_u32 s94, s73, 61440\n"
"buffer_load_dwordx4 v[92:95], v0, s[44:47], s94 offen sc1\n"
"s_waitcnt vmcnt(0)\n"
"s_branch L_ks_1\n"
"L_kv_s_1:\n"
"s_cmp_eq_u32 s57, 0\n"
"s_cbranch_scc1 L_kv_go_1\n"
"s_sub_u32 s57, s57, 1\n"
"s_waitcnt vmcnt(0) lgkmcnt(0)\n"
"s_sleep 0\n"
"s_nop 15\n"
"s_branch L_kr_1\n"
"L_kn_s_1:\n"
"v_max3_u32 v19, v32, v33, v34\n"
"v_max_u32 v19, v19, v35\n"
"v_max3_u32 v19, v19, v36, v37\n"
"v_max3_u32 v19, v19, v38, v39\n"
"v_max3_u32 v19, v19, v40, v41\n"
"v_max3_u32 v19, v19, v42, v43\n"
"v_max3_u32 v19, v19, v44, v45\n"
"v_max3_u32 v19, v19, v46, v47\n"
"v_max3_u32 v19, v19, v48, v49\n"
"v_max3_u32 v19, v19, v50, v51\n"
"v_max3_u32 v19, v19, v52, v53\n"
"v_max3_u32 v19, v19, v54, v55\n"
"v_max3_u32 v19, v19, v56, v57\n"
"v_max3_u32 v19, v19, v58, v59\n"
"v_max3_u32 v19, v19, v60, v61\n"
"v_max3_u32 v19, v19, v62, v63\n"
"v_max3_u32 v19, v19, v64, v65\n"
"v_max3_u32 v19, v19, v66, v67\n"
"v_max3_u32 v19, v19, v68, v69\n"
"v_max3_u32 v19, v19, v70, v71\n"
"v_max3_u32 v19, v19, v72, v73\n"
"v_max3_u32 v19, v19, v74, v75\n"
"v_max3_u32 v19, v19, v76, v77\n"
"v_max3_u32 v19, v19, v78, v79\n"
"v_max3_u32 v19, v19, v80, v81\n"
"v_max3_u32 v19, v19, v82, v83\n"
"v_max3_u32 v19, v19, v84, v85\n"
"v_max3_u32 v19, v19, v86, v87\n"
"v_max3_u32 v19, v19, v88, v89\n"
"v_max3_u32 v19, v19, v90, v91\n"
"v_max3_u32 v19, v19, v92, v93\n"
"v_max3_u32 v19, v19, v94, v95\n"
"v_cmp_eq_u32 vcc, -1, v19\n"
"s_cbranch_vccnz L_kv_s_1\n"
"s_branch L_kv_go_1\n"
"L_op_s_1:\n"
"s_cmp_eq_u32 s57, 0\n"
"s_cbranch_scc1 L_op_go_1\n"
"s_sub_u32 s57, s57, 1\n"
"s_waitcnt vmcnt(0) lgkmcnt(0)\n"
"s_sleep 0\n"
"s_nop 15\n"
"s_branch L_op_1\n"
"L_ov_s_1:\n"
"s_cmp_eq_u32 s57, 0\n"
"s_cbranch_scc1 L_ov_go_1\n"
"s_sub_u32 s57, s57, 1\n"
"s_waitcnt vmcnt(0) lgkmcnt(0)\n"
"s_sleep 0\n"
"s_nop 15\n"
"s_branch L_op_1\n"
"L_ip_s_1:\n"
"s_cmp_eq_u32 s57, 0\n"
"s_cbranch_scc1 L_ip_go_1\n"
"s_sub_u32 s57, s57, 1\n"
"s_waitcnt vmcnt(0) lgkmcnt(0)\n"
"s_sleep 0\n"
"s_nop 15\n"
"s_branch L_ip_1\n"
"L_x1p_s_1:\n"
"s_cmp_eq_u32 s57, 0\n"
"s_cbranch_scc1 L_x1p_go_1\n"
"s_sub_u32 s57, s57, 1\n"
"s_waitcnt vmcnt(0) lgkmcnt(0)\n"
"s_sleep 0\n"
"s_nop 15\n"
"s_branch L_x1p_1\n"
"L_x1v_s_1:\n"
"s_cmp_eq_u32 s57, 0\n"
"s_cbranch_scc1 L_x1v_go_1\n"
"s_sub_u32 s57, s57, 1\n"
"s_waitcnt vmcnt(0) lgkmcnt(0)\n"
"s_sleep 0\n"
"s_nop 15\n"
"s_branch L_x1p_1\n"
"L_hr1_1:\n"
"buffer_load_dwordx4 v[48:51], v0, s[44:47], s83 offen offset:1024 sc1\n"
"buffer_load_dwordx4 v[52:55], v0, s[44:47], s84 offen offset:1024 sc1\n"
"s_add_u32 s94, s83, 8192\n"
"buffer_load_dwordx4 v[56:59], v0, s[44:47], s94 offen sc1\n"
"s_add_u32 s94, s84, 8192\n"
"buffer_load_dwordx4 v[60:63], v0, s[44:47], s94 offen sc1\n"
"s_add_u32 s94, s83, 12288\n"
"buffer_load_dwordx4 v[64:67], v0, s[44:47], s94 offen offset:3072 sc1\n"
"s_add_u32 s94, s84, 12288\n"
"buffer_load_dwordx4 v[68:71], v0, s[44:47], s94 offen offset:3072 sc1\n"
"s_add_u32 s94, s83, 20480\n"
"buffer_load_dwordx4 v[72:75], v0, s[44:47], s94 offen offset:2048 sc1\n"
"s_add_u32 s94, s84, 20480\n"
"buffer_load_dwordx4 v[76:79], v0, s[44:47], s94 offen offset:2048 sc1\n"
"s_waitcnt vmcnt(0)\n"
"v_max3_u32 v19, v48, v49, v50\n"
"v_max_u32 v19, v19, v51\n"
"v_max3_u32 v19, v19, v52, v53\n"
"v_max3_u32 v19, v19, v54, v55\n"
"v_max3_u32 v19, v19, v56, v57\n"
"v_max3_u32 v19, v19, v58, v59\n"
"v_max3_u32 v19, v19, v60, v61\n"
"v_max3_u32 v19, v19, v62, v63\n"
"v_max3_u32 v19, v19, v64, v65\n"
"v_max3_u32 v19, v19, v66, v67\n"
"v_max3_u32 v19, v19, v68, v69\n"
"v_max3_u32 v19, v19, v70, v71\n"
"v_max3_u32 v19, v19, v72, v73\n"
"v_max3_u32 v19, v19, v74, v75\n"
"v_max3_u32 v19, v19, v76, v77\n"
"v_max3_u32 v19, v19, v78, v79\n"
"s_branch L_hc1_1\n"
"L_h1_s_1:\n"
"s_cmp_eq_u32 s57, 0\n"
"s_cbranch_scc1 L_h1_go_1\n"
"s_sub_u32 s57, s57, 1\n"
"s_waitcnt vmcnt(0) lgkmcnt(0)\n"
"s_sleep 0\n"
"s_nop 15\n"
"s_branch L_hr1_1\n"
"L_hr2_1:\n"
"buffer_load_dwordx4 v[80:83], v0, s[44:47], s83 offen offset:2048 sc1\n"
"buffer_load_dwordx4 v[84:87], v0, s[44:47], s84 offen offset:2048 sc1\n"
"s_add_u32 s94, s83, 8192\n"
"buffer_load_dwordx4 v[88:91], v0, s[44:47], s94 offen offset:1024 sc1\n"
"s_add_u32 s94, s84, 8192\n"
"buffer_load_dwordx4 v[92:95], v0, s[44:47], s94 offen offset:1024 sc1\n"
"s_add_u32 s94, s83, 16384\n"
"buffer_load_dwordx4 v[96:99], v0, s[44:47], s94 offen sc1\n"
"s_add_u32 s94, s84, 16384\n"
"buffer_load_dwordx4 v[100:103], v0, s[44:47], s94 offen sc1\n"
"s_add_u32 s94, s83, 20480\n"
"buffer_load_dwordx4 v[104:107], v0, s[44:47], s94 offen offset:3072 sc1\n"
"s_add_u32 s94, s84, 20480\n"
"buffer_load_dwordx4 v[108:111], v0, s[44:47], s94 offen offset:3072 sc1\n"
"s_waitcnt vmcnt(0)\n"
"v_max3_u32 v19, v80, v81, v82\n"
"v_max_u32 v19, v19, v83\n"
"v_max3_u32 v19, v19, v84, v85\n"
"v_max3_u32 v19, v19, v86, v87\n"
"v_max3_u32 v19, v19, v88, v89\n"
"v_max3_u32 v19, v19, v90, v91\n"
"v_max3_u32 v19, v19, v92, v93\n"
"v_max3_u32 v19, v19, v94, v95\n"
"v_max3_u32 v19, v19, v96, v97\n"
"v_max3_u32 v19, v19, v98, v99\n"
"v_max3_u32 v19, v19, v100, v101\n"
"v_max3_u32 v19, v19, v102, v103\n"
"v_max3_u32 v19, v19, v104, v105\n"
"v_max3_u32 v19, v19, v106, v107\n"
"v_max3_u32 v19, v19, v108, v109\n"
"v_max3_u32 v19, v19, v110, v111\n"
"s_branch L_hc2_1\n"
"L_h2_s_1:\n"
"s_cmp_eq_u32 s57, 0\n"
"s_cbranch_scc1 L_h2_go_1\n"
"s_sub_u32 s57, s57, 1\n"
"s_waitcnt vmcnt(0) lgkmcnt(0)\n"
"s_sleep 0\n"
"s_nop 15\n"
"s_branch L_hr2_1\n"
"L_hr3_1:\n"
"buffer_load_dwordx4 v[112:115], v0, s[44:47], s83 offen offset:3072 sc1\n"
"buffer_load_dwordx4 v[116:119], v0, s[44:47], s84 offen offset:3072 sc1\n"
"s_add_u32 s94, s83, 8192\n"
"buffer_load_dwordx4 v[120:123], v0, s[44:47], s94 offen offset:2048 sc1\n"
"s_add_u32 s94, s84, 8192\n"
"buffer_load_dwordx4 v[124:127], v0, s[44:47], s94 offen offset:2048 sc1\n"
"s_add_u32 s94, s83, 16384\n"
"buffer_load_dwordx4 v[128:131], v0, s[44:47], s94 offen offset:1024 sc1\n"
"s_add_u32 s94, s84, 16384\n"
"buffer_load_dwordx4 v[132:135], v0, s[44:47], s94 offen offset:1024 sc1\n"
"s_add_u32 s94, s83, 24576\n"
"buffer_load_dwordx4 v[136:139], v0, s[44:47], s94 offen sc1\n"
"s_add_u32 s94, s84, 24576\n"
"buffer_load_dwordx4 v[140:143], v0, s[44:47], s94 offen sc1\n"
"s_waitcnt vmcnt(0)\n"
"v_max3_u32 v19, v112, v113, v114\n"
"v_max_u32 v19, v19, v115\n"
"v_max3_u32 v19, v19, v116, v117\n"
"v_max3_u32 v19, v19, v118, v119\n"
"v_max3_u32 v19, v19, v120, v121\n"
"v_max3_u32 v19, v19, v122, v123\n"
"v_max3_u32 v19, v19, v124, v125\n"
"v_max3_u32 v19, v19, v126, v127\n"
"v_max3_u32 v19, v19, v128, v129\n"
"v_max3_u32 v19, v19, v130, v131\n"
"v_max3_u32 v19, v19, v132, v133\n"
"v_max3_u32 v19, v19, v134, v135\n"
"v_max3_u32 v19, v19, v136, v137\n"
"v_max3_u32 v19, v19, v138, v139\n"
"v_max3_u32 v19, v19, v140, v141\n"
"v_max3_u32 v19, v19, v142, v143\n"
"s_branch L_hc3_1\n"
"L_h3_s_1:\n"
"s_cmp_eq_u32 s57, 0\n"
"s_cbranch_scc1 L_h3_go_1\n"
"s_sub_u32 s57, s57, 1\n"
"s_waitcnt vmcnt(0) lgkmcnt(0)\n"
"s_sleep 0\n"
"s_nop 15\n"
"s_branch L_hr3_1\n"
"L_hr4_1:\n"
"s_add_u32 s94, s83, 4096\n"
"buffer_load_dwordx4 v[208:211], v0, s[44:47], s94 offen sc1\n"
"s_add_u32 s94, s84, 4096\n"
"buffer_load_dwordx4 v[212:215], v0, s[44:47], s94 offen sc1\n"
"s_add_u32 s94, s83, 8192\n"
"buffer_load_dwordx4 v[216:219], v0, s[44:47], s94 offen offset:3072 sc1\n"
"s_add_u32 s94, s84, 8192\n"
"buffer_load_dwordx4 v[220:223], v0, s[44:47], s94 offen offset:3072 sc1\n"
"s_add_u32 s94, s83, 16384\n"
"buffer_load_dwordx4 v[224:227], v0, s[44:47], s94 offen offset:2048 sc1\n"
"s_add_u32 s94, s84, 16384\n"
"buffer_load_dwordx4 v[228:231], v0, s[44:47], s94 offen offset:2048 sc1\n"
"s_add_u32 s94, s83, 24576\n"
"buffer_load_dwordx4 v[232:235], v0, s[44:47], s94 offen offset:1024 sc1\n"
"s_add_u32 s94, s84, 24576\n"
"buffer_load_dwordx4 v[236:239], v0, s[44:47], s94 offen offset:1024 sc1\n"
"s_waitcnt vmcnt(0)\n"
"v_max3_u32 v19, v208, v209, v210\n"
"v_max_u32 v19, v19, v211\n"
"v_max3_u32 v19, v19, v212, v213\n"
"v_max3_u32 v19, v19, v214, v215\n"
"v_max3_u32 v19, v19, v216, v217\n"
"v_max3_u32 v19, v19, v218, v219\n"
"v_max3_u32 v19, v19, v220, v221\n"
"v_max3_u32 v19, v19, v222, v223\n"
"v_max3_u32 v19, v19, v224, v225\n"
"v_max3_u32 v19, v19, v226, v227\n"
"v_max3_u32 v19, v19, v228, v229\n"
"v_max3_u32 v19, v19, v230, v231\n"
"v_max3_u32 v19, v19, v232, v233\n"
"v_max3_u32 v19, v19, v234, v235\n"
"v_max3_u32 v19, v19, v236, v237\n"
"v_max3_u32 v19, v19, v238, v239\n"
"s_branch L_hc4_1\n"
"L_h4_s_1:\n"
"s_cmp_eq_u32 s57, 0\n"
"s_cbranch_scc1 L_h4_go_1\n"
"s_sub_u32 s57, s57, 1\n"
"s_waitcnt vmcnt(0) lgkmcnt(0)\n"
"s_sleep 0\n"
"s_nop 15\n"
"s_branch L_hr4_1\n"
"L_hr5_1:\n"
"s_add_u32 s94, s83, 4096\n"
"buffer_load_dwordx4 v[240:243], v0, s[44:47], s94 offen offset:1024 sc1\n"
"s_add_u32 s94, s84, 4096\n"
"buffer_load_dwordx4 v[244:247], v0, s[44:47], s94 offen offset:1024 sc1\n"
"s_add_u32 s94, s83, 12288\n"
"buffer_load_dwordx4 v[248:251], v0, s[44:47], s94 offen sc1\n"
"s_add_u32 s94, s84, 12288\n"
"buffer_load_dwordx4 v[252:255], v0, s[44:47], s94 offen sc1\n"
"s_add_u32 s94, s83, 16384\n"
"buffer_load_dwordx4 v[144:147], v0, s[44:47], s94 offen offset:3072 sc1\n"
"s_add_u32 s94, s84, 16384\n"
"buffer_load_dwordx4 v[148:151], v0, s[44:47], s94 offen offset:3072 sc1\n"
"s_add_u32 s94, s83, 24576\n"
"buffer_load_dwordx4 v[152:155], v0, s[44:47], s94 offen offset:2048 sc1\n"
"s_add_u32 s94, s84, 24576\n"
"buffer_load_dwordx4 v[156:159], v0, s[44:47], s94 offen offset:2048 sc1\n"
"s_waitcnt vmcnt(0)\n"
"v_max3_u32 v19, v240, v241, v242\n"
"v_max_u32 v19, v19, v243\n"
"v_max3_u32 v19, v19, v244, v245\n"
"v_max3_u32 v19, v19, v246, v247\n"
"v_max3_u32 v19, v19, v248, v249\n"
"v_max3_u32 v19, v19, v250, v251\n"
"v_max3_u32 v19, v19, v252, v253\n"
"v_max3_u32 v19, v19, v254, v255\n"
"v_max3_u32 v19, v19, v144, v145\n"
"v_max3_u32 v19, v19, v146, v147\n"
"v_max3_u32 v19, v19, v148, v149\n"
"v_max3_u32 v19, v19, v150, v151\n"
"v_max3_u32 v19, v19, v152, v153\n"
"v_max3_u32 v19, v19, v154, v155\n"
"v_max3_u32 v19, v19, v156, v157\n"
"v_max3_u32 v19, v19, v158, v159\n"
"s_branch L_hc5_1\n"
"L_h5_s_1:\n"
"s_cmp_eq_u32 s57, 0\n"
"s_cbranch_scc1 L_h5_go_1\n"
"s_sub_u32 s57, s57, 1\n"
"s_waitcnt vmcnt(0) lgkmcnt(0)\n"
"s_sleep 0\n"
"s_nop 15\n"
"s_branch L_hr5_1\n"
"L_hr6_1:\n"
"s_add_u32 s94, s83, 4096\n"
"buffer_load_dwordx4 v[192:195], v0, s[44:47], s94 offen offset:2048 sc1\n"
"s_add_u32 s94, s84, 4096\n"
"buffer_load_dwordx4 v[196:199], v0, s[44:47], s94 offen offset:2048 sc1\n"
"s_add_u32 s94, s83, 12288\n"
"buffer_load_dwordx4 v[200:203], v0, s[44:47], s94 offen offset:1024 sc1\n"
"s_add_u32 s94, s84, 12288\n"
"buffer_load_dwordx4 v[204:207], v0, s[44:47], s94 offen offset:1024 sc1\n"
"s_add_u32 s94, s83, 20480\n"
"buffer_load_dwordx4 v[32:35], v0, s[44:47], s94 offen sc1\n"
"s_add_u32 s94, s84, 20480\n"
"buffer_load_dwordx4 v[36:39], v0, s[44:47], s94 offen sc1\n"
"s_add_u32 s94, s83, 24576\n"
"buffer_load_dwordx4 v[40:43], v0, s[44:47], s94 offen offset:3072 sc1\n"
"s_add_u32 s94, s84, 24576\n"
"buffer_load_dwordx4 v[44:47], v0, s[44:47], s94 offen offset:3072 sc1\n"
"s_waitcnt vmcnt(0)\n"
"v_max3_u32 v19, v192, v193, v194\n"
"v_max_u32 v19, v19, v195\n"
"v_max3_u32 v19, v19, v196, v197\n"
"v_max3_u32 v19, v19, v198, v199\n"
"v_max3_u32 v19, v19, v200, v201\n"
"v_max3_u32 v19, v19, v202, v203\n"
"v_max3_u32 v19, v19, v204, v205\n"
"v_max3_u32 v19, v19, v206, v207\n"
"v_max3_u32 v19, v19, v32, v33\n"
"v_max3_u32 v19, v19, v34, v35\n"
"v_max3_u32 v19, v19, v36, v37\n"
"v_max3_u32 v19, v19, v38, v39\n"
"v_max3_u32 v19, v19, v40, v41\n"
"v_max3_u32 v19, v19, v42, v43\n"
"v_max3_u32 v19, v19, v44, v45\n"
"v_max3_u32 v19, v19, v46, v47\n"
"s_branch L_hc6_1\n"
"L_h6_s_1:\n"
"s_cmp_eq_u32 s57, 0\n"
"s_cbranch_scc1 L_h6_go_1\n"
"s_sub_u32 s57, s57, 1\n"
"s_waitcnt vmcnt(0) lgkmcnt(0)\n"
"s_sleep 0\n"
"s_nop 15\n"
"s_branch L_hr6_1\n"
"L_hr0_1:\n"
"buffer_load_dwordx4 v[192:195], v0, s[44:47], s83 offen sc1\n"
"buffer_load_dwordx4 v[196:199], v0, s[44:47], s84 offen sc1\n"
"s_add_u32 s94, s83, 4096\n"
"buffer_load_dwordx4 v[200:203], v0, s[44:47], s94 offen offset:3072 sc1\n"
"s_add_u32 s94, s84, 4096\n"
"buffer_load_dwordx4 v[204:207], v0, s[44:47], s94 offen offset:3072 sc1\n"
"s_add_u32 s94, s83, 12288\n"
"buffer_load_dwordx4 v[32:35], v0, s[44:47], s94 offen offset:2048 sc1\n"
"s_add_u32 s94, s84, 12288\n"
"buffer_load_dwordx4 v[36:39], v0, s[44:47], s94 offen offset:2048 sc1\n"
"s_add_u32 s94, s83, 20480\n"
"buffer_load_dwordx4 v[40:43], v0, s[44:47], s94 offen offset:1024 sc1\n"
"s_add_u32 s94, s84, 20480\n"
"buffer_load_dwordx4 v[44:47], v0, s[44:47], s94 offen offset:1024 sc1\n"
"s_waitcnt vmcnt(0)\n"
"v_max3_u32 v19, v192, v193, v194\n"
"v_max_u32 v19, v19, v195\n"
"v_max3_u32 v19, v19, v196, v197\n"
"v_max3_u32 v19, v19, v198, v199\n"
"v_max3_u32 v19, v19, v200, v201\n"
"v_max3_u32 v19, v19, v202, v203\n"
"v_max3_u32 v19, v19, v204, v205\n"
"v_max3_u32 v19, v19, v206, v207\n"
"v_max3_u32 v19, v19, v32, v33\n"
"v_max3_u32 v19, v19, v34, v35\n"
"v_max3_u32 v19, v19, v36, v37\n"
"v_max3_u32 v19, v19, v38, v39\n"
"v_max3_u32 v19, v19, v40, v41\n"
"v_max3_u32 v19, v19, v42, v43\n"
"v_max3_u32 v19, v19, v44, v45\n"
"v_max3_u32 v19, v19, v46, v47\n"
"s_branch L_hc0_1\n"
"L_h0_s_1:\n"
"s_cmp_eq_u32 s57, 0\n"
"s_cbranch_scc1 L_h0_go_1\n"
"s_sub_u32 s57, s57, 1\n"
"s_waitcnt vmcnt(0) lgkmcnt(0)\n"
"s_sleep 0\n"
"s_nop 15\n"
"s_branch L_hr0_1\n"
      :
      : [ws] "s"(ws), [out] "s"(out), [w] "s"(w), [qH] "s"(qH), [qR] "s"(qR), [og] "s"(og), [ota] "s"(ota), [gR] "s"(gR), [gS] "s"(gS), [act] "s"(act), [blk] "s"(b), [slw] "s"(slw)
      : "memory", "vcc", "scc", "v0", "v1", "v2", "v3", "v4", "v5", "v6", "v7", "v8", "v9", "v10", "v11", "v12", "v13", "v14", "v15", "v16", "v17", "v18", "v19", "v20", "v21", "v22", "v23",
        "v24", "v25", "v26", "v27", "v28", "v29", "v30", "v31", "v32", "v33", "v34", "v35", "v36", "v37", "v38", "v39", "v40", "v41", "v42", "v43", "v44", "v45", "v46", "v47",
        "v48", "v49", "v50", "v51", "v52", "v53", "v54", "v55", "v56", "v57", "v58", "v59", "v60", "v61", "v62", "v63", "v64", "v65", "v66", "v67", "v68", "v69", "v70", "v71",
        "v72", "v73", "v74", "v75", "v76", "v77", "v78", "v79", "v80", "v81", "v82", "v83", "v84", "v85", "v86", "v87", "v88", "v89", "v90", "v91", "v92", "v93", "v94", "v95",
        "v96", "v97", "v98", "v99", "v100", "v101", "v102", "v103", "v104", "v105", "v106", "v107", "v108", "v109", "v110", "v111", "v112", "v113", "v114", "v115", "v116", "v117", "v118", "v119",
        "v120", "v121", "v122", "v123", "v124", "v125", "v126", "v127", "v128", "v129", "v130", "v131", "v132", "v133", "v134", "v135", "v136", "v137", "v138", "v139", "v140", "v141", "v142", "v143",
        "v144", "v145", "v146", "v147", "v148", "v149", "v150", "v151", "v152", "v153", "v154", "v155", "v156", "v157", "v158", "v159", "v160", "v161", "v162", "v163", "v164", "v165", "v166", "v167",
        "v168", "v169", "v170", "v171", "v172", "v173", "v174", "v175", "v176", "v177", "v178", "v179", "v180", "v181", "v182", "v183", "v184", "v185", "v186", "v187", "v188", "v189", "v190", "v191",
        "v192", "v193", "v194", "v195", "v196", "v197", "v198", "v199", "v200", "v201", "v202", "v203", "v204", "v205", "v206", "v207", "v208", "v209", "v210", "v211", "v212", "v213", "v214", "v215",
        "v216", "v217", "v218", "v219", "v220", "v221", "v222", "v223", "v224", "v225", "v226", "v227", "v228", "v229", "v230", "v231", "v232", "v233", "v234", "v235", "v236", "v237", "v238", "v239",
        "v240", "v241", "v242", "v243", "v244", "v245", "v246", "v247", "v248", "v249", "v250", "v251", "v252", "v253", "v254", "v255", "a0", "a1", "a2", "a3", "a4", "a5", "a6", "a7",
        "a8", "a9", "a10", "a11", "a12", "a13", "a14", "a15", "a16", "a17", "a18", "a19", "a20", "a21", "a22", "a23", "a24", "a25", "a26", "a27", "a28", "a29", "a30", "a31",
        "a32", "a33", "a34", "a35", "a36", "a37", "a38", "a39", "a40", "a41", "a42", "a43", "a44", "a45", "a46", "a47", "a48", "a49", "a50", "a51", "a52", "a53", "a54", "a55",
        "a56", "a57", "a58", "a59", "a60", "a61", "a62", "a63", "a64", "a65", "a66", "a67", "a68", "a69", "a70", "a71", "a72", "a73", "a74", "a75", "a76", "a77", "a78", "a79",
        "a80", "a81", "a82", "a83", "a84", "a85", "a86", "a87", "a88", "a89", "a90", "a91", "a92", "a93", "a94", "a95", "a96", "a97", "a98", "a99", "a100", "a101", "a102", "a103",
        "a104", "a105", "a106", "a107", "a108", "a109", "a110", "a111", "a112", "a113", "a114", "a115", "a116", "a117", "a118", "a119", "a120", "a121", "a122", "a123", "a124", "a125", "a126", "a127",
        "a128", "a129", "a130", "a131", "a132", "a133", "a134", "a135", "a136", "a137", "a138", "a139", "a140", "a141", "a142", "a143", "a144", "a145", "a146", "a147", "a148", "a149", "a150", "a151",
        "a152", "a153", "a154", "a155", "a156", "a157", "a158", "a159", "a160", "a161", "a162", "a163", "a164", "a165", "a166", "a167", "a168", "a169", "a170", "a171", "a172", "a173", "a174", "a175",
        "a176", "a177", "a178", "a179", "a180", "a181", "a182", "a183", "a184", "a185", "a186", "a187", "a188", "a189", "a190", "a191", "a192", "a193", "a194", "a195", "a196", "a197", "a198", "a199",
        "a200", "a201", "a202", "a203", "a204", "a205", "a206", "a207", "a208", "a209", "a210", "a211", "a212", "a213", "a214", "a215", "a216", "a217", "a218", "a219", "a220", "a221", "a222", "a223",
        "a224", "a225", "a226", "a227", "a228", "a229", "a230", "a231", "a232", "a233", "a234", "a235", "a236", "a237", "a238", "a239", "a240", "a241", "a242", "a243", "a244", "a245", "a246", "a247",
        "a248", "a249", "a250", "a251", "a252", "a253", "a254", "a255", "s40", "s41", "s42", "s43", "s44", "s45", "s46", "s47", "s48", "s49", "s50", "s51", "s52", "s53", "s54", "s55",
        "s56", "s57", "s58", "s59", "s60", "s61", "s62", "s63", "s64", "s65", "s66", "s67", "s68", "s69", "s70", "s71", "s72", "s73", "s74", "s75", "s76", "s77", "s78", "s79",
        "s80", "s81", "s82", "s83", "s84", "s85", "s86", "s87", "s88", "s89", "s90", "s91", "s92", "s93", "s94", "s95", "s96", "s97", "s98", "s99");
}

extern "C" void kernel_launch(void* const* d_in, const int* in_sizes, int n_in, void* d_out, int out_size, void* d_ws, size_t ws_size, hipStream_t stream) {
  P0 p0;
  p0.x = (const u16*)d_in[0]; p0.wqkv = (const u16*)d_in[1]; p0.wo = (const u16*)d_in[2];
  p0.w1 = (const u16*)d_in[4]; p0.w3 = (const u16*)d_in[5]; p0.w2 = (const u16*)d_in[6]; p0.ws = (unsigned char*)d_ws;
  k_prep<<<NWAVES / 4, 256, 0, stream>>>(p0);
  (void)hipFuncSetAttribute((const void*)k_main, hipFuncAttributeMaxDynamicSharedMemorySize, LDS_BYTES);
  k_main<<<256, 256, LDS_BYTES, stream>>>((unsigned char*)d_ws, (u16*)d_out);
}
```
